```python
import torch

def _softmax(x, axis=-1):
    return torch.softmax(x, dim=axis)
def _gelu(x):
    return torch.nn.functional.gelu(x, approximate="tanh")
def _silu(x):
    return torch.nn.functional.silu(x)
def _sigmoid(x):
    return torch.sigmoid(x)
def _layernorm(x, axis=-1, eps=1e-5):
    m = x.mean(dim=axis, keepdim=True)
    v = x.var(dim=axis, keepdim=True, unbiased=False)
    return (x - m) / torch.sqrt(v + eps)
def _scatter_add(tgt, idx, src, axis=0):
    return tgt.clone().index_add_(axis, idx, src)
def _segment_sum(src, seg, n):
    out = torch.zeros((n,) + src.shape[1:], dtype=src.dtype)
    return out.index_add_(0, seg, src)
def _pad(x, pw):
    flat = tuple(v for pair in reversed(pw) for v in pair)
    return torch.nn.functional.pad(x, flat)

kernel_name = 'gather_scatter__edge_featured_mpnn_gatedgcn__graph'

def setup_inputs():

    _w = (lambda a: (lambda t: t.to(torch.bfloat16) if t.is_floating_point() else t)(torch.as_tensor(a)))
    N, deg, D, De = 10000, 16, 256, max(0, 1)
    E = N * deg
    return {
        "x": _w((lambda *s: torch.randn(*s, dtype=torch.bfloat16) * 0.4)(N, D)),
        "e_attr": _w((lambda *s: torch.randn(*s, dtype=torch.bfloat16) * 0.8)(E, De)),
        "src": _w((lambda lo, hi, *s: torch.randint(lo, hi, s))(0, N, E).to(torch.int64)),
        "dst": _w((lambda lo, hi, *s: torch.randint(lo, hi, s))(0, N, E).to(torch.int64)),
        "w_a": _w((lambda *s: torch.randn(*s, dtype=torch.bfloat16) * 0.4)(D, D)),
        "w_b": _w((lambda *s: torch.randn(*s, dtype=torch.bfloat16) * 0.4)(D, D)),
        "w_c": _w((lambda *s: torch.randn(*s, dtype=torch.bfloat16) * 0.8)(De, D)),
        "w_u": _w((lambda *s: torch.randn(*s, dtype=torch.bfloat16) * 0.05)(D, D)),
        "w_v": _w((lambda *s: torch.randn(*s, dtype=torch.bfloat16) * 0.05)(D, D)),
    }

def reference(x, e_attr, src, dst, w_a, w_b, w_c, w_u, w_v):
    N, D = x.shape
    hv = x @ w_v
    g = x[dst] @ w_a + x[src] @ w_b + e_attr @ w_c
    eta = _sigmoid(g)
    denom = _segment_sum(eta, dst, N) + 1e-6
    eta_n = eta / denom[dst]
    agg = _segment_sum(eta_n * hv[src], dst, N)
    z = x @ w_u + agg
    relu_z = torch.where(z > 0, z, 0 * z)
    return x + relu_z
```

```cpp
#include <hip/hip_runtime.h>
#include <hip/hip_bf16.h>
#include <stdint.h>

typedef unsigned short u16;
__device__ __forceinline__ float bf2f(u16 v) { return __uint_as_float(((unsigned)v) << 16); }
__device__ __forceinline__ u16 f2bf(float f) { unsigned u = __float_as_uint(f); u += 0x7FFF + ((u >> 16) & 1); return (u16)(u >> 16); }

__global__ void k_gemm(const u16* x, const u16* wa, const u16* wb, const u16* wu, const u16* wv, float* A, float* B, float* U, float* V, int N, int D) {
    int n = blockIdx.x; int c = threadIdx.x;
    float a = 0, b = 0, u = 0, v = 0;
    for (int k = 0; k < D; ++k) {
        float xv = bf2f(x[n * D + k]);
        a += xv * bf2f(wa[k * D + c]); b += xv * bf2f(wb[k * D + c]);
        u += xv * bf2f(wu[k * D + c]); v += xv * bf2f(wv[k * D + c]);
    }
    A[n * D + c] = a; B[n * D + c] = b; U[n * D + c] = u; V[n * D + c] = v;
}
__global__ void k_edge(const int* src, const int* dst, const u16* ea, const u16* wc, const float* A, const float* B, const float* V, float* num, float* den, int E, int D) {
    int e = blockIdx.x; int c = threadIdx.x;
    int s = src[e], d = dst[e];
    float g = A[d * D + c] + B[s * D + c] + bf2f(ea[e]) * bf2f(wc[c]);
    float eta = 1.f / (1.f + __expf(-g));
    atomicAdd(&den[d * D + c], eta);
    atomicAdd(&num[d * D + c], eta * V[s * D + c]);
}
__global__ void k_final(const u16* x, const float* U, const float* num, const float* den, u16* out, int D) {
    int n = blockIdx.x; int c = threadIdx.x;
    float z = U[n * D + c] + num[n * D + c] / (den[n * D + c] + 1e-6f);
    float r = z > 0 ? z : 0.f * z;
    out[n * D + c] = f2bf(bf2f(x[n * D + c]) + r);
}

extern "C" void kernel_launch(void* const* d_in, const int* in_sizes, int n_in,
                              void* d_out, int out_size, void* d_ws, size_t ws_size,
                              hipStream_t stream) {
    const u16* x = (const u16*)d_in[0];
    const u16* ea = (const u16*)d_in[1];
    const int* src = (const int*)d_in[2];
    const int* dst = (const int*)d_in[3];
    const u16* wa = (const u16*)d_in[4];
    const u16* wb = (const u16*)d_in[5];
    const u16* wc = (const u16*)d_in[6];
    const u16* wu = (const u16*)d_in[7];
    const u16* wv = (const u16*)d_in[8];
    int D = 256; int N = in_sizes[0] / D; int E = in_sizes[2];
    float* A = (float*)d_ws; float* B = A + N * D; float* U = B + N * D; float* V = U + N * D; float* num = V + N * D; float* den = num + N * D;
    hipMemsetAsync(num, 0, (size_t)2 * N * D * 4, stream);
    k_gemm<<<N, D, 0, stream>>>(x, wa, wb, wu, wv, A, B, U, V, N, D);
    k_edge<<<E, D, 0, stream>>>(src, dst, ea, wc, A, B, V, num, den, E, D);
    k_final<<<N, D, 0, stream>>>(x, U, num, den, (u16*)d_out, D);
}
```

```cpp
#include <hip/hip_runtime.h>
#include <hip/hip_bf16.h>
#include <stdint.h>

typedef unsigned short u16;
typedef unsigned int u32;
typedef u32 u32x2 __attribute__((ext_vector_type(2)));
typedef u32 u32x4 __attribute__((ext_vector_type(4)));
typedef float f32x2 __attribute__((ext_vector_type(2)));
typedef float f32x4 __attribute__((ext_vector_type(4)));
typedef float f32x16 __attribute__((ext_vector_type(16)));
typedef short s16x4 __attribute__((ext_vector_type(4)));
typedef short s16x8 __attribute__((ext_vector_type(8)));
typedef __bf16 bf16x8 __attribute__((ext_vector_type(8)));
typedef __bf16 bf16x4 __attribute__((ext_vector_type(4)));
typedef __bf16 bf16x2 __attribute__((ext_vector_type(2)));

constexpr int NN = 10000;
constexpr int NE = 160000;
constexpr int DD = 256;
constexpr int NP = 10048;
constexpr int RG = 320;
constexpr int NBK = 64;
constexpr int NB = 157;
constexpr int NCH = 64;
constexpr int CHE = 2500;
constexpr int CAP = 80;
constexpr int CAPN = 44;
constexpr int LST = 45;
constexpr float NLOG2E = -1.4426950408889634f;
constexpr u32 NEUTRAL_REC = (u32)NN;
constexpr u32 NEUTRAL_BV = 0x00007F00u;

constexpr size_t OFF_BV = 0;
constexpr size_t OFF_AU = OFF_BV + (size_t)4 * NP * 256;
constexpr size_t OFF_R4 = OFF_AU + (size_t)4 * NP * 256;
constexpr size_t OFF_R2 = OFF_R4 + (size_t)NBK * NCH * CAP * 4;
constexpr size_t OFF_CNT = OFF_R2 + (size_t)NBK * NCH * CAP * 2;

#define LDS_AS __attribute__((address_space(3)))
#define GLB_AS __attribute__((address_space(1)))

__device__ __forceinline__ void glds16(const void* g, void* l) {
    __builtin_amdgcn_global_load_lds((const GLB_AS u32*)g, (LDS_AS u32*)l, 16, 0, 0);
}
__device__ __forceinline__ u32 cvt_pk_bf16(float lo, float hi) {
    u32 r; asm("v_cvt_pk_bf16_f32 %0, %1, %2" : "=v"(r) : "v"(lo), "v"(hi)); return r;
}
#define WAIT_V(n) asm volatile("s_waitcnt vmcnt(" #n ")" ::: "memory")
#define WAIT_L(n) asm volatile("s_waitcnt lgkmcnt(" #n ")" ::: "memory")
#define BAR() __builtin_amdgcn_s_barrier()
#define STAMP(i)
#define STAMPC(i)

constexpr int K1_R0 = 0, K1_R1 = 65536, K1_ST4 = 131072, K1_ST2 = K1_ST4 + NBK * CAP * 4, K1_BC = K1_ST2 + NBK * CAP * 2;
constexpr int K1_LDS = K1_BC + NBK * 4;

__global__ __launch_bounds__(512, 2) void k1_gemm_bin(
    const u16* __restrict__ x, const u16* __restrict__ wa, const u16* __restrict__ wb, const u16* __restrict__ wu, const u16* __restrict__ wv,
    const int* __restrict__ src, const int* __restrict__ dst, const u16* __restrict__ ea, char* __restrict__ ws) {
    extern __shared__ __attribute__((aligned(16))) char smem[];
    const int tid = threadIdx.x, lane = tid & 63;
    const int w = __builtin_amdgcn_readfirstlane(tid >> 6);
    const int b = blockIdx.x;
    const int ct = (b >> 3) & 7;
    const int rg = (b & 7) * 4 + (b >> 6);
    const int row0 = rg * RG;
    const bool binner = b < NCH;

    constexpr int KOFF = 0;
    STAMP(0);
    {
        const int m = w >> 1;
        const u16* W = m == 0 ? wb : (m == 1 ? wv : (m == 2 ? wa : wu));
        const int kb0 = (w & 1) * 128;
        const u16* g = W + (size_t)(kb0 + (lane >> 2)) * DD + ct * 32 + (lane & 3) * 8;
#pragma unroll
        for (int i = 0; i < 8; ++i) glds16(g + (size_t)i * 16 * DD, smem + K1_R0 + m * 16384 + (kb0 / 16 + i) * 1024);
    }
    auto stage_x = [&](int tile, int q, int ldsbase) {
        const int r = q * 2 + (lane >> 5);
        const int c = (lane & 31) ^ (r & 15);
        int grow = row0 + tile * 32 + r; grow = grow < NN ? grow : NN - 1;
        glds16(x + (size_t)grow * DD + c * 8, smem + ldsbase + q * 1024);
    };
#pragma unroll
    for (int i = 0; i < 8; ++i) { const int q = w * 8 + i; stage_x(q >> 4, q & 15, K1_R1 + (q >> 4) * 16384); }

    int bd[5], bs[5]; u32 be[5];
    if (binner) {
#pragma unroll
        for (int i = 0; i < 5; ++i) {
            const int k = tid + 512 * i;
            const int e = b * CHE + (k < CHE ? k : 0);
            bd[i] = dst[e]; bs[i] = src[e]; be[i] = ea[e];
        }
        if (tid < NBK) *(u32*)(smem + K1_BC + tid * 4) = 0;
    }
    STAMP(1);
    WAIT_V(0); WAIT_L(0);
    STAMP(2);
    BAR();
    STAMP(3);
    const int type = w & 1;
    bf16x8 wf0[16], wf1[16];
    {
        const int h = lane >> 5, blk = (lane >> 4) & 1, q = (lane & 15) >> 2, p = lane & 3;
        const int base = K1_R0 + type * 32768 + (8 * h + q) * 64 + blk * 32 + p * 8;
#pragma unroll
        for (int s = 0; s < 16; ++s) {
            s16x4 a0 = __builtin_amdgcn_ds_read_tr16_b64_v4i16((LDS_AS s16x4*)(smem + base + s * 1024));
            s16x4 a1 = __builtin_amdgcn_ds_read_tr16_b64_v4i16((LDS_AS s16x4*)(smem + base + s * 1024 + 256));
            s16x4 b0 = __builtin_amdgcn_ds_read_tr16_b64_v4i16((LDS_AS s16x4*)(smem + base + 16384 + s * 1024));
            s16x4 b1 = __builtin_amdgcn_ds_read_tr16_b64_v4i16((LDS_AS s16x4*)(smem + base + 16384 + s * 1024 + 256));
            s16x8 fa = __builtin_shufflevector(a0, a1, 0, 1, 2, 3, 4, 5, 6, 7);
            s16x8 fb = __builtin_shufflevector(b0, b1, 0, 1, 2, 3, 4, 5, 6, 7);
            wf0[s] = __builtin_bit_cast(bf16x8, fa);
            wf1[s] = __builtin_bit_cast(bf16x8, fb);
        }
    }
    if (binner) {
#pragma unroll
        for (int i = 0; i < 5; ++i) {
            const int k = tid + 512 * i;
            if (k < CHE) {
                const int bk = (int)(((u32)bd[i] * 53432u) >> 23);
                const int dl = bd[i] - bk * NB;
                const u32 pos = atomicAdd((u32*)(smem + K1_BC + bk * 4), 1u);
                if (pos < CAP) {
                    *(u32*)(smem + K1_ST4 + (bk * CAP + pos) * 4) = (u32)bs[i] | (be[i] << 16);
                    *(u16*)(smem + K1_ST2 + (bk * CAP + pos) * 2) = (u16)dl;
                }
            }
        }
    }
    WAIT_L(0);
    STAMP(4);
    BAR();
    STAMP(5);
#pragma unroll
    for (int i = 0; i < 8; ++i) { const int q = w * 8 + i; stage_x(4 + (q >> 4), q & 15, K1_R0 + (q >> 4) * 16384); }
    if (binner) {
        u32* R4 = (u32*)(ws + OFF_R4); u16* R2 = (u16*)(ws + OFF_R2); u32* CNT = (u32*)(ws + OFF_CNT);
        for (int q = tid; q < NBK * (CAP / 4); q += 512) {
            const int bk = q / (CAP / 4), o = q - bk * (CAP / 4);
            *(u32x4*)(R4 + ((size_t)bk * NCH + b) * CAP + o * 4) = *(const u32x4*)(smem + K1_ST4 + q * 16);
            *(u32x2*)(R2 + ((size_t)bk * NCH + b) * CAP + o * 4) = *(const u32x2*)(smem + K1_ST2 + q * 8);
        }
        if (tid < NBK) CNT[tid * NCH + b] = *(const u32*)(smem + K1_BC + tid * 4);
    }

    const int r = lane & 31, h = lane >> 5;
    const int abase = r * 512 + ((((r & 14) | ((h ^ r) & 1))) << 4);
    u32* const table = (u32*)(ws + (type ? OFF_AU : OFF_BV)) + (size_t)(ct >> 1) * NP * 64 + (ct & 1) * 32 + r;

    auto do_tile = [&](int tile, int ldsbase) {
        const int trow = row0 + tile * 32;
        if (trow > NN) return;
        f32x16 acc0, acc1;
#pragma unroll
        for (int i = 0; i < 16; ++i) { acc0[i] = 0.f; acc1[i] = 0.f; }
#pragma unroll
        for (int s = 0; s < 16; ++s) {
            const bf16x8 a = *(const bf16x8*)(smem + ldsbase + ((abase ^ ((s & 7) << 5)) + (s >> 3) * 256));
            acc0 = __builtin_amdgcn_mfma_f32_32x32x16_bf16(a, wf0[s], acc0, 0, 0, 0);
            acc1 = __builtin_amdgcn_mfma_f32_32x32x16_bf16(a, wf1[s], acc1, 0, 0, 0);
        }
#pragma unroll
        for (int i = 0; i < 16; i += 2) {
            u32 d[2];
            if (type == 0) {
                const u32 bb = cvt_pk_bf16(acc0[i] * NLOG2E, acc0[i + 1] * NLOG2E);
#pragma unroll
                for (int j = 0; j < 2; ++j) {
                    const u32 vb = __float_as_uint(acc1[i + j]);
                    const u32 bl = j ? (bb >> 16) : (bb & 0xFFFFu);
                    const u32 mag = (vb & 0x7FFFFFFFu) + 0x8000u;
                    const u32 u = __builtin_elementwise_sub_sat(mag, bl);
                    d[j] = (vb & 0x80000000u) | (u & 0x7FFF0000u) | bl;
                }
            } else {
                d[0] = cvt_pk_bf16(acc0[i] * NLOG2E, acc1[i]);
                d[1] = cvt_pk_bf16(acc0[i + 1] * NLOG2E, acc1[i + 1]);
            }
#pragma unroll
            for (int j = 0; j < 2; ++j) {
                const int row = trow + ((i + j) & 3) + 8 * ((i + j) >> 2) + 4 * h;
                if (row < NN) table[(size_t)row * 64] = d[j];
                else if (row == NN && type == 0) table[(size_t)row * 64] = NEUTRAL_BV;
            }
        }
    };

    const int r4 = w >> 1;
    STAMP(6);
    do_tile(r4, K1_R1 + r4 * 16384);
    STAMP(7);
    WAIT_V(0);
    STAMP(8);
    BAR();
#pragma unroll
    for (int i = 0; i < 4; ++i) { const int q = w * 4 + i; stage_x(8 + (q >> 4), q & 15, K1_R1 + (q >> 4) * 16384); }
    STAMP(9);
    do_tile(4 + r4, K1_R0 + r4 * 16384);
    STAMP(10);
    WAIT_V(0);
    STAMP(11);
    BAR();
    if (r4 < 2) do_tile(8 + r4, K1_R1 + r4 * 16384);
    STAMP(12);
    WAIT_V(0);
    STAMP(13);
}

constexpr int LSTB = LST * 4;
constexpr int K2_X = 0;
constexpr int K2_AU = K2_X + 20480;
constexpr int K2_LIDX = K2_AU + 40960;
constexpr int K2_LSZ = (NB * LSTB + 32 + 256 + 15) / 16 * 16;
constexpr int K2_LE = K2_LIDX + K2_LSZ;
constexpr int K2_CNT = K2_LE + K2_LSZ;
constexpr int K2_HIST = K2_CNT + 160 * 4;
constexpr int K2_ORD = K2_HIST + 128 * 4;
constexpr int K2_MISC = K2_ORD + 80 * 32;
constexpr int K2_LDS = K2_MISC + 16;
constexpr int K2_TRASH = NB * LSTB + 32;
static_assert(K2_TRASH + 256 <= K2_LSZ, "trash");
static_assert(K2_LSZ < 65536 - 64, "ds offset");

__device__ __forceinline__ float dot2sel(u32 d, u32 sel, float c) {
    return __builtin_amdgcn_fdot2_f32_bf16(__builtin_bit_cast(bf16x2, d), __builtin_bit_cast(bf16x2, sel), c, false);
}

struct Acc { f32x2 n01, n23, d01, d23; };

__device__ __forceinline__ void edge_step(Acc& a, const u32x4 d, const u32 rec, const f32x2 a01, const f32x2 a23, const f32x2 w01, const f32x2 w23, const u32 sel) {
    const float e = __uint_as_float(rec & 0xFFFF0000u);
    const f32x2 ee = {e, e};
    const f32x2 t01 = __builtin_elementwise_fma(ee, w01, a01);
    const f32x2 t23 = __builtin_elementwise_fma(ee, w23, a23);
    f32x2 E01, E23;
    E01.x = __builtin_amdgcn_exp2f(dot2sel(d.x, sel, t01.x));
    E01.y = __builtin_amdgcn_exp2f(dot2sel(d.y, sel, t01.y));
    E23.x = __builtin_amdgcn_exp2f(dot2sel(d.z, sel, t23.x));
    E23.y = __builtin_amdgcn_exp2f(dot2sel(d.w, sel, t23.y));
    const f32x2 one = {1.f, 1.f};
    E01 += one; E23 += one;
    f32x2 s01, s23;
    s01.x = __builtin_amdgcn_rcpf(E01.x); s01.y = __builtin_amdgcn_rcpf(E01.y);
    s23.x = __builtin_amdgcn_rcpf(E23.x); s23.y = __builtin_amdgcn_rcpf(E23.y);
    a.d01 += s01; a.d23 += s23;
    const f32x2 v01 = {__uint_as_float(d.x), __uint_as_float(d.y)};
    const f32x2 v23 = {__uint_as_float(d.z), __uint_as_float(d.w)};
    a.n01 = __builtin_elementwise_fma(s01, v01, a.n01);
    a.n23 = __builtin_elementwise_fma(s23, v23, a.n23);
}

__device__ __forceinline__ void node_finish(const Acc& a, const u32x4 au, const u32x2 xv, u16* outp) {
    const float num[4] = {a.n01.x, a.n01.y, a.n23.x, a.n23.y};
    const float den[4] = {a.d01.x, a.d01.y, a.d23.x, a.d23.y};
    const u32 aus[4] = {au.x, au.y, au.z, au.w};
    const float xs[4] = {__uint_as_float(xv.x << 16), __uint_as_float(xv.x & 0xFFFF0000u), __uint_as_float(xv.y << 16), __uint_as_float(xv.y & 0xFFFF0000u)};
    float o[4];
#pragma unroll
    for (int c = 0; c < 4; ++c) {
        const float z = __uint_as_float(aus[c] & 0xFFFF0000u) + num[c] * __builtin_amdgcn_rcpf(den[c] + 1e-6f);
        const float rl = z > 0.f ? z : 0.f * z;
        o[c] = xs[c] + rl;
    }
    u32x2 ov; ov.x = cvt_pk_bf16(o[0], o[1]); ov.y = cvt_pk_bf16(o[2], o[3]);
    *(u32x2*)outp = ov;
}

template <int CTRL, int RM> __device__ __forceinline__ u32 dpp_add(u32 v) {
    return v + (u32)__builtin_amdgcn_update_dpp(0, (int)v, CTRL, RM, 0xf, false);
}

__global__ __launch_bounds__(1024, 4) void k2_edges(
    const u16* __restrict__ x, const u16* __restrict__ wc, const int* __restrict__ src, const int* __restrict__ dst, const u16* __restrict__ ea,
    const char* __restrict__ ws, u16* __restrict__ out) {
    extern __shared__ __attribute__((aligned(16))) char smem[];
    const int tid = threadIdx.x, lane = tid & 63;
    const int w = __builtin_amdgcn_readfirstlane(tid >> 6);
    const int b = blockIdx.x;
    const int slice = (b & 7) >> 1;
    const int bucket = (b >> 3) * 2 + (b & 1);
    const int node0 = bucket * NB;
    const int nvalid = (NN - node0) < NB ? (NN - node0) : NB;
    u32* const cnt = (u32*)(smem + K2_CNT);
    u32* const hist = (u32*)(smem + K2_HIST);
    u32* const misc = (u32*)(smem + K2_MISC);
    constexpr int KOFF = 256 * 16;
    STAMP(0);

    const u32* R4 = (const u32*)(ws + OFF_R4) + (size_t)bucket * NCH * CAP;
    const u16* R2 = (const u16*)(ws + OFF_R2) + (size_t)bucket * NCH * CAP;
    const u32* CNT = (const u32*)(ws + OFF_CNT) + bucket * NCH;
    const int ch0 = tid / (CAP / 4), ch1 = (tid + 1024) / (CAP / 4);
    const u32x4 r40 = *(const u32x4*)(R4 + tid * 4);
    const u32x2 r20 = *(const u32x2*)(R2 + tid * 4);
    const u32 c0 = CNT[ch0];
    u32x4 r41 = {0, 0, 0, 0}; u32x2 r21 = {0, 0}; u32 c1 = 0;
    if (w < 4) { r41 = *(const u32x4*)(R4 + (tid + 1024) * 4); r21 = *(const u32x2*)(R2 + (tid + 1024) * 4); c1 = CNT[ch1]; }
    const char* const bvb = ws + OFF_BV + (size_t)slice * NP * 256;
    const char* const aub = ws + OFF_AU + (size_t)slice * NP * 256;
    {
        const char* g = aub + (size_t)node0 * 256 + lane * 16;
#pragma unroll
        for (int i = 0; i < 3; ++i) { const int q = w + 16 * i; if (q < 40) glds16(g + q * 1024, smem + K2_AU + q * 1024); }
#pragma unroll
        for (int i = 0; i < 2; ++i) {
            const int q = w + 16 * i;
            if (q < 20) { int row = node0 + q * 8 + (lane >> 3); row = row < NN ? row : NN - 1;
                glds16(x + (size_t)row * DD + slice * 64 + (lane & 7) * 8, smem + K2_X + q * 1024); }
        }
    }
    for (int i = tid; i < K2_LSZ / 16; i += 1024) {
        *(u32x4*)(smem + K2_LIDX + i * 16) = (u32x4){NEUTRAL_REC, NEUTRAL_REC, NEUTRAL_REC, NEUTRAL_REC};
        *(u32x4*)(smem + K2_LE + i * 16) = (u32x4){0, 0, 0, 0};
    }
    if (tid < 160 + 64) cnt[tid] = 0;
    if (tid == 0) { misc[0] = 16; misc[1] = 0; }
    STAMP(1);
    WAIT_L(0); BAR();
    STAMP(2);
    {
        const int p0 = (tid - ch0 * (CAP / 4)) * 4;
        const u32 rec0[4] = {r40.x, r40.y, r40.z, r40.w};
        const u32 dl0[4] = {r20.x & 0xFFFF, r20.x >> 16, r20.y & 0xFFFF, r20.y >> 16};
        u32 slot[4];
        bool ovf = c0 > CAP;
#pragma unroll
        for (int k = 0; k < 4; ++k) slot[k] = atomicAdd(&cnt[p0 + k < c0 ? dl0[k] : 224 + lane], 1u);
        if (w < 4) {
            const int p1 = (tid + 1024 - ch1 * (CAP / 4)) * 4;
            const u32 rec1[4] = {r41.x, r41.y, r41.z, r41.w};
            const u32 dl1[4] = {r21.x & 0xFFFF, r21.x >> 16, r21.y & 0xFFFF, r21.y >> 16};
            u32 slot1[4];
            ovf |= c1 > CAP;
#pragma unroll
            for (int k = 0; k < 4; ++k) slot1[k] = atomicAdd(&cnt[p1 + k < c1 ? dl1[k] : 224 + lane], 1u);
#pragma unroll
            for (int k = 0; k < 4; ++k) {
                const bool v = p1 + k < c1, ok = v && slot1[k] < CAPN;
                ovf |= v && !ok;
                const u32 o = ok ? dl1[k] * LSTB + slot1[k] * 4 : K2_TRASH + lane * 4;
                *(u32*)(smem + K2_LIDX + o) = rec1[k] & 0xFFFFu;
                *(u32*)(smem + K2_LE + o) = rec1[k] & 0xFFFF0000u;
            }
        }
#pragma unroll
        for (int k = 0; k < 4; ++k) {
            const bool v = p0 + k < c0, ok = v && slot[k] < CAPN;
            ovf |= v && !ok;
            const u32 o = ok ? dl0[k] * LSTB + slot[k] * 4 : K2_TRASH + lane * 4;
            *(u32*)(smem + K2_LIDX + o) = rec0[k] & 0xFFFFu;
            *(u32*)(smem + K2_LE + o) = rec0[k] & 0xFFFF0000u;
        }
        if (ovf) misc[1] = 1;
    }
    STAMP(3);
    WAIT_V(0); WAIT_L(0); BAR();
    STAMP(4);
    const bool slow = misc[1] != 0;

    const int g = lane >> 4, j = lane & 15;
    const u32x2 wcv = *(const u32x2*)(wc + slice * 64 + j * 4);
    const f32x2 w01 = {__uint_as_float(wcv.x << 16) * NLOG2E, __uint_as_float(wcv.x & 0xFFFF0000u) * NLOG2E};
    const f32x2 w23 = {__uint_as_float(wcv.y << 16) * NLOG2E, __uint_as_float(wcv.y & 0xFFFF0000u) * NLOG2E};
    const u32 joff = j * 16;

    if (slow) {
        u32 sel = 0x00003F80u;
        asm volatile("" : "+s"(sel));
        for (int task = w; task * 4 < nvalid; task += 16) {
            const int n = task * 4 + g;
            const bool nv = n < nvalid;
            const int node = node0 + (nv ? n : 0);
            const u32x4 au = *(const u32x4*)(aub + (size_t)node * 256 + joff);
            const u32x2 xv = *(const u32x2*)(x + (size_t)node * DD + slice * 64 + j * 4);
            const f32x2 a01 = {__uint_as_float(au.x << 16), __uint_as_float(au.y << 16)};
            const f32x2 a23 = {__uint_as_float(au.z << 16), __uint_as_float(au.w << 16)};
            Acc acc = {{0.f, 0.f}, {0.f, 0.f}, {0.f, 0.f}, {0.f, 0.f}};
            const int lo = node0 + task * 4;
            for (int e0 = 0; e0 < NE; e0 += 64) {
                const int d = dst[e0 + lane];
                unsigned long long m = __ballot((u32)(d - lo) < 4u);
                if (m) {
                    const u32 rc = (u32)src[e0 + lane] | ((u32)ea[e0 + lane] << 16);
                    while (m) {
                        const int l = __builtin_ctzll(m); m &= m - 1;
                        const int dd = __builtin_amdgcn_readlane(d, l);
                        const u32 rr = __builtin_amdgcn_readlane(rc, l);
                        const u32 rec = (nv && dd == node) ? rr : NEUTRAL_REC;
                        const u32x4 dv = *(const u32x4*)(bvb + (size_t)((rec & 0xFFFFu) * 256 + joff));
                        edge_step(acc, dv, rec, a01, a23, w01, w23, sel);
                    }
                }
            }
            if (nv) node_finish(acc, au, xv, out + (size_t)node * DD + slice * 64 + j * 4);
        }
        return;
    }

    u32 mydeg = 0, myrank = 0;
    if (tid < nvalid) { mydeg = cnt[tid]; myrank = atomicAdd(&hist[63 - mydeg], 1u); }
    WAIT_L(0); BAR();
    if (w < 3) {
        const u32 h0 = hist[lane];
        u32 v = h0;
        v = dpp_add<0x111, 0xf>(v); v = dpp_add<0x112, 0xf>(v); v = dpp_add<0x114, 0xf>(v); v = dpp_add<0x118, 0xf>(v);
        v = dpp_add<0x142, 0xa>(v); v = dpp_add<0x143, 0xc>(v);
        const u32 base = (u32)__builtin_amdgcn_ds_bpermute((int)((63 - mydeg) * 4), (int)(v - h0));
        if (tid < nvalid) {
            const u32 pos = base + myrank;
            const u32x2 ent = {(u32)(K2_LIDX + tid * LSTB), (u32)(tid * 256) + mydeg};
            u32x2* o = (u32x2*)(smem + K2_ORD) + pos;
            o[0] = ent;
            if (pos == (u32)nvalid - 1) { o[1] = ent; o[2] = ent; o[3] = ent; }
        }
    }
    WAIT_L(0); BAR();
    STAMP(5);
    STAMPC(14);

    const int ntask = (nvalid + 3) >> 2;
    if (w < ntask) {
        u32x4 srdBV, srdO;
        { const size_t p = (size_t)bvb; srdBV = (u32x4){(u32)p, (u32)(p >> 32) | (256u << 16), 0xFFFFFFFFu, 0x00020000u}; }
        { const size_t ob = (size_t)node0 * 512 + slice * 128; const size_t p = (size_t)out + ob; srdO = (u32x4){(u32)p, (u32)(p >> 32), (u32)((size_t)NN * 512 - ob), 0x00020000u}; }
        u32 g8 = K2_ORD + g * 8, ctr = K2_MISC, one = 1, j8 = j * 8;
        unsigned long long eps2 = 0x358637BD358637BDull;
        u32 s_cnt, s_nxt, s_t, s_tc;
#define STAGE(DS0, DS1, DS2, DS3, DSL, DSH, IX, EP, E1, DN, IXN, O1, O2) \
            "s_waitcnt lgkmcnt(0)\n\t" \
            "buffer_load_dwordx4 " DN ", " IXN ", %[bv], 0 idxen offen\n\t" \
            "v_pk_fma_f32 v[112:113], " EP ", v[100:101], v[96:97] op_sel_hi:[0,1,1]\n\t" \
            "v_pk_fma_f32 v[114:115], " EP ", v[102:103], v[98:99] op_sel_hi:[0,1,1]\n\t" \
            "ds_read_b32 " IX ", v89 offset:" O1 "\n\t" \
            "ds_read_b32 " E1 ", v89 offset:" O2 "\n\t" \
            "s_waitcnt vmcnt(3)\n\t" \
            "v_dot2c_f32_bf16 v112, %[slo], " DS0 "\n\t" \
            "v_dot2c_f32_bf16 v113, %[slo], " DS1 "\n\t" \
            "v_dot2c_f32_bf16 v114, %[slo], " DS2 "\n\t" \
            "v_dot2c_f32_bf16 v115, %[slo], " DS3 "\n\t" \
            "v_exp_f32 v112, v112\n\t" \
            "v_exp_f32 v113, v113\n\t" \
            "v_exp_f32 v114, v114\n\t" \
            "v_exp_f32 v115, v115\n\t" \
            "v_pk_add_f32 v[112:113], v[112:113], 1.0 op_sel_hi:[1,0]\n\t" \
            "v_pk_add_f32 v[114:115], v[114:115], 1.0 op_sel_hi:[1,0]\n\t" \
            "v_rcp_f32 v112, v112\n\t" \
            "v_rcp_f32 v113, v113\n\t" \
            "v_rcp_f32 v114, v114\n\t" \
            "v_rcp_f32 v115, v115\n\t" \
            "s_sub_u32 %[cnt], %[cnt], 1\n\t" \
            "v_pk_add_f32 v[108:109], v[108:109], v[112:113]\n\t" \
            "v_pk_fma_f32 v[104:105], v[112:113], " DSL ", v[104:105]\n\t" \
            "v_pk_add_f32 v[110:111], v[110:111], v[114:115]\n\t" \
            "v_pk_fma_f32 v[106:107], v[114:115], " DSH ", v[106:107]\n\t" \
            "s_cmp_eq_u32 %[cnt], 0\n\t"
#define PRIO_ON 1
#define PRIO_INIT "s_setprio 3\n\t"
#define PRIO_STEP \
            "s_add_u32 %[tc], %[tc], 1\n\t" \
            "s_cmp_lg_u32 %[tc], 1\n\t s_cbranch_scc1 L_p1%=\n\t s_setprio 2\n\t L_p1%=:\n\t" \
            "s_cmp_lg_u32 %[tc], 2\n\t s_cbranch_scc1 L_p2%=\n\t s_setprio 1\n\t L_p2%=:\n\t" \
            "s_cmp_lg_u32 %[tc], 3\n\t s_cbranch_scc1 L_p3%=\n\t s_setprio 0\n\t L_p3%=:\n\t"
        asm volatile(
            PRIO_INIT
            "s_mov_b32 %[tc], 0\n\t"
            "s_lshl_b32 %[t], %[task], 5\n\t"
            "v_add_u32 v127, %[t], v91\n\t"
            "ds_read_b64 v[122:123], v127\n\t"
            "s_mov_b64 exec, 1\n\t"
            "ds_add_rtn_u32 v125, v93, v95\n\t"
            "s_mov_b64 exec, -1\n\t"
            "L_task%=:\n\t"
            "s_waitcnt lgkmcnt(0)\n\t"
            "v_readfirstlane_b32 %[nxt], v125\n\t"
            "v_readfirstlane_b32 %[cnt], v123\n\t"
            "v_mov_b32 v89, v122\n\t"
            "v_and_b32 v127, 0xffffff00, v123\n\t"
            "ds_read_b32 v80, v89\n\t"
            "ds_read_b32 v82, v89 offset:4\n\t"
            "ds_read_b32 v84, v89 offset:8\n\t"
            "ds_read_b32 v86, v89 offset:12\n\t"
            "ds_read_b32 v88, v89 offset:%c[le0]\n\t"
            "ds_read_b32 v90, v89 offset:%c[le1]\n\t"
            "ds_read_b32 v92, v89 offset:%c[le2]\n\t"
            "ds_read_b32 v94, v89 offset:%c[le3]\n\t"
            "v_add_u32 v112, v127, v81\n\t"
            "v_lshl_add_u32 v124, v127, 1, v126\n\t"
            "v_lshrrev_b32 v113, 1, v112\n\t"
            "ds_read_b128 v[116:119], v112 offset:%c[au]\n\t"
            "ds_read_b64 v[120:121], v113\n\t"
            "s_and_b32 %[cnt], %[cnt], 0xff\n\t"
            "s_lshl_b32 %[t], %[nxt], 5\n\t"
            "v_add_u32 v127, %[t], v91\n\t"
            "ds_read_b64 v[122:123], v127\n\t"
            "s_mov_b64 exec, 1\n\t"
            "ds_add_rtn_u32 v125, v93, v95\n\t"
            "s_mov_b64 exec, -1\n\t"
            "s_waitcnt lgkmcnt(9)\n\t"
            "buffer_load_dwordx4 v[64:67], v[80:81], %[bv], 0 idxen offen\n\t"
            "buffer_load_dwordx4 v[68:71], v[82:83], %[bv], 0 idxen offen\n\t"
            "buffer_load_dwordx4 v[72:75], v[84:85], %[bv], 0 idxen offen\n\t"
            "v_mov_b64 v[104:105], 0\n\t"
            "v_mov_b64 v[106:107], 0\n\t"
            "v_mov_b64 v[108:109], 0\n\t"
            "v_mov_b64 v[110:111], 0\n\t"
            "s_waitcnt lgkmcnt(2)\n\t"
            "v_lshlrev_b32 v96, 16, v116\n\t"
            "v_lshlrev_b32 v97, 16, v117\n\t"
            "v_lshlrev_b32 v98, 16, v118\n\t"
            "v_lshlrev_b32 v99, 16, v119\n\t"
            "s_cmp_eq_u32 %[cnt], 0\n\t"
            "s_cbranch_scc1 L_fin%=\n\t"
            "L_loop%=:\n\t"
            STAGE("v64", "v65", "v66", "v67", "v[64:65]", "v[66:67]", "v80", "v[88:89]", "v88", "v[76:79]", "v[86:87]", "16", "%c[le4]")
            "s_cbranch_scc1 L_fin%=\n\t"
            STAGE("v68", "v69", "v70", "v71", "v[68:69]", "v[70:71]", "v82", "v[90:91]", "v90", "v[64:67]", "v[80:81]", "20", "%c[le5]")
            "s_cbranch_scc1 L_fin%=\n\t"
            STAGE("v72", "v73", "v74", "v75", "v[72:73]", "v[74:75]", "v84", "v[92:93]", "v92", "v[68:71]", "v[82:83]", "24", "%c[le6]")
            "s_cbranch_scc1 L_fin%=\n\t"
            STAGE("v76", "v77", "v78", "v79", "v[76:77]", "v[78:79]", "v86", "v[94:95]", "v94", "v[72:75]", "v[84:85]", "28", "%c[le7]")
            "v_add_u32 v89, 16, v89\n\t"
            "s_cbranch_scc0 L_loop%=\n\t"
            "L_fin%=:\n\t"
            "v_pk_add_f32 v[108:109], v[108:109], %[eps]\n\t"
            "v_pk_add_f32 v[110:111], v[110:111], %[eps]\n\t"
            "v_rcp_f32 v108, v108\n\t"
            "v_rcp_f32 v109, v109\n\t"
            "v_rcp_f32 v110, v110\n\t"
            "v_rcp_f32 v111, v111\n\t"
            "v_pk_mul_f32 v[104:105], v[104:105], v[108:109]\n\t"
            "v_pk_mul_f32 v[106:107], v[106:107], v[110:111]\n\t"
            "v_dot2c_f32_bf16 v104, %[shi], v116\n\t"
            "v_dot2c_f32_bf16 v105, %[shi], v117\n\t"
            "v_dot2c_f32_bf16 v106, %[shi], v118\n\t"
            "v_dot2c_f32_bf16 v107, %[shi], v119\n\t"
            "v_maximum3_f32 v104, v104, 0, 0\n\t"
            "v_maximum3_f32 v105, v105, 0, 0\n\t"
            "v_maximum3_f32 v106, v106, 0, 0\n\t"
            "v_maximum3_f32 v107, v107, 0, 0\n\t"
            "v_dot2c_f32_bf16 v104, %[slo], v120\n\t"
            "v_dot2c_f32_bf16 v105, %[shi], v120\n\t"
            "v_dot2c_f32_bf16 v106, %[slo], v121\n\t"
            "v_dot2c_f32_bf16 v107, %[shi], v121\n\t"
            "s_nop 1\n\t"
            "v_cvt_pk_bf16_f32 v112, v104, v105\n\t"
            "v_cvt_pk_bf16_f32 v113, v106, v107\n\t"
            "buffer_store_dwordx2 v[112:113], v124, %[o], 0 offen\n\t"
            PRIO_STEP
            "s_cmp_lt_u32 %[nxt], %[ntask]\n\t"
            "s_cbranch_scc1 L_task%=\n\t"
            "s_waitcnt vmcnt(0) lgkmcnt(0)"
            : [cnt] "=&s"(s_cnt), [nxt] "=&s"(s_nxt), [t] "=&s"(s_t), [tc] "=&s"(s_tc)
            : "{v81}"(joff), "{v83}"(joff), "{v85}"(joff), "{v87}"(joff), "{v91}"(g8), "{v93}"(ctr), "{v95}"(one), "{v126}"(j8),
              "{v[100:101]}"(w01), "{v[102:103]}"(w23),
              [task] "s"(w), [ntask] "s"(ntask), [bv] "s"(srdBV), [o] "s"(srdO), [slo] "s"(0x00003F80u), [shi] "s"(0x3F800000u), [eps] "s"(eps2),
              [au] "i"(K2_AU), [le0] "i"(K2_LSZ), [le1] "i"(K2_LSZ + 4), [le2] "i"(K2_LSZ + 8), [le3] "i"(K2_LSZ + 12),
              [le4] "i"(K2_LSZ + 16), [le5] "i"(K2_LSZ + 20), [le6] "i"(K2_LSZ + 24), [le7] "i"(K2_LSZ + 28)
            : "v64", "v65", "v66", "v67", "v68", "v69", "v70", "v71", "v72", "v73", "v74", "v75", "v76", "v77", "v78", "v79",
              "v80", "v82", "v84", "v86", "v88", "v89", "v90", "v92", "v94", "v96", "v97", "v98", "v99",
              "v104", "v105", "v106", "v107", "v108", "v109", "v110", "v111", "v112", "v113", "v114", "v115",
              "v116", "v117", "v118", "v119", "v120", "v121", "v122", "v123", "v124", "v125", "v127", "scc", "memory");
#undef STAGE
    }
    STAMP(10);
    STAMPC(15);
}

extern "C" void kernel_launch(void* const* d_in, const int* in_sizes, int n_in,
                              void* d_out, int out_size, void* d_ws, size_t ws_size,
                              hipStream_t stream) {
    const u16* x = (const u16*)d_in[0];
    const u16* ea = (const u16*)d_in[1];
    const int* src = (const int*)d_in[2];
    const int* dst = (const int*)d_in[3];
    const u16* wa = (const u16*)d_in[4];
    const u16* wb = (const u16*)d_in[5];
    const u16* wc = (const u16*)d_in[6];
    const u16* wu = (const u16*)d_in[7];
    const u16* wv = (const u16*)d_in[8];
    (void)hipFuncSetAttribute((const void*)k1_gemm_bin, hipFuncAttributeMaxDynamicSharedMemorySize, K1_LDS);
    k1_gemm_bin<<<256, 512, K1_LDS, stream>>>(x, wa, wb, wu, wv, src, dst, ea, (char*)d_ws);
    (void)hipFuncSetAttribute((const void*)k2_edges, hipFuncAttributeMaxDynamicSharedMemorySize, K2_LDS);
    k2_edges<<<256, 1024, K2_LDS, stream>>>(x, wc, src, dst, ea, (const char*)d_ws, (u16*)d_out);
}
```

```cpp
#include <hip/hip_runtime.h>
#include <hip/hip_bf16.h>
#include <stdint.h>

typedef unsigned short u16;
typedef unsigned int u32;
typedef u32 u32x2 __attribute__((ext_vector_type(2)));
typedef u32 u32x4 __attribute__((ext_vector_type(4)));
typedef float f32x2 __attribute__((ext_vector_type(2)));
typedef float f32x4 __attribute__((ext_vector_type(4)));
typedef float f32x16 __attribute__((ext_vector_type(16)));
typedef short s16x4 __attribute__((ext_vector_type(4)));
typedef short s16x8 __attribute__((ext_vector_type(8)));
typedef __bf16 bf16x8 __attribute__((ext_vector_type(8)));
typedef __bf16 bf16x4 __attribute__((ext_vector_type(4)));
typedef __bf16 bf16x2 __attribute__((ext_vector_type(2)));

constexpr int NN = 10000;
constexpr int NE = 160000;
constexpr int DD = 256;
constexpr int NP = 10048;
constexpr int RG = 320;
constexpr int NBK = 64;
constexpr int NB = 157;
constexpr int NCH = 64;
constexpr int CHE = 2500;
constexpr int CAP = 80;
constexpr int CAPN = 44;
constexpr int LST = 45;
constexpr float NLOG2E = -1.4426950408889634f;
constexpr u32 NEUTRAL_REC = (u32)NN;
constexpr u32 NEUTRAL_BV = 0x00007F00u;

constexpr size_t OFF_BV = 0;
constexpr size_t OFF_AU = OFF_BV + (size_t)4 * NP * 256;
constexpr size_t OFF_R4 = OFF_AU + (size_t)4 * NP * 256;
constexpr size_t OFF_R2 = OFF_R4 + (size_t)NBK * NCH * CAP * 4;
constexpr size_t OFF_CNT = OFF_R2 + (size_t)NBK * NCH * CAP * 2;

#define LDS_AS __attribute__((address_space(3)))
#define GLB_AS __attribute__((address_space(1)))

__device__ __forceinline__ void glds16(const void* g, void* l) {
    __builtin_amdgcn_global_load_lds((const GLB_AS u32*)g, (LDS_AS u32*)l, 16, 0, 0);
}
__device__ __forceinline__ void glds16a(const void* sbase, u32 voff, u32 lds) {
    u32 keep;
    asm volatile("s_mov_b32 %0, m0\n\ts_mov_b32 m0, %3\n\ts_nop 0\n\tglobal_load_lds_dwordx4 %1, %2\n\ts_mov_b32 m0, %0" : "=&s"(keep) : "v"(voff), "s"(sbase), "s"(lds) : "memory");
}
__device__ __forceinline__ u32 cvt_pk_bf16(float lo, float hi) {
    const f32x2 v = {lo, hi}; return __builtin_bit_cast(u32, __builtin_convertvector(v, bf16x2));
}
#define WAIT_V(n) asm volatile("s_waitcnt vmcnt(" #n ")" ::: "memory")
#define WAIT_L(n) asm volatile("s_waitcnt lgkmcnt(" #n ")" ::: "memory")
#define BAR() __builtin_amdgcn_s_barrier()
#define EXIT2_AT 99
#ifndef EXIT_AT
#define EXIT_AT 99
#endif
#define STORE_AUX 16
#define SPAN0()
#define SPAN1(K)
#define STAMP(i)
#define STAMPC(i)
#define TICK(v)

constexpr int K1_R0 = 0, K1_R1 = 65536, K1_SCR = 131072;
constexpr int K1_ST4 = K1_SCR, K1_ST2 = K1_ST4 + NBK * CAP * 4, K1_BC = K1_ST2 + NBK * CAP * 2;
constexpr int K1_LDS = K1_SCR + 32768;
static_assert(K1_BC + NBK * 4 <= K1_LDS, "lds");

__global__ __launch_bounds__(512, 2) void k1_gemm_bin(
    const u16* __restrict__ x, const u16* __restrict__ wa, const u16* __restrict__ wb, const u16* __restrict__ wu, const u16* __restrict__ wv,
    const int* __restrict__ src, const int* __restrict__ dst, const u16* __restrict__ ea, char* __restrict__ ws) {
    extern __shared__ __attribute__((aligned(16))) char smem[];
    const int tid = threadIdx.x, lane = tid & 63;
    const int w = __builtin_amdgcn_readfirstlane(tid >> 6);
    const int b = blockIdx.x;
    const int type = (b >> 5) & 1;
    const int ct2 = (b >> 3) & 3;
    const int rg = (b & 7) * 4 + (b >> 6);
    const int row0 = rg * RG;
    const int chunk = (b & 31) | ((b >> 6) << 5) ;
    const bool binner = type == 1 && b < 128;
    constexpr int KOFF = 0;
    SPAN0();
#if EXIT_AT == 0
    if (ws) { SPAN1(0); return; }
#endif
    STAMP(0);
    int bd[5], bs[5]; u32 be[5];
    if (binner) {
#pragma unroll
        for (int i = 0; i < 5; ++i) {
            const int k = tid + 512 * i;
            const int e = chunk * CHE + (k < CHE ? k : 0);
            bd[i] = dst[e]; bs[i] = src[e]; be[i] = ea[e];
        }
        if (tid < NBK) *(u32*)(smem + K1_BC + tid * 4) = 0;
    }
    {
        const int m = w >> 2;
        const u16* W = type == 0 ? (m == 0 ? wb : wv) : (m == 0 ? wa : wu);
        const int kb0 = (w & 3) * 64, kr = lane >> 3;
        const u16* g = W + (size_t)(kb0 + kr) * DD + ct2 * 64 + (((lane & 7) ^ ((kr & 2) << 1)) << 3);
#pragma unroll
        for (int i = 0; i < 8; ++i) glds16(g + (size_t)i * 8 * DD, smem + K1_R0 + m * 32768 + (kb0 / 8 + i) * 1024);
    }
    const int xr = lane >> 5, xp = lane & 31;
    auto stage_x = [&](int tile, int q, int ldsbase) {
        const int r = q * 2 + xr;
        int grow = row0 + tile * 32 + r; grow = grow < NN ? grow : NN - 1;
        glds16((const char*)x + (u32)(grow * 512 + ((xp ^ (r & 15)) << 4)), smem + ldsbase + q * 1024);
    };
#pragma unroll
    for (int i = 0; i < 8; ++i) { const int q = w * 8 + i; stage_x(q >> 4, q & 15, K1_R1 + (q >> 4) * 16384); }
    STAMP(1);
    WAIT_V(8); WAIT_L(0);
    BAR();
    STAMP(2);
#if EXIT_AT == 1
    if (ws) { SPAN1(0); return; }
#endif
    const int ch = w >> 2;
    bf16x8 wf0[16], wf1[16];
    {
        const int h = lane >> 5, blk = (lane >> 4) & 1, q = (lane & 15) >> 2, p = lane & 3;
        const u32 base = K1_R0 + (8 * h + q) * 128 + (((ch * 4 + blk * 2 + (p >> 1)) ^ ((q & 2) << 1)) << 4) + (p & 1) * 8;
#define TRRD(S) { u32x2 a0, a1, b0, b1; \
            asm volatile("ds_read_b64_tr_b16 %0, %4 offset:%c5\n\tds_read_b64_tr_b16 %1, %4 offset:%c6\n\t" \
                         "ds_read_b64_tr_b16 %2, %4 offset:%c7\n\tds_read_b64_tr_b16 %3, %4 offset:%c8" \
                         : "=&v"(a0), "=&v"(a1), "=&v"(b0), "=&v"(b1) \
                         : "v"(base), "i"((S) * 2048), "i"((S) * 2048 + 512), "i"(32768 + (S) * 2048), "i"(32768 + (S) * 2048 + 512) : "memory"); \
            wf0[S] = __builtin_bit_cast(bf16x8, __builtin_shufflevector(a0, a1, 0, 1, 2, 3)); \
            wf1[S] = __builtin_bit_cast(bf16x8, __builtin_shufflevector(b0, b1, 0, 1, 2, 3)); }
        TRRD(0) TRRD(1) TRRD(2) TRRD(3) TRRD(4) TRRD(5) TRRD(6) TRRD(7) TRRD(8) TRRD(9) TRRD(10) TRRD(11) TRRD(12) TRRD(13) TRRD(14) TRRD(15)
#undef TRRD
    }
    if (binner) {
#pragma unroll
        for (int i = 0; i < 5; ++i) {
            const int k = tid + 512 * i;
            if (k < CHE) {
                const int bk = (int)(((u32)bd[i] * 53432u) >> 23);
                const int dl = bd[i] - bk * NB;
                const u32 pos = atomicAdd((u32*)(smem + K1_BC + bk * 4), 1u);
                if (pos < CAP) {
                    *(u32*)(smem + K1_ST4 + (bk * CAP + pos) * 4) = (u32)bs[i] | (be[i] << 16);
                    *(u16*)(smem + K1_ST2 + (bk * CAP + pos) * 2) = (u16)dl;
                }
            }
        }
    }
    STAMP(3);
    WAIT_V(0); WAIT_L(0);
    __builtin_amdgcn_sched_barrier(0);
    BAR();
    STAMP(4);
#if EXIT_AT == 2
    if (ws) { asm volatile("" :: "v"(wf0[0]), "v"(wf1[15])); SPAN1(0); return; }
#endif
#pragma unroll
    for (int i = 0; i < 8; ++i) { const int q = w * 8 + i; stage_x(4 + (q >> 4), q & 15, K1_R0 + (q >> 4) * 16384); }
    if (binner) {
        u32* R4 = (u32*)(ws + OFF_R4); u16* R2 = (u16*)(ws + OFF_R2); u32* CNT = (u32*)(ws + OFF_CNT);
        u32x4 t4[3]; u32x2 t2[3];
#pragma unroll
        for (int i = 0; i < 3; ++i) { const int q = tid + 512 * i; const int qq = q < NBK * (CAP / 4) ? q : 0;
            t4[i] = *(const u32x4*)(smem + K1_ST4 + qq * 16); t2[i] = *(const u32x2*)(smem + K1_ST2 + qq * 8); }
        const u32 cv = *(const u32*)(smem + K1_BC + (tid & 63) * 4);
#pragma unroll
        for (int i = 0; i < 3; ++i) { const int q = tid + 512 * i;
            if (q < NBK * (CAP / 4)) { const int bk = q / (CAP / 4), o = q - bk * (CAP / 4);
                *(u32x4*)(R4 + ((size_t)bk * NCH + chunk) * CAP + o * 4) = t4[i];
                *(u32x2*)(R2 + ((size_t)bk * NCH + chunk) * CAP + o * 4) = t2[i]; } }
        if (tid < NBK) CNT[tid * NCH + chunk] = cv;
        WAIT_L(0);
        BAR();
    }
    if (rg == 0 && type == 0 && w == 0) ((u32*)(ws + OFF_BV))[((size_t)ct2 * NP + NN) * 64 + lane] = NEUTRAL_BV;

    const int r = lane & 31, h = lane >> 5;
    const int abase = r * 512 + ((((r & 14) | ((h ^ r) & 1))) << 4);
    char* const scr = smem + K1_SCR + w * 4096;
    const __amdgpu_buffer_rsrc_t trs = __builtin_amdgcn_make_buffer_rsrc(ws + (type ? OFF_AU : OFF_BV) + (size_t)ct2 * NP * 256, 0, NN * 256, 0x00020000);
    const u32 svo = (lane >> 3) * 256 + ch * 128 + (lane & 7) * 16;

    const int s4 = w & 3;
    if (ch == 0) __builtin_amdgcn_s_setprio(1);
    STAMP(5);
#pragma unroll 1
    for (int st = 0; st < (EXIT_AT == 3 ? 1 : (EXIT_AT == 4 ? 2 : 3)); ++st) {
        int tile, ldsbase;
        if (st == 0) { tile = s4; ldsbase = K1_R1 + s4 * 16384; }
        else if (st == 1) { tile = 4 + s4; ldsbase = K1_R0 + s4 * 16384; }
        else { const int t = (w < 2) ? w : (w >= 6 ? w - 6 : 99); tile = 8 + t; ldsbase = K1_R1 + (t & 1) * 16384; }
        const int trow = row0 + tile * 32;
        const bool active = tile < 10 && trow < NN;
        unsigned long long tkA = 0, tkB = 0, tkC = 0, tkD = 0;
        if (active) {
            TICK(tkA);
            bf16x8 a[16];
#pragma unroll
            for (int s = 0; s < 8; ++s) a[s] = *(const bf16x8*)(smem + ldsbase + (abase ^ (s << 5)));
            __builtin_amdgcn_sched_barrier(0);
            f32x16 acc0, acc1;
#pragma unroll
            for (int i = 0; i < 16; ++i) { acc0[i] = 0.f; acc1[i] = 0.f; }
#pragma unroll
            for (int s = 0; s < 8; ++s) {
                a[s + 8] = *(const bf16x8*)(smem + ldsbase + (abase ^ (s << 5)) + 256);
                acc0 = __builtin_amdgcn_mfma_f32_32x32x16_bf16(a[s], wf0[s], acc0, 0, 0, 0);
                acc1 = __builtin_amdgcn_mfma_f32_32x32x16_bf16(a[s], wf1[s], acc1, 0, 0, 0);
                __builtin_amdgcn_sched_group_barrier(0x100, 1, 0);
                __builtin_amdgcn_sched_group_barrier(0x8, 2, 0);
            }
            __builtin_amdgcn_sched_barrier(0);
#pragma unroll
            for (int s = 8; s < 16; ++s) {
                acc0 = __builtin_amdgcn_mfma_f32_32x32x16_bf16(a[s], wf0[s], acc0, 0, 0, 0);
                acc1 = __builtin_amdgcn_mfma_f32_32x32x16_bf16(a[s], wf1[s], acc1, 0, 0, 0);
            }
            TICK(tkB);
            char* const sw = scr + h * 512 + r * 4;
            if (type == 0) {
#pragma unroll
                for (int i = 0; i < 16; i += 2) {
                    const u32 bb = cvt_pk_bf16(acc0[i] * NLOG2E, acc0[i + 1] * NLOG2E);
#pragma unroll
                    for (int j = 0; j < 2; ++j) {
                        const u32 vb = __float_as_uint(acc1[i + j]);
                        const u32 bl = j ? (bb >> 16) : (bb & 0xFFFFu);
                        const u32 u = __builtin_elementwise_sub_sat((vb & 0x7FFFFFFFu) + 0x8000u, bl);
                        *(u32*)(sw + (((i + j) & 3) + 8 * ((i + j) >> 2)) * 128) = (vb & 0x80000000u) | (u & 0x7FFF0000u) | bl;
                    }
                }
            } else {
#pragma unroll
                for (int i = 0; i < 16; ++i) *(u32*)(sw + ((i & 3) + 8 * (i >> 2)) * 128) = cvt_pk_bf16(acc0[i] * NLOG2E, acc1[i]);
            }
            TICK(tkC);
#pragma unroll
            for (int i = 0; i < 4; ++i) {
                const u32x4 v = *(const u32x4*)(scr + i * 1024 + lane * 16);
                __builtin_amdgcn_raw_buffer_store_b128(v, trs, svo + (u32)(trow + i * 8) * 256, 0, STORE_AUX);
            }
        }
        TICK(tkD);
        if (st == 0) STAMP(6); else if (st == 1) STAMP(8); else STAMP(10);
        if (st < 2) {
            if (active) WAIT_V(4); else WAIT_V(0);
            BAR();
            if (st == 0) {
#pragma unroll
                for (int i = 0; i < 4; ++i) { const int q = w * 4 + i; stage_x(8 + (q >> 4), q & 15, K1_R1 + (q >> 4) * 16384); }
            }
        }
    }
    SPAN1(0);
}

constexpr int LSTB = LST * 4;
constexpr int K2_X = 0;
constexpr int K2_AU = K2_X + 20480;
constexpr int K2_LIDX = K2_AU + 40960;
constexpr int K2_LSZ = (NB * LSTB + 32 + 256 + 15) / 16 * 16;
constexpr int K2_LE = K2_LIDX + K2_LSZ;
constexpr int K2_CNT = K2_LE + K2_LSZ;
constexpr int K2_HIST = K2_CNT + 160 * 4;
constexpr int K2_ORD = K2_HIST + 128 * 4;
constexpr int K2_MISC = K2_ORD + 80 * 32;
constexpr int K2_LDS = K2_MISC + 16;
constexpr int K2_TRASH = NB * LSTB + 32;
static_assert(K2_TRASH + 256 <= K2_LSZ, "trash");
static_assert(K2_LSZ < 65536 - 64, "ds offset");

__device__ __forceinline__ float dot2sel(u32 d, u32 sel, float c) {
    return __builtin_amdgcn_fdot2_f32_bf16(__builtin_bit_cast(bf16x2, d), __builtin_bit_cast(bf16x2, sel), c, false);
}

struct Acc { f32x2 n01, n23, d01, d23; };

__device__ __forceinline__ void edge_step(Acc& a, const u32x4 d, const u32 rec, const f32x2 a01, const f32x2 a23, const f32x2 w01, const f32x2 w23, const u32 sel) {
    const float e = __uint_as_float(rec & 0xFFFF0000u);
    const f32x2 ee = {e, e};
    const f32x2 t01 = __builtin_elementwise_fma(ee, w01, a01);
    const f32x2 t23 = __builtin_elementwise_fma(ee, w23, a23);
    f32x2 E01, E23;
    E01.x = __builtin_amdgcn_exp2f(dot2sel(d.x, sel, t01.x));
    E01.y = __builtin_amdgcn_exp2f(dot2sel(d.y, sel, t01.y));
    E23.x = __builtin_amdgcn_exp2f(dot2sel(d.z, sel, t23.x));
    E23.y = __builtin_amdgcn_exp2f(dot2sel(d.w, sel, t23.y));
    const f32x2 one = {1.f, 1.f};
    E01 += one; E23 += one;
    f32x2 s01, s23;
    s01.x = __builtin_amdgcn_rcpf(E01.x); s01.y = __builtin_amdgcn_rcpf(E01.y);
    s23.x = __builtin_amdgcn_rcpf(E23.x); s23.y = __builtin_amdgcn_rcpf(E23.y);
    a.d01 += s01; a.d23 += s23;
    const f32x2 v01 = {__uint_as_float(d.x), __uint_as_float(d.y)};
    const f32x2 v23 = {__uint_as_float(d.z), __uint_as_float(d.w)};
    a.n01 = __builtin_elementwise_fma(s01, v01, a.n01);
    a.n23 = __builtin_elementwise_fma(s23, v23, a.n23);
}

__device__ __forceinline__ void node_finish(const Acc& a, const u32x4 au, const u32x2 xv, u16* outp) {
    const float num[4] = {a.n01.x, a.n01.y, a.n23.x, a.n23.y};
    const float den[4] = {a.d01.x, a.d01.y, a.d23.x, a.d23.y};
    const u32 aus[4] = {au.x, au.y, au.z, au.w};
    const float xs[4] = {__uint_as_float(xv.x << 16), __uint_as_float(xv.x & 0xFFFF0000u), __uint_as_float(xv.y << 16), __uint_as_float(xv.y & 0xFFFF0000u)};
    float o[4];
#pragma unroll
    for (int c = 0; c < 4; ++c) {
        const float z = __uint_as_float(aus[c] & 0xFFFF0000u) + num[c] * __builtin_amdgcn_rcpf(den[c] + 1e-6f);
        const float rl = z > 0.f ? z : 0.f * z;
        o[c] = xs[c] + rl;
    }
    u32x2 ov; ov.x = cvt_pk_bf16(o[0], o[1]); ov.y = cvt_pk_bf16(o[2], o[3]);
    *(u32x2*)outp = ov;
}

template <int CTRL, int RM> __device__ __forceinline__ u32 dpp_add(u32 v) {
    return v + (u32)__builtin_amdgcn_update_dpp(0, (int)v, CTRL, RM, 0xf, false);
}

__global__ __launch_bounds__(1024, 4) void k2_edges(
    const u16* __restrict__ x, const u16* __restrict__ wc, const int* __restrict__ src, const int* __restrict__ dst, const u16* __restrict__ ea,
    const char* __restrict__ ws, u16* __restrict__ out) {
    extern __shared__ __attribute__((aligned(16))) char smem[];
    const int tid = threadIdx.x, lane = tid & 63;
    const int w = __builtin_amdgcn_readfirstlane(tid >> 6);
    const int b = blockIdx.x;
    const int slice = (b & 7) >> 1;
    const int bucket = (b >> 3) * 2 + (b & 1);
    const int node0 = bucket * NB;
    const int nvalid = (NN - node0) < NB ? (NN - node0) : NB;
    u32* const cnt = (u32*)(smem + K2_CNT);
    u32* const hist = (u32*)(smem + K2_HIST);
    u32* const misc = (u32*)(smem + K2_MISC);
    constexpr int KOFF = 256 * 16;
    SPAN0();
    STAMP(0);

    const u32* R4 = (const u32*)(ws + OFF_R4) + (size_t)bucket * NCH * CAP;
    const u16* R2 = (const u16*)(ws + OFF_R2) + (size_t)bucket * NCH * CAP;
    const u32* CNT = (const u32*)(ws + OFF_CNT) + bucket * NCH;
    const int ch0 = tid / (CAP / 4), ch1 = (tid + 1024) / (CAP / 4);
    const u32x4 r40 = *(const u32x4*)(R4 + tid * 4);
    const u32x2 r20 = *(const u32x2*)(R2 + tid * 4);
    const u32 c0 = CNT[ch0];
    u32x4 r41 = {0, 0, 0, 0}; u32x2 r21 = {0, 0}; u32 c1 = 0;
    if (w < 4) { r41 = *(const u32x4*)(R4 + (tid + 1024) * 4); r21 = *(const u32x2*)(R2 + (tid + 1024) * 4); c1 = CNT[ch1]; }
    const char* const bvb = ws + OFF_BV + (size_t)slice * NP * 256;
    const char* const aub = ws + OFF_AU + (size_t)slice * NP * 256;
    const int g = lane >> 4, j = lane & 15;
    const u32x2 wcv = *(const u32x2*)(wc + slice * 64 + j * 4);
    for (int i = tid; i < K2_LSZ / 16; i += 1024) {
        *(u32x4*)(smem + K2_LIDX + i * 16) = (u32x4){NEUTRAL_REC, NEUTRAL_REC, NEUTRAL_REC, NEUTRAL_REC};
        *(u32x4*)(smem + K2_LE + i * 16) = (u32x4){0, 0, 0, 0};
    }
    if (tid < 160 + 64) cnt[tid] = 0;
    if (tid == 0) { misc[0] = 16; misc[1] = 0; }
    f32x2 w01 = {__uint_as_float(wcv.x << 16) * NLOG2E, __uint_as_float(wcv.x & 0xFFFF0000u) * NLOG2E};
    f32x2 w23 = {__uint_as_float(wcv.y << 16) * NLOG2E, __uint_as_float(wcv.y & 0xFFFF0000u) * NLOG2E};
    asm volatile("" : "+v"(w01), "+v"(w23));
    STAMP(1);
    WAIT_V(0); WAIT_L(0); BAR();
    {
        const char* ga = aub + (size_t)node0 * 256;
#pragma unroll
        for (int i = 0; i < 3; ++i) { const int q = w + 16 * i; if (q < 40) glds16a(ga, q * 1024 + lane * 16, K2_AU + q * 1024); }
        const char* gx = (const char*)x + slice * 128;
#pragma unroll
        for (int i = 0; i < 2; ++i) {
            const int q = w + 16 * i;
            if (q < 20) { int row = node0 + q * 8 + (lane >> 3); row = row < NN ? row : NN - 1;
                glds16a(gx, row * 512 + (lane & 7) * 16, K2_X + q * 1024); }
        }
    }
    STAMP(2);
    {
        const int p0 = (tid - ch0 * (CAP / 4)) * 4;
        const u32 rec0[4] = {r40.x, r40.y, r40.z, r40.w};
        const u32 dl0[4] = {r20.x & 0xFFFF, r20.x >> 16, r20.y & 0xFFFF, r20.y >> 16};
        u32 slot[4];
        bool ovf = c0 > CAP;
#pragma unroll
        for (int k = 0; k < 4; ++k) slot[k] = atomicAdd(&cnt[p0 + k < c0 ? dl0[k] : 224 + lane], 1u);
        if (w < 4) {
            const int p1 = (tid + 1024 - ch1 * (CAP / 4)) * 4;
            const u32 rec1[4] = {r41.x, r41.y, r41.z, r41.w};
            const u32 dl1[4] = {r21.x & 0xFFFF, r21.x >> 16, r21.y & 0xFFFF, r21.y >> 16};
            u32 slot1[4];
            ovf |= c1 > CAP;
#pragma unroll
            for (int k = 0; k < 4; ++k) slot1[k] = atomicAdd(&cnt[p1 + k < c1 ? dl1[k] : 224 + lane], 1u);
#pragma unroll
            for (int k = 0; k < 4; ++k) {
                const bool v = p1 + k < c1, ok = v && slot1[k] < CAPN;
                ovf |= v && !ok;
                const u32 o = ok ? dl1[k] * LSTB + slot1[k] * 4 : K2_TRASH + lane * 4;
                *(u32*)(smem + K2_LIDX + o) = rec1[k] & 0xFFFFu;
                *(u32*)(smem + K2_LE + o) = rec1[k] & 0xFFFF0000u;
            }
        }
#pragma unroll
        for (int k = 0; k < 4; ++k) {
            const bool v = p0 + k < c0, ok = v && slot[k] < CAPN;
            ovf |= v && !ok;
            const u32 o = ok ? dl0[k] * LSTB + slot[k] * 4 : K2_TRASH + lane * 4;
            *(u32*)(smem + K2_LIDX + o) = rec0[k] & 0xFFFFu;
            *(u32*)(smem + K2_LE + o) = rec0[k] & 0xFFFF0000u;
        }
        if (ovf) misc[1] = 1;
    }
    STAMP(3);
    WAIT_L(0); BAR();
    STAMP(4);
    const bool slow = misc[1] != 0;

    const u32 joff = j * 16;

    if (slow) {
        u32 sel = 0x00003F80u;
        asm volatile("" : "+s"(sel));
        for (int task = w; task * 4 < nvalid; task += 16) {
            const int n = task * 4 + g;
            const bool nv = n < nvalid;
            const int node = node0 + (nv ? n : 0);
            const u32x4 au = *(const u32x4*)(aub + (size_t)node * 256 + joff);
            const u32x2 xv = *(const u32x2*)(x + (size_t)node * DD + slice * 64 + j * 4);
            const f32x2 a01 = {__uint_as_float(au.x << 16), __uint_as_float(au.y << 16)};
            const f32x2 a23 = {__uint_as_float(au.z << 16), __uint_as_float(au.w << 16)};
            Acc acc = {{0.f, 0.f}, {0.f, 0.f}, {0.f, 0.f}, {0.f, 0.f}};
            const int lo = node0 + task * 4;
            for (int e0 = 0; e0 < NE; e0 += 64) {
                const int d = dst[e0 + lane];
                unsigned long long m = __ballot((u32)(d - lo) < 4u);
                if (m) {
                    const u32 rc = (u32)src[e0 + lane] | ((u32)ea[e0 + lane] << 16);
                    while (m) {
                        const int l = __builtin_ctzll(m); m &= m - 1;
                        const int dd = __builtin_amdgcn_readlane(d, l);
                        const u32 rr = __builtin_amdgcn_readlane(rc, l);
                        const u32 rec = (nv && dd == node) ? rr : NEUTRAL_REC;
                        const u32x4 dv = *(const u32x4*)(bvb + (size_t)((rec & 0xFFFFu) * 256 + joff));
                        edge_step(acc, dv, rec, a01, a23, w01, w23, sel);
                    }
                }
            }
            if (nv) node_finish(acc, au, xv, out + (size_t)node * DD + slice * 64 + j * 4);
        }
        return;
    }

    u32 mydeg = 0, myrank = 0;
    if (tid < nvalid) { mydeg = cnt[tid]; myrank = atomicAdd(&hist[63 - mydeg], 1u); }
    WAIT_L(0); BAR();
    if (w < 3) {
        const u32 h0 = hist[lane];
        u32 v = h0;
        v = dpp_add<0x111, 0xf>(v); v = dpp_add<0x112, 0xf>(v); v = dpp_add<0x114, 0xf>(v); v = dpp_add<0x118, 0xf>(v);
        v = dpp_add<0x142, 0xa>(v); v = dpp_add<0x143, 0xc>(v);
        const u32 base = (u32)__builtin_amdgcn_ds_bpermute((int)((63 - mydeg) * 4), (int)(v - h0));
        if (tid < nvalid) {
            const u32 pos = base + myrank;
            const u32x2 ent = {(u32)(K2_LIDX + tid * LSTB), (u32)(tid * 256) + mydeg};
            u32x2* o = (u32x2*)(smem + K2_ORD) + pos;
            o[0] = ent;
            if (pos == (u32)nvalid - 1) { o[1] = ent; o[2] = ent; o[3] = ent; }
        }
    }
    WAIT_V(0); WAIT_L(0); BAR();
    STAMP(5);
    STAMPC(14);

    const int ntask = (nvalid + 3) >> 2;
    if (w < ntask) {
        u32x4 srdBV, srdO;
        { const size_t p = (size_t)bvb; srdBV = (u32x4){(u32)p, (u32)(p >> 32) | (256u << 16), 0xFFFFFFFFu, 0x00020000u}; }
        { const size_t ob = (size_t)node0 * 512 + slice * 128; const size_t p = (size_t)out + ob; srdO = (u32x4){(u32)p, (u32)(p >> 32), (u32)((size_t)NN * 512 - ob), 0x00020000u}; }
        u32 g8 = K2_ORD + g * 8, ctr = K2_MISC, one = 1, j8 = j * 8;
        unsigned long long eps2 = 0x358637BD358637BDull;
        u32 s_cnt, s_nxt, s_t, s_tc;
#define STAGE(DS0, DS1, DS2, DS3, DSL, DSH, IX, EP, E1, DN, IXN, O1, O2) \
            "s_waitcnt lgkmcnt(0)\n\t" \
            "buffer_load_dwordx4 " DN ", " GIX(IXN) ", %[bv], 0 idxen offen\n\t" \
            "v_pk_fma_f32 v[112:113], " EP ", v[100:101], v[96:97] op_sel_hi:[0,1,1]\n\t" \
            "v_pk_fma_f32 v[114:115], " EP ", v[102:103], v[98:99] op_sel_hi:[0,1,1]\n\t" \
            "ds_read_b32 " IX ", v89 offset:" O1 "\n\t" \
            "ds_read_b32 " E1 ", v89 offset:" O2 "\n\t" \
            "s_waitcnt vmcnt(3)\n\t" \
            "v_dot2c_f32_bf16 v112, %[slo], " DS0 "\n\t" \
            "v_dot2c_f32_bf16 v113, %[slo], " DS1 "\n\t" \
            "v_dot2c_f32_bf16 v114, %[slo], " DS2 "\n\t" \
            "v_dot2c_f32_bf16 v115, %[slo], " DS3 "\n\t" \
            "v_exp_f32 v112, v112\n\t" \
            "v_exp_f32 v113, v113\n\t" \
            "v_exp_f32 v114, v114\n\t" \
            "v_exp_f32 v115, v115\n\t" \
            "v_pk_add_f32 v[112:113], v[112:113], 1.0 op_sel_hi:[1,0]\n\t" \
            "v_pk_add_f32 v[114:115], v[114:115], 1.0 op_sel_hi:[1,0]\n\t" \
            "v_rcp_f32 v112, v112\n\t" \
            "v_rcp_f32 v113, v113\n\t" \
            "v_rcp_f32 v114, v114\n\t" \
            "v_rcp_f32 v115, v115\n\t" \
            "s_sub_u32 %[cnt], %[cnt], 1\n\t" \
            "v_pk_add_f32 v[108:109], v[108:109], v[112:113]\n\t" \
            "v_pk_fma_f32 v[104:105], v[112:113], " DSL ", v[104:105]\n\t" \
            "v_pk_add_f32 v[110:111], v[110:111], v[114:115]\n\t" \
            "v_pk_fma_f32 v[106:107], v[114:115], " DSH ", v[106:107]\n\t" \
            "s_cmp_eq_u32 %[cnt], 0\n\t"
#define GIX(x) x
#define PRIO_ON 1
#define PRIO_INIT "s_setprio 3\n\t"
#define PRIO_STEP \
            "s_add_u32 %[tc], %[tc], 1\n\t" \
            "s_cmp_lg_u32 %[tc], 1\n\t s_cbranch_scc1 L_p1%=\n\t s_setprio 2\n\t L_p1%=:\n\t" \
            "s_cmp_lg_u32 %[tc], 2\n\t s_cbranch_scc1 L_p2%=\n\t s_setprio 1\n\t L_p2%=:\n\t" \
            "s_cmp_lg_u32 %[tc], 3\n\t s_cbranch_scc1 L_p3%=\n\t s_setprio 0\n\t L_p3%=:\n\t"
        asm volatile(
            PRIO_INIT
            "s_mov_b32 %[tc], 0\n\t"
            "s_lshl_b32 %[t], %[task], 5\n\t"
            "v_add_u32 v127, %[t], v91\n\t"
            "ds_read_b64 v[122:123], v127\n\t"
            "s_mov_b64 exec, 1\n\t"
            "ds_add_rtn_u32 v125, v93, v95\n\t"
            "s_mov_b64 exec, -1\n\t"
            "L_task%=:\n\t"
            "s_waitcnt lgkmcnt(0)\n\t"
            "v_readfirstlane_b32 %[nxt], v125\n\t"
            "v_readfirstlane_b32 %[cnt], v123\n\t"
            "v_mov_b32 v89, v122\n\t"
            "v_and_b32 v127, 0xffffff00, v123\n\t"
            "ds_read_b32 v80, v89\n\t"
            "ds_read_b32 v82, v89 offset:4\n\t"
            "ds_read_b32 v84, v89 offset:8\n\t"
            "ds_read_b32 v86, v89 offset:12\n\t"
            "ds_read_b32 v88, v89 offset:%c[le0]\n\t"
            "ds_read_b32 v90, v89 offset:%c[le1]\n\t"
            "ds_read_b32 v92, v89 offset:%c[le2]\n\t"
            "ds_read_b32 v94, v89 offset:%c[le3]\n\t"
            "v_add_u32 v112, v127, v81\n\t"
            "v_lshl_add_u32 v124, v127, 1, v126\n\t"
            "v_lshrrev_b32 v113, 1, v112\n\t"
            "ds_read_b128 v[116:119], v112 offset:%c[au]\n\t"
            "ds_read_b64 v[120:121], v113\n\t"
            "s_and_b32 %[cnt], %[cnt], 0xff\n\t"
            "s_lshl_b32 %[t], %[nxt], 5\n\t"
            "v_add_u32 v127, %[t], v91\n\t"
            "ds_read_b64 v[122:123], v127\n\t"
            "s_mov_b64 exec, 1\n\t"
            "ds_add_rtn_u32 v125, v93, v95\n\t"
            "s_mov_b64 exec, -1\n\t"
            "s_waitcnt lgkmcnt(9)\n\t"
            "buffer_load_dwordx4 v[64:67], v[80:81], %[bv], 0 idxen offen\n\t"
            "buffer_load_dwordx4 v[68:71], v[82:83], %[bv], 0 idxen offen\n\t"
            "buffer_load_dwordx4 v[72:75], v[84:85], %[bv], 0 idxen offen\n\t"
            "v_mov_b64 v[104:105], 0\n\t"
            "v_mov_b64 v[106:107], 0\n\t"
            "v_mov_b64 v[108:109], 0\n\t"
            "v_mov_b64 v[110:111], 0\n\t"
            "s_waitcnt lgkmcnt(2)\n\t"
            "v_lshlrev_b32 v96, 16, v116\n\t"
            "v_lshlrev_b32 v97, 16, v117\n\t"
            "v_lshlrev_b32 v98, 16, v118\n\t"
            "v_lshlrev_b32 v99, 16, v119\n\t"
            "s_cmp_eq_u32 %[cnt], 0\n\t"
            "s_cbranch_scc1 L_fin%=\n\t"
            "L_loop%=:\n\t"
            STAGE("v64", "v65", "v66", "v67", "v[64:65]", "v[66:67]", "v80", "v[88:89]", "v88", "v[76:79]", "v[86:87]", "16", "%c[le4]")
            "s_cbranch_scc1 L_fin%=\n\t"
            STAGE("v68", "v69", "v70", "v71", "v[68:69]", "v[70:71]", "v82", "v[90:91]", "v90", "v[64:67]", "v[80:81]", "20", "%c[le5]")
            "s_cbranch_scc1 L_fin%=\n\t"
            STAGE("v72", "v73", "v74", "v75", "v[72:73]", "v[74:75]", "v84", "v[92:93]", "v92", "v[68:71]", "v[82:83]", "24", "%c[le6]")
            "s_cbranch_scc1 L_fin%=\n\t"
            STAGE("v76", "v77", "v78", "v79", "v[76:77]", "v[78:79]", "v86", "v[94:95]", "v94", "v[72:75]", "v[84:85]", "28", "%c[le7]")
            "v_add_u32 v89, 16, v89\n\t"
            "s_cbranch_scc0 L_loop%=\n\t"
            "L_fin%=:\n\t"
            "v_pk_add_f32 v[108:109], v[108:109], %[eps]\n\t"
            "v_pk_add_f32 v[110:111], v[110:111], %[eps]\n\t"
            "v_rcp_f32 v108, v108\n\t"
            "v_rcp_f32 v109, v109\n\t"
            "v_rcp_f32 v110, v110\n\t"
            "v_rcp_f32 v111, v111\n\t"
            "v_pk_mul_f32 v[104:105], v[104:105], v[108:109]\n\t"
            "v_pk_mul_f32 v[106:107], v[106:107], v[110:111]\n\t"
            "v_dot2c_f32_bf16 v104, %[shi], v116\n\t"
            "v_dot2c_f32_bf16 v105, %[shi], v117\n\t"
            "v_dot2c_f32_bf16 v106, %[shi], v118\n\t"
            "v_dot2c_f32_bf16 v107, %[shi], v119\n\t"
            "v_maximum3_f32 v104, v104, 0, 0\n\t"
            "v_maximum3_f32 v105, v105, 0, 0\n\t"
            "v_maximum3_f32 v106, v106, 0, 0\n\t"
            "v_maximum3_f32 v107, v107, 0, 0\n\t"
            "v_dot2c_f32_bf16 v104, %[slo], v120\n\t"
            "v_dot2c_f32_bf16 v105, %[shi], v120\n\t"
            "v_dot2c_f32_bf16 v106, %[slo], v121\n\t"
            "v_dot2c_f32_bf16 v107, %[shi], v121\n\t"
            "s_nop 1\n\t"
            "v_cvt_pk_bf16_f32 v112, v104, v105\n\t"
            "v_cvt_pk_bf16_f32 v113, v106, v107\n\t"
            "buffer_store_dwordx2 v[112:113], v124, %[o], 0 offen\n\t"
            PRIO_STEP
            "s_cmp_lt_u32 %[nxt], %[ntask]\n\t"
            "s_cbranch_scc1 L_task%=\n\t"
            "s_waitcnt vmcnt(0) lgkmcnt(0)"
            : [cnt] "=&s"(s_cnt), [nxt] "=&s"(s_nxt), [t] "=&s"(s_t), [tc] "=&s"(s_tc)
            : "{v81}"(joff), "{v83}"(joff), "{v85}"(joff), "{v87}"(joff), "{v91}"(g8), "{v93}"(ctr), "{v95}"(one), "{v126}"(j8),
              "{v[100:101]}"(w01), "{v[102:103]}"(w23),
              [task] "s"(w), [ntask] "s"(ntask), [bv] "s"(srdBV), [o] "s"(srdO), [slo] "s"(0x00003F80u), [shi] "s"(0x3F800000u), [eps] "s"(eps2),
              [au] "i"(K2_AU), [le0] "i"(K2_LSZ), [le1] "i"(K2_LSZ + 4), [le2] "i"(K2_LSZ + 8), [le3] "i"(K2_LSZ + 12),
              [le4] "i"(K2_LSZ + 16), [le5] "i"(K2_LSZ + 20), [le6] "i"(K2_LSZ + 24), [le7] "i"(K2_LSZ + 28)
            : "v64", "v65", "v66", "v67", "v68", "v69", "v70", "v71", "v72", "v73", "v74", "v75", "v76", "v77", "v78", "v79",
              "v80", "v82", "v84", "v86", "v88", "v89", "v90", "v92", "v94", "v96", "v97", "v98", "v99",
              "v104", "v105", "v106", "v107", "v108", "v109", "v110", "v111", "v112", "v113", "v114", "v115",
              "v116", "v117", "v118", "v119", "v120", "v121", "v122", "v123", "v124", "v125", "v127", "scc", "memory");
#undef STAGE
    }
    STAMP(10);
    STAMPC(15);
    SPAN1(1);
}

extern "C" void kernel_launch(void* const* d_in, const int* in_sizes, int n_in,
                              void* d_out, int out_size, void* d_ws, size_t ws_size,
                              hipStream_t stream) {
    const u16* x = (const u16*)d_in[0];
    const u16* ea = (const u16*)d_in[1];
    const int* src = (const int*)d_in[2];
    const int* dst = (const int*)d_in[3];
    const u16* wa = (const u16*)d_in[4];
    const u16* wb = (const u16*)d_in[5];
    const u16* wc = (const u16*)d_in[6];
    const u16* wu = (const u16*)d_in[7];
    const u16* wv = (const u16*)d_in[8];
    (void)hipFuncSetAttribute((const void*)k1_gemm_bin, hipFuncAttributeMaxDynamicSharedMemorySize, K1_LDS);
    k1_gemm_bin<<<256, 512, K1_LDS, stream>>>(x, wa, wb, wu, wv, src, dst, ea, (char*)d_ws);
    (void)hipFuncSetAttribute((const void*)k2_edges, hipFuncAttributeMaxDynamicSharedMemorySize, K2_LDS);
    k2_edges<<<256, 1024, K2_LDS, stream>>>(x, wc, src, dst, ea, (const char*)d_ws, (u16*)d_out);
}
```

```cpp
#include <hip/hip_runtime.h>
#include <hip/hip_bf16.h>
#include <stdint.h>

typedef unsigned short u16;
typedef unsigned int u32;
typedef u32 u32x2 __attribute__((ext_vector_type(2)));
typedef u32 u32x4 __attribute__((ext_vector_type(4)));
typedef float f32x2 __attribute__((ext_vector_type(2)));
typedef float f32x4 __attribute__((ext_vector_type(4)));
typedef float f32x16 __attribute__((ext_vector_type(16)));
typedef short s16x4 __attribute__((ext_vector_type(4)));
typedef short s16x8 __attribute__((ext_vector_type(8)));
typedef __bf16 bf16x8 __attribute__((ext_vector_type(8)));
typedef __bf16 bf16x4 __attribute__((ext_vector_type(4)));
typedef __bf16 bf16x2 __attribute__((ext_vector_type(2)));

constexpr int NN = 10000;
constexpr int NE = 160000;
constexpr int DD = 256;
constexpr int NP = 10048;
constexpr int RG = 320;
constexpr int NBK = 64;
constexpr int NB = 157;
constexpr int NCH = 64;
constexpr int CHE = 2500;
constexpr int CAP = 80;
constexpr int CAPN = 44;
constexpr int LST = 45;
constexpr float NLOG2E = -1.4426950408889634f;
constexpr u32 NEUTRAL_REC = (u32)NN;
constexpr u32 NEUTRAL_BV = 0x00007F00u;

constexpr size_t OFF_BV = 0;
constexpr size_t OFF_AU = OFF_BV + (size_t)4 * NP * 256;
constexpr size_t OFF_R4 = OFF_AU + (size_t)4 * NP * 256;
constexpr size_t OFF_R2 = OFF_R4 + (size_t)NBK * NCH * CAP * 4;
constexpr size_t OFF_CNT = OFF_R2 + (size_t)NBK * NCH * CAP * 2;

#define LDS_AS __attribute__((address_space(3)))
#define GLB_AS __attribute__((address_space(1)))

__device__ __forceinline__ void glds16(const void* g, void* l) {
    __builtin_amdgcn_global_load_lds((const GLB_AS u32*)g, (LDS_AS u32*)l, 16, 0, 0);
}
__device__ __forceinline__ void glds16a(const void* sbase, u32 voff, u32 lds) {
    u32 keep;
    asm volatile("s_mov_b32 %0, m0\n\ts_mov_b32 m0, %3\n\ts_nop 0\n\tglobal_load_lds_dwordx4 %1, %2\n\ts_mov_b32 m0, %0" : "=&s"(keep) : "v"(voff), "s"(sbase), "s"(lds) : "memory");
}
__device__ __forceinline__ u32 cvt_pk_bf16(float lo, float hi) {
    const f32x2 v = {lo, hi}; return __builtin_bit_cast(u32, __builtin_convertvector(v, bf16x2));
}
#define WAIT_V(n) asm volatile("s_waitcnt vmcnt(" #n ")" ::: "memory")
#define WAIT_L(n) asm volatile("s_waitcnt lgkmcnt(" #n ")" ::: "memory")
#define BAR() __builtin_amdgcn_s_barrier()
#define EXIT2_AT 99
#ifndef EXIT_AT
#define EXIT_AT 99
#endif
#define PT_STRIDE (512u * 1024u)
#define PT_STRIDE2 (416u * 1024u)
#define STORE_AUX 16
#define SPAN0()
#define SPAN1(K)
#define STAMP(i)
#define STAMPC(i)
#define TICK(v)

constexpr int K1_R0 = 0, K1_R1 = 65536, K1_SCR = 131072;
constexpr int K1_ST4 = K1_SCR, K1_ST2 = K1_ST4 + NBK * CAP * 4, K1_BC = K1_ST2 + NBK * CAP * 2;
constexpr int K1_LDS = K1_SCR + 32768;
static_assert(K1_BC + NBK * 4 <= K1_LDS, "lds");

__global__ __launch_bounds__(512, 2) void k1_gemm_bin(
    const u16* __restrict__ x, const u16* __restrict__ wa, const u16* __restrict__ wb, const u16* __restrict__ wu, const u16* __restrict__ wv,
    const int* __restrict__ src, const int* __restrict__ dst, const u16* __restrict__ ea, char* __restrict__ ws, const char* __restrict__ outp, const char* __restrict__ wcp) {
    extern __shared__ __attribute__((aligned(16))) char smem[];
    const int tid = threadIdx.x, lane = tid & 63;
    const int w = __builtin_amdgcn_readfirstlane(tid >> 6);
    const int b = blockIdx.x;
    const int type = (b >> 5) & 1;
    const int ct2 = (b >> 3) & 3;
    const int rg = (b & 7) * 4 + (b >> 6);
    const int row0 = rg * RG;
    const int chunk = (b & 31) | ((b >> 6) << 5) ;
    const bool binner = type == 1 && b < 128;
    constexpr int KOFF = 0;
    SPAN0();
#if EXIT_AT == 0
    if (ws) { SPAN1(0); return; }
#endif
    STAMP(0);
    int bd[5], bs[5]; u32 be[5];
    if (binner) {
#pragma unroll
        for (int i = 0; i < 5; ++i) {
            const int k = tid + 512 * i;
            const int e = chunk * CHE + (k < CHE ? k : 0);
            bd[i] = dst[e]; bs[i] = src[e]; be[i] = ea[e];
        }
        if (tid < NBK) *(u32*)(smem + K1_BC + tid * 4) = 0;
    }
    {
        const int m = w >> 2;
        const u16* W = type == 0 ? (m == 0 ? wb : wv) : (m == 0 ? wa : wu);
        const int kb0 = (w & 3) * 64, kr = lane >> 3;
        const u16* g = W + (size_t)(kb0 + kr) * DD + ct2 * 64 + (((lane & 7) ^ ((kr & 2) << 1)) << 3);
#pragma unroll
        for (int i = 0; i < 8; ++i) glds16(g + (size_t)i * 8 * DD, smem + K1_R0 + m * 32768 + (kb0 / 8 + i) * 1024);
    }
    const int xr = lane >> 5, xp = lane & 31;
    auto stage_x = [&](int tile, int q, int ldsbase) {
        const int r = q * 2 + xr;
        int grow = row0 + tile * 32 + r; grow = grow < NN ? grow : NN - 1;
        glds16((const char*)x + (u32)(grow * 512 + ((xp ^ (r & 15)) << 4)), smem + ldsbase + q * 1024);
    };
#pragma unroll
    for (int i = 0; i < 8; ++i) { const int q = w * 8 + i; stage_x(q >> 4, q & 15, K1_R1 + (q >> 4) * 16384); }
    if (w == 7) {
        const char* p = lane < 48 ? ws + (size_t)lane * PT_STRIDE : (lane < 60 ? outp + (size_t)(lane - 48) * PT_STRIDE2 : wcp);
        u32 keep;
        asm volatile("s_mov_b32 %0, m0\n\ts_mov_b32 m0, %2\n\ts_nop 0\n\tglobal_load_lds_dword %1, off\n\ts_mov_b32 m0, %0" : "=&s"(keep) : "v"(p), "s"(K1_LDS - 256) : "memory");
        WAIT_V(9);
    } else WAIT_V(8);
    STAMP(1);
    WAIT_L(0);
    BAR();
    STAMP(2);
#if EXIT_AT == 1
    if (ws) { SPAN1(0); return; }
#endif
    const int ch = w >> 2;
    bf16x8 wf0[16], wf1[16];
    {
        const int h = lane >> 5, blk = (lane >> 4) & 1, q = (lane & 15) >> 2, p = lane & 3;
        const u32 base = K1_R0 + (8 * h + q) * 128 + (((ch * 4 + blk * 2 + (p >> 1)) ^ ((q & 2) << 1)) << 4) + (p & 1) * 8;
#define TRRD(S) { u32x2 a0, a1, b0, b1; \
            asm volatile("ds_read_b64_tr_b16 %0, %4 offset:%c5\n\tds_read_b64_tr_b16 %1, %4 offset:%c6\n\t" \
                         "ds_read_b64_tr_b16 %2, %4 offset:%c7\n\tds_read_b64_tr_b16 %3, %4 offset:%c8" \
                         : "=&v"(a0), "=&v"(a1), "=&v"(b0), "=&v"(b1) \
                         : "v"(base), "i"((S) * 2048), "i"((S) * 2048 + 512), "i"(32768 + (S) * 2048), "i"(32768 + (S) * 2048 + 512) : "memory"); \
            wf0[S] = __builtin_bit_cast(bf16x8, __builtin_shufflevector(a0, a1, 0, 1, 2, 3)); \
            wf1[S] = __builtin_bit_cast(bf16x8, __builtin_shufflevector(b0, b1, 0, 1, 2, 3)); }
        TRRD(0) TRRD(1) TRRD(2) TRRD(3) TRRD(4) TRRD(5) TRRD(6) TRRD(7) TRRD(8) TRRD(9) TRRD(10) TRRD(11) TRRD(12) TRRD(13) TRRD(14) TRRD(15)
#undef TRRD
    }
    if (binner) {
#pragma unroll
        for (int i = 0; i < 5; ++i) {
            const int k = tid + 512 * i;
            if (k < CHE) {
                const int bk = (int)(((u32)bd[i] * 53432u) >> 23);
                const int dl = bd[i] - bk * NB;
                const u32 pos = atomicAdd((u32*)(smem + K1_BC + bk * 4), 1u);
                if (pos < CAP) {
                    *(u32*)(smem + K1_ST4 + (bk * CAP + pos) * 4) = (u32)bs[i] | (be[i] << 16);
                    *(u16*)(smem + K1_ST2 + (bk * CAP + pos) * 2) = (u16)dl;
                }
            }
        }
    }
    STAMP(3);
    WAIT_V(0); WAIT_L(0);
    __builtin_amdgcn_sched_barrier(0);
    BAR();
    STAMP(4);
#if EXIT_AT == 2
    if (ws) { asm volatile("" :: "v"(wf0[0]), "v"(wf1[15])); SPAN1(0); return; }
#endif
#pragma unroll
    for (int i = 0; i < 8; ++i) { const int q = w * 8 + i; stage_x(4 + (q >> 4), q & 15, K1_R0 + (q >> 4) * 16384); }
    if (binner) {
        u32* R4 = (u32*)(ws + OFF_R4); u16* R2 = (u16*)(ws + OFF_R2); u32* CNT = (u32*)(ws + OFF_CNT);
        u32x4 t4[3]; u32x2 t2[3];
#pragma unroll
        for (int i = 0; i < 3; ++i) { const int q = tid + 512 * i; const int qq = q < NBK * (CAP / 4) ? q : 0;
            t4[i] = *(const u32x4*)(smem + K1_ST4 + qq * 16); t2[i] = *(const u32x2*)(smem + K1_ST2 + qq * 8); }
        const u32 cv = *(const u32*)(smem + K1_BC + (tid & 63) * 4);
#pragma unroll
        for (int i = 0; i < 3; ++i) { const int q = tid + 512 * i;
            if (q < NBK * (CAP / 4)) { const int bk = q / (CAP / 4), o = q - bk * (CAP / 4);
                *(u32x4*)(R4 + ((size_t)bk * NCH + chunk) * CAP + o * 4) = t4[i];
                *(u32x2*)(R2 + ((size_t)bk * NCH + chunk) * CAP + o * 4) = t2[i]; } }
        if (tid < NBK) CNT[tid * NCH + chunk] = cv;
        WAIT_L(0);
        BAR();
    }
    if (rg == 0 && type == 0 && w == 0) ((u32*)(ws + OFF_BV))[((size_t)ct2 * NP + NN) * 64 + lane] = NEUTRAL_BV;

    const int r = lane & 31, h = lane >> 5;
    const int abase = r * 512 + ((((r & 14) | ((h ^ r) & 1))) << 4);
    char* const scr = smem + K1_SCR + w * 4096;
    const __amdgpu_buffer_rsrc_t trs = __builtin_amdgcn_make_buffer_rsrc(ws + (type ? OFF_AU : OFF_BV) + (size_t)ct2 * NP * 256, 0, NN * 256, 0x00020000);
    const u32 svo = (lane >> 3) * 256 + ch * 128 + (lane & 7) * 16;

    const int s4 = w & 3;
    if (ch == 0) __builtin_amdgcn_s_setprio(1);
    STAMP(5);
#pragma unroll 1
    for (int st = 0; st < (EXIT_AT == 3 ? 1 : (EXIT_AT == 4 ? 2 : 3)); ++st) {
        int tile, ldsbase;
        if (st == 0) { tile = s4; ldsbase = K1_R1 + s4 * 16384; }
        else if (st == 1) { tile = 4 + s4; ldsbase = K1_R0 + s4 * 16384; }
        else { const int t = (w < 2) ? w : (w >= 6 ? w - 6 : 99); tile = 8 + t; ldsbase = K1_R1 + (t & 1) * 16384; }
        const int trow = row0 + tile * 32;
        const bool active = tile < 10 && trow < NN;
        unsigned long long tkA = 0, tkB = 0, tkC = 0, tkD = 0;
        if (active) {
            TICK(tkA);
            bf16x8 a[16];
#pragma unroll
            for (int s = 0; s < 8; ++s) a[s] = *(const bf16x8*)(smem + ldsbase + (abase ^ (s << 5)));
            __builtin_amdgcn_sched_barrier(0);
            f32x16 acc0, acc1;
#pragma unroll
            for (int i = 0; i < 16; ++i) { acc0[i] = 0.f; acc1[i] = 0.f; }
#pragma unroll
            for (int s = 0; s < 8; ++s) {
                a[s + 8] = *(const bf16x8*)(smem + ldsbase + (abase ^ (s << 5)) + 256);
                acc0 = __builtin_amdgcn_mfma_f32_32x32x16_bf16(a[s], wf0[s], acc0, 0, 0, 0);
                acc1 = __builtin_amdgcn_mfma_f32_32x32x16_bf16(a[s], wf1[s], acc1, 0, 0, 0);
                __builtin_amdgcn_sched_group_barrier(0x100, 1, 0);
                __builtin_amdgcn_sched_group_barrier(0x8, 2, 0);
            }
            __builtin_amdgcn_sched_barrier(0);
#pragma unroll
            for (int s = 8; s < 16; ++s) {
                acc0 = __builtin_amdgcn_mfma_f32_32x32x16_bf16(a[s], wf0[s], acc0, 0, 0, 0);
                acc1 = __builtin_amdgcn_mfma_f32_32x32x16_bf16(a[s], wf1[s], acc1, 0, 0, 0);
            }
            TICK(tkB);
            char* const sw = scr + h * 512 + r * 4;
            if (type == 0) {
#pragma unroll
                for (int i = 0; i < 16; i += 2) {
                    const u32 bb = cvt_pk_bf16(acc0[i] * NLOG2E, acc0[i + 1] * NLOG2E);
#pragma unroll
                    for (int j = 0; j < 2; ++j) {
                        const u32 vb = __float_as_uint(acc1[i + j]);
                        const u32 bl = j ? (bb >> 16) : (bb & 0xFFFFu);
                        const u32 u = __builtin_elementwise_sub_sat((vb & 0x7FFFFFFFu) + 0x8000u, bl);
                        *(u32*)(sw + (((i + j) & 3) + 8 * ((i + j) >> 2)) * 128) = (vb & 0x80000000u) | (u & 0x7FFF0000u) | bl;
                    }
                }
            } else {
#pragma unroll
                for (int i = 0; i < 16; ++i) *(u32*)(sw + ((i & 3) + 8 * (i >> 2)) * 128) = cvt_pk_bf16(acc0[i] * NLOG2E, acc1[i]);
            }
            TICK(tkC);
#pragma unroll
            for (int i = 0; i < 4; ++i) {
                const u32x4 v = *(const u32x4*)(scr + i * 1024 + lane * 16);
                __builtin_amdgcn_raw_buffer_store_b128(v, trs, svo + (u32)(trow + i * 8) * 256, 0, STORE_AUX);
            }
        }
        TICK(tkD);
        if (st == 0) STAMP(6); else if (st == 1) STAMP(8); else STAMP(10);
        if (st < 2) {
            if (active) WAIT_V(4); else WAIT_V(0);
            BAR();
            if (st == 0) {
#pragma unroll
                for (int i = 0; i < 4; ++i) { const int q = w * 4 + i; stage_x(8 + (q >> 4), q & 15, K1_R1 + (q >> 4) * 16384); }
            }
        }
    }
    SPAN1(0);
}

constexpr int LSTB = LST * 4;
constexpr int K2_X = 0;
constexpr int K2_AU = K2_X + 20480;
constexpr int K2_LIDX = K2_AU + 40960;
constexpr int K2_LSZ = (NB * LSTB + 32 + 256 + 15) / 16 * 16;
constexpr int K2_LE = K2_LIDX + K2_LSZ;
constexpr int K2_CNT = K2_LE + K2_LSZ;
constexpr int K2_HIST = K2_CNT + 160 * 4;
constexpr int K2_ORD = K2_HIST + 128 * 4;
constexpr int K2_MISC = K2_ORD + 80 * 32;
constexpr int K2_LDS = K2_MISC + 16;
constexpr int K2_TRASH = NB * LSTB + 32;
static_assert(K2_TRASH + 256 <= K2_LSZ, "trash");
static_assert(K2_LSZ < 65536 - 64, "ds offset");

__device__ __forceinline__ float dot2sel(u32 d, u32 sel, float c) {
    return __builtin_amdgcn_fdot2_f32_bf16(__builtin_bit_cast(bf16x2, d), __builtin_bit_cast(bf16x2, sel), c, false);
}

struct Acc { f32x2 n01, n23, d01, d23; };

__device__ __forceinline__ void edge_step(Acc& a, const u32x4 d, const u32 rec, const f32x2 a01, const f32x2 a23, const f32x2 w01, const f32x2 w23, const u32 sel) {
    const float e = __uint_as_float(rec & 0xFFFF0000u);
    const f32x2 ee = {e, e};
    const f32x2 t01 = __builtin_elementwise_fma(ee, w01, a01);
    const f32x2 t23 = __builtin_elementwise_fma(ee, w23, a23);
    f32x2 E01, E23;
    E01.x = __builtin_amdgcn_exp2f(dot2sel(d.x, sel, t01.x));
    E01.y = __builtin_amdgcn_exp2f(dot2sel(d.y, sel, t01.y));
    E23.x = __builtin_amdgcn_exp2f(dot2sel(d.z, sel, t23.x));
    E23.y = __builtin_amdgcn_exp2f(dot2sel(d.w, sel, t23.y));
    const f32x2 one = {1.f, 1.f};
    E01 += one; E23 += one;
    f32x2 s01, s23;
    s01.x = __builtin_amdgcn_rcpf(E01.x); s01.y = __builtin_amdgcn_rcpf(E01.y);
    s23.x = __builtin_amdgcn_rcpf(E23.x); s23.y = __builtin_amdgcn_rcpf(E23.y);
    a.d01 += s01; a.d23 += s23;
    const f32x2 v01 = {__uint_as_float(d.x), __uint_as_float(d.y)};
    const f32x2 v23 = {__uint_as_float(d.z), __uint_as_float(d.w)};
    a.n01 = __builtin_elementwise_fma(s01, v01, a.n01);
    a.n23 = __builtin_elementwise_fma(s23, v23, a.n23);
}

__device__ __forceinline__ void node_finish(const Acc& a, const u32x4 au, const u32x2 xv, u16* outp) {
    const float num[4] = {a.n01.x, a.n01.y, a.n23.x, a.n23.y};
    const float den[4] = {a.d01.x, a.d01.y, a.d23.x, a.d23.y};
    const u32 aus[4] = {au.x, au.y, au.z, au.w};
    const float xs[4] = {__uint_as_float(xv.x << 16), __uint_as_float(xv.x & 0xFFFF0000u), __uint_as_float(xv.y << 16), __uint_as_float(xv.y & 0xFFFF0000u)};
    float o[4];
#pragma unroll
    for (int c = 0; c < 4; ++c) {
        const float z = __uint_as_float(aus[c] & 0xFFFF0000u) + num[c] * __builtin_amdgcn_rcpf(den[c] + 1e-6f);
        const float rl = z > 0.f ? z : 0.f * z;
        o[c] = xs[c] + rl;
    }
    u32x2 ov; ov.x = cvt_pk_bf16(o[0], o[1]); ov.y = cvt_pk_bf16(o[2], o[3]);
    *(u32x2*)outp = ov;
}

template <int CTRL, int RM> __device__ __forceinline__ u32 dpp_add(u32 v) {
    return v + (u32)__builtin_amdgcn_update_dpp(0, (int)v, CTRL, RM, 0xf, false);
}

__global__ __launch_bounds__(1024, 4) void k2_edges(
    const u16* __restrict__ x, const u16* __restrict__ wc, const int* __restrict__ src, const int* __restrict__ dst, const u16* __restrict__ ea,
    const char* __restrict__ ws, u16* __restrict__ out) {
    extern __shared__ __attribute__((aligned(16))) char smem[];
    const int tid = threadIdx.x, lane = tid & 63;
    const int w = __builtin_amdgcn_readfirstlane(tid >> 6);
    const int b = blockIdx.x;
    const int slice = (b & 7) >> 1;
    const int bucket = (b >> 3) * 2 + (b & 1);
    const int node0 = bucket * NB;
    const int nvalid = (NN - node0) < NB ? (NN - node0) : NB;
    u32* const cnt = (u32*)(smem + K2_CNT);
    u32* const hist = (u32*)(smem + K2_HIST);
    u32* const misc = (u32*)(smem + K2_MISC);
    constexpr int KOFF = 256 * 16;
    SPAN0();
    STAMP(0);

    const u32* R4 = (const u32*)(ws + OFF_R4) + (size_t)bucket * NCH * CAP;
    const u16* R2 = (const u16*)(ws + OFF_R2) + (size_t)bucket * NCH * CAP;
    const u32* CNT = (const u32*)(ws + OFF_CNT) + bucket * NCH;
    const int ch0 = tid / (CAP / 4), ch1 = (tid + 1024) / (CAP / 4);
    const u32x4 r40 = *(const u32x4*)(R4 + tid * 4);
    const u32x2 r20 = *(const u32x2*)(R2 + tid * 4);
    const u32 c0 = CNT[ch0];
    u32x4 r41 = {0, 0, 0, 0}; u32x2 r21 = {0, 0}; u32 c1 = 0;
    if (w < 4) { r41 = *(const u32x4*)(R4 + (tid + 1024) * 4); r21 = *(const u32x2*)(R2 + (tid + 1024) * 4); c1 = CNT[ch1]; }
    const char* const bvb = ws + OFF_BV + (size_t)slice * NP * 256;
    const char* const aub = ws + OFF_AU + (size_t)slice * NP * 256;
    const int g = lane >> 4, j = lane & 15;
    const u32x2 wcv = *(const u32x2*)(wc + slice * 64 + j * 4);
    for (int i = tid; i < K2_LSZ / 16; i += 1024) {
        *(u32x4*)(smem + K2_LIDX + i * 16) = (u32x4){NEUTRAL_REC, NEUTRAL_REC, NEUTRAL_REC, NEUTRAL_REC};
        *(u32x4*)(smem + K2_LE + i * 16) = (u32x4){0, 0, 0, 0};
    }
    if (tid < 160 + 64) cnt[tid] = 0;
    if (tid == 0) { misc[0] = 16; misc[1] = 0; }
    f32x2 w01 = {__uint_as_float(wcv.x << 16) * NLOG2E, __uint_as_float(wcv.x & 0xFFFF0000u) * NLOG2E};
    f32x2 w23 = {__uint_as_float(wcv.y << 16) * NLOG2E, __uint_as_float(wcv.y & 0xFFFF0000u) * NLOG2E};
    asm volatile("" : "+v"(w01), "+v"(w23));
    STAMP(1);
    WAIT_V(0); WAIT_L(0); BAR();
    {
        const char* ga = aub + (size_t)node0 * 256;
#pragma unroll
        for (int i = 0; i < 3; ++i) { const int q = w + 16 * i; if (q < 40) glds16a(ga, q * 1024 + lane * 16, K2_AU + q * 1024); }
        const char* gx = (const char*)x + slice * 128;
#pragma unroll
        for (int i = 0; i < 2; ++i) {
            const int q = w + 16 * i;
            if (q < 20) { int row = node0 + q * 8 + (lane >> 3); row = row < NN ? row : NN - 1;
                glds16a(gx, row * 512 + (lane & 7) * 16, K2_X + q * 1024); }
        }
    }
    STAMP(2);
    {
        const int p0 = (tid - ch0 * (CAP / 4)) * 4;
        const u32 rec0[4] = {r40.x, r40.y, r40.z, r40.w};
        const u32 dl0[4] = {r20.x & 0xFFFF, r20.x >> 16, r20.y & 0xFFFF, r20.y >> 16};
        u32 slot[4];
        bool ovf = c0 > CAP;
#pragma unroll
        for (int k = 0; k < 4; ++k) slot[k] = atomicAdd(&cnt[p0 + k < c0 ? dl0[k] : 224 + lane], 1u);
        if (w < 4) {
            const int p1 = (tid + 1024 - ch1 * (CAP / 4)) * 4;
            const u32 rec1[4] = {r41.x, r41.y, r41.z, r41.w};
            const u32 dl1[4] = {r21.x & 0xFFFF, r21.x >> 16, r21.y & 0xFFFF, r21.y >> 16};
            u32 slot1[4];
            ovf |= c1 > CAP;
#pragma unroll
            for (int k = 0; k < 4; ++k) slot1[k] = atomicAdd(&cnt[p1 + k < c1 ? dl1[k] : 224 + lane], 1u);
#pragma unroll
            for (int k = 0; k < 4; ++k) {
                const bool v = p1 + k < c1, ok = v && slot1[k] < CAPN;
                ovf |= v && !ok;
                const u32 o = ok ? dl1[k] * LSTB + slot1[k] * 4 : K2_TRASH + lane * 4;
                *(u32*)(smem + K2_LIDX + o) = rec1[k] & 0xFFFFu;
                *(u32*)(smem + K2_LE + o) = rec1[k] & 0xFFFF0000u;
            }
        }
#pragma unroll
        for (int k = 0; k < 4; ++k) {
            const bool v = p0 + k < c0, ok = v && slot[k] < CAPN;
            ovf |= v && !ok;
            const u32 o = ok ? dl0[k] * LSTB + slot[k] * 4 : K2_TRASH + lane * 4;
            *(u32*)(smem + K2_LIDX + o) = rec0[k] & 0xFFFFu;
            *(u32*)(smem + K2_LE + o) = rec0[k] & 0xFFFF0000u;
        }
        if (ovf) misc[1] = 1;
    }
    STAMP(3);
    WAIT_L(0); BAR();
    STAMP(4);
    const bool slow = misc[1] != 0;

    const u32 joff = j * 16;

    if (slow) {
        u32 sel = 0x00003F80u;
        asm volatile("" : "+s"(sel));
        for (int task = w; task * 4 < nvalid; task += 16) {
            const int n = task * 4 + g;
            const bool nv = n < nvalid;
            const int node = node0 + (nv ? n : 0);
            const u32x4 au = *(const u32x4*)(aub + (size_t)node * 256 + joff);
            const u32x2 xv = *(const u32x2*)(x + (size_t)node * DD + slice * 64 + j * 4);
            const f32x2 a01 = {__uint_as_float(au.x << 16), __uint_as_float(au.y << 16)};
            const f32x2 a23 = {__uint_as_float(au.z << 16), __uint_as_float(au.w << 16)};
            Acc acc = {{0.f, 0.f}, {0.f, 0.f}, {0.f, 0.f}, {0.f, 0.f}};
            const int lo = node0 + task * 4;
            for (int e0 = 0; e0 < NE; e0 += 64) {
                const int d = dst[e0 + lane];
                unsigned long long m = __ballot((u32)(d - lo) < 4u);
                if (m) {
                    const u32 rc = (u32)src[e0 + lane] | ((u32)ea[e0 + lane] << 16);
                    while (m) {
                        const int l = __builtin_ctzll(m); m &= m - 1;
                        const int dd = __builtin_amdgcn_readlane(d, l);
                        const u32 rr = __builtin_amdgcn_readlane(rc, l);
                        const u32 rec = (nv && dd == node) ? rr : NEUTRAL_REC;
                        const u32x4 dv = *(const u32x4*)(bvb + (size_t)((rec & 0xFFFFu) * 256 + joff));
                        edge_step(acc, dv, rec, a01, a23, w01, w23, sel);
                    }
                }
            }
            if (nv) node_finish(acc, au, xv, out + (size_t)node * DD + slice * 64 + j * 4);
        }
        return;
    }

    u32 mydeg = 0, myrank = 0;
    if (tid < nvalid) { mydeg = cnt[tid]; myrank = atomicAdd(&hist[63 - mydeg], 1u); }
    WAIT_L(0); BAR();
    if (w < 3) {
        const u32 h0 = hist[lane];
        u32 v = h0;
        v = dpp_add<0x111, 0xf>(v); v = dpp_add<0x112, 0xf>(v); v = dpp_add<0x114, 0xf>(v); v = dpp_add<0x118, 0xf>(v);
        v = dpp_add<0x142, 0xa>(v); v = dpp_add<0x143, 0xc>(v);
        const u32 base = (u32)__builtin_amdgcn_ds_bpermute((int)((63 - mydeg) * 4), (int)(v - h0));
        if (tid < nvalid) {
            const u32 pos = base + myrank;
            const u32x2 ent = {(u32)(K2_LIDX + tid * LSTB), (u32)(tid * 256) + mydeg};
            u32x2* o = (u32x2*)(smem + K2_ORD) + pos;
            o[0] = ent;
            if (pos == (u32)nvalid - 1) { o[1] = ent; o[2] = ent; o[3] = ent; }
        }
    }
    WAIT_V(0); WAIT_L(0); BAR();
    STAMP(5);
    STAMPC(14);

    const int ntask = (nvalid + 3) >> 2;
    if (w < ntask) {
        u32x4 srdBV, srdO;
        { const size_t p = (size_t)bvb; srdBV = (u32x4){(u32)p, (u32)(p >> 32) | (256u << 16), 0xFFFFFFFFu, 0x00020000u}; }
        { const size_t ob = (size_t)node0 * 512 + slice * 128; const size_t p = (size_t)out + ob; srdO = (u32x4){(u32)p, (u32)(p >> 32), (u32)((size_t)NN * 512 - ob), 0x00020000u}; }
        u32 g8 = K2_ORD + g * 8, ctr = K2_MISC, one = 1, j8 = j * 8;
        unsigned long long eps2 = 0x358637BD358637BDull;
        u32 s_cnt, s_nxt, s_t, s_tc;
#define STAGE(DS0, DS1, DS2, DS3, DSL, DSH, IX, EP, E1, DN, IXN, O1, O2) \
            "s_waitcnt lgkmcnt(0)\n\t" \
            "buffer_load_dwordx4 " DN ", " GIX(IXN) ", %[bv], 0 idxen offen\n\t" \
            "v_pk_fma_f32 v[112:113], " EP ", v[100:101], v[96:97] op_sel_hi:[0,1,1]\n\t" \
            "v_pk_fma_f32 v[114:115], " EP ", v[102:103], v[98:99] op_sel_hi:[0,1,1]\n\t" \
            "ds_read_b32 " IX ", v89 offset:" O1 "\n\t" \
            "ds_read_b32 " E1 ", v89 offset:" O2 "\n\t" \
            "s_waitcnt vmcnt(3)\n\t" \
            "v_dot2c_f32_bf16 v112, %[slo], " DS0 "\n\t" \
            "v_dot2c_f32_bf16 v113, %[slo], " DS1 "\n\t" \
            "v_dot2c_f32_bf16 v114, %[slo], " DS2 "\n\t" \
            "v_dot2c_f32_bf16 v115, %[slo], " DS3 "\n\t" \
            "v_exp_f32 v112, v112\n\t" \
            "v_exp_f32 v113, v113\n\t" \
            "v_exp_f32 v114, v114\n\t" \
            "v_exp_f32 v115, v115\n\t" \
            "v_pk_add_f32 v[112:113], v[112:113], 1.0 op_sel_hi:[1,0]\n\t" \
            "v_pk_add_f32 v[114:115], v[114:115], 1.0 op_sel_hi:[1,0]\n\t" \
            "v_rcp_f32 v112, v112\n\t" \
            "v_rcp_f32 v113, v113\n\t" \
            "v_rcp_f32 v114, v114\n\t" \
            "v_rcp_f32 v115, v115\n\t" \
            "s_sub_u32 %[cnt], %[cnt], 1\n\t" \
            "v_pk_add_f32 v[108:109], v[108:109], v[112:113]\n\t" \
            "v_pk_fma_f32 v[104:105], v[112:113], " DSL ", v[104:105]\n\t" \
            "v_pk_add_f32 v[110:111], v[110:111], v[114:115]\n\t" \
            "v_pk_fma_f32 v[106:107], v[114:115], " DSH ", v[106:107]\n\t" \
            "s_cmp_eq_u32 %[cnt], 0\n\t"
#define GIX(x) x
#define PRIO_ON 1
#define PRIO_INIT "s_setprio 3\n\t"
#define PRIO_STEP \
            "s_add_u32 %[tc], %[tc], 1\n\t" \
            "s_cmp_lg_u32 %[tc], 1\n\t s_cbranch_scc1 L_p1%=\n\t s_setprio 2\n\t L_p1%=:\n\t" \
            "s_cmp_lg_u32 %[tc], 2\n\t s_cbranch_scc1 L_p2%=\n\t s_setprio 1\n\t L_p2%=:\n\t" \
            "s_cmp_lg_u32 %[tc], 3\n\t s_cbranch_scc1 L_p3%=\n\t s_setprio 0\n\t L_p3%=:\n\t"
        asm volatile(
            PRIO_INIT
            "s_mov_b32 %[tc], 0\n\t"
            "s_lshl_b32 %[t], %[task], 5\n\t"
            "v_add_u32 v127, %[t], v91\n\t"
            "ds_read_b64 v[122:123], v127\n\t"
            "s_mov_b64 exec, 1\n\t"
            "ds_add_rtn_u32 v125, v93, v95\n\t"
            "s_mov_b64 exec, -1\n\t"
            "L_task%=:\n\t"
            "s_waitcnt lgkmcnt(0)\n\t"
            "v_readfirstlane_b32 %[nxt], v125\n\t"
            "v_readfirstlane_b32 %[cnt], v123\n\t"
            "v_mov_b32 v89, v122\n\t"
            "v_and_b32 v127, 0xffffff00, v123\n\t"
            "ds_read_b32 v80, v89\n\t"
            "ds_read_b32 v82, v89 offset:4\n\t"
            "ds_read_b32 v84, v89 offset:8\n\t"
            "ds_read_b32 v86, v89 offset:12\n\t"
            "ds_read_b32 v88, v89 offset:%c[le0]\n\t"
            "ds_read_b32 v90, v89 offset:%c[le1]\n\t"
            "ds_read_b32 v92, v89 offset:%c[le2]\n\t"
            "ds_read_b32 v94, v89 offset:%c[le3]\n\t"
            "v_add_u32 v112, v127, v81\n\t"
            "v_lshl_add_u32 v124, v127, 1, v126\n\t"
            "v_lshrrev_b32 v113, 1, v112\n\t"
            "ds_read_b128 v[116:119], v112 offset:%c[au]\n\t"
            "ds_read_b64 v[120:121], v113\n\t"
            "s_and_b32 %[cnt], %[cnt], 0xff\n\t"
            "s_lshl_b32 %[t], %[nxt], 5\n\t"
            "v_add_u32 v127, %[t], v91\n\t"
            "ds_read_b64 v[122:123], v127\n\t"
            "s_mov_b64 exec, 1\n\t"
            "ds_add_rtn_u32 v125, v93, v95\n\t"
            "s_mov_b64 exec, -1\n\t"
            "s_waitcnt lgkmcnt(9)\n\t"
            "buffer_load_dwordx4 v[64:67], v[80:81], %[bv], 0 idxen offen\n\t"
            "buffer_load_dwordx4 v[68:71], v[82:83], %[bv], 0 idxen offen\n\t"
            "buffer_load_dwordx4 v[72:75], v[84:85], %[bv], 0 idxen offen\n\t"
            "v_mov_b64 v[104:105], 0\n\t"
            "v_mov_b64 v[106:107], 0\n\t"
            "v_mov_b64 v[108:109], 0\n\t"
            "v_mov_b64 v[110:111], 0\n\t"
            "s_waitcnt lgkmcnt(2)\n\t"
            "v_lshlrev_b32 v96, 16, v116\n\t"
            "v_lshlrev_b32 v97, 16, v117\n\t"
            "v_lshlrev_b32 v98, 16, v118\n\t"
            "v_lshlrev_b32 v99, 16, v119\n\t"
            "s_cmp_eq_u32 %[cnt], 0\n\t"
            "s_cbranch_scc1 L_fin%=\n\t"
            "L_loop%=:\n\t"
            STAGE("v64", "v65", "v66", "v67", "v[64:65]", "v[66:67]", "v80", "v[88:89]", "v88", "v[76:79]", "v[86:87]", "16", "%c[le4]")
            "s_cbranch_scc1 L_fin%=\n\t"
            STAGE("v68", "v69", "v70", "v71", "v[68:69]", "v[70:71]", "v82", "v[90:91]", "v90", "v[64:67]", "v[80:81]", "20", "%c[le5]")
            "s_cbranch_scc1 L_fin%=\n\t"
            STAGE("v72", "v73", "v74", "v75", "v[72:73]", "v[74:75]", "v84", "v[92:93]", "v92", "v[68:71]", "v[82:83]", "24", "%c[le6]")
            "s_cbranch_scc1 L_fin%=\n\t"
            STAGE("v76", "v77", "v78", "v79", "v[76:77]", "v[78:79]", "v86", "v[94:95]", "v94", "v[72:75]", "v[84:85]", "28", "%c[le7]")
            "v_add_u32 v89, 16, v89\n\t"
            "s_cbranch_scc0 L_loop%=\n\t"
            "L_fin%=:\n\t"
            "v_pk_add_f32 v[108:109], v[108:109], %[eps]\n\t"
            "v_pk_add_f32 v[110:111], v[110:111], %[eps]\n\t"
            "v_rcp_f32 v108, v108\n\t"
            "v_rcp_f32 v109, v109\n\t"
            "v_rcp_f32 v110, v110\n\t"
            "v_rcp_f32 v111, v111\n\t"
            "v_pk_mul_f32 v[104:105], v[104:105], v[108:109]\n\t"
            "v_pk_mul_f32 v[106:107], v[106:107], v[110:111]\n\t"
            "v_dot2c_f32_bf16 v104, %[shi], v116\n\t"
            "v_dot2c_f32_bf16 v105, %[shi], v117\n\t"
            "v_dot2c_f32_bf16 v106, %[shi], v118\n\t"
            "v_dot2c_f32_bf16 v107, %[shi], v119\n\t"
            "v_maximum3_f32 v104, v104, 0, 0\n\t"
            "v_maximum3_f32 v105, v105, 0, 0\n\t"
            "v_maximum3_f32 v106, v106, 0, 0\n\t"
            "v_maximum3_f32 v107, v107, 0, 0\n\t"
            "v_dot2c_f32_bf16 v104, %[slo], v120\n\t"
            "v_dot2c_f32_bf16 v105, %[shi], v120\n\t"
            "v_dot2c_f32_bf16 v106, %[slo], v121\n\t"
            "v_dot2c_f32_bf16 v107, %[shi], v121\n\t"
            "s_nop 1\n\t"
            "v_cvt_pk_bf16_f32 v112, v104, v105\n\t"
            "v_cvt_pk_bf16_f32 v113, v106, v107\n\t"
            "buffer_store_dwordx2 v[112:113], v124, %[o], 0 offen\n\t"
            PRIO_STEP
            "s_cmp_lt_u32 %[nxt], %[ntask]\n\t"
            "s_cbranch_scc1 L_task%=\n\t"
            "s_waitcnt vmcnt(0) lgkmcnt(0)"
            : [cnt] "=&s"(s_cnt), [nxt] "=&s"(s_nxt), [t] "=&s"(s_t), [tc] "=&s"(s_tc)
            : "{v81}"(joff), "{v83}"(joff), "{v85}"(joff), "{v87}"(joff), "{v91}"(g8), "{v93}"(ctr), "{v95}"(one), "{v126}"(j8),
              "{v[100:101]}"(w01), "{v[102:103]}"(w23),
              [task] "s"(w), [ntask] "s"(ntask), [bv] "s"(srdBV), [o] "s"(srdO), [slo] "s"(0x00003F80u), [shi] "s"(0x3F800000u), [eps] "s"(eps2),
              [au] "i"(K2_AU), [le0] "i"(K2_LSZ), [le1] "i"(K2_LSZ + 4), [le2] "i"(K2_LSZ + 8), [le3] "i"(K2_LSZ + 12),
              [le4] "i"(K2_LSZ + 16), [le5] "i"(K2_LSZ + 20), [le6] "i"(K2_LSZ + 24), [le7] "i"(K2_LSZ + 28)
            : "v64", "v65", "v66", "v67", "v68", "v69", "v70", "v71", "v72", "v73", "v74", "v75", "v76", "v77", "v78", "v79",
              "v80", "v82", "v84", "v86", "v88", "v89", "v90", "v92", "v94", "v96", "v97", "v98", "v99",
              "v104", "v105", "v106", "v107", "v108", "v109", "v110", "v111", "v112", "v113", "v114", "v115",
              "v116", "v117", "v118", "v119", "v120", "v121", "v122", "v123", "v124", "v125", "v127", "scc", "memory");
#undef STAGE
    }
    STAMP(10);
    STAMPC(15);
    SPAN1(1);
}

extern "C" void kernel_launch(void* const* d_in, const int* in_sizes, int n_in,
                              void* d_out, int out_size, void* d_ws, size_t ws_size,
                              hipStream_t stream) {
    const u16* x = (const u16*)d_in[0];
    const u16* ea = (const u16*)d_in[1];
    const int* src = (const int*)d_in[2];
    const int* dst = (const int*)d_in[3];
    const u16* wa = (const u16*)d_in[4];
    const u16* wb = (const u16*)d_in[5];
    const u16* wc = (const u16*)d_in[6];
    const u16* wu = (const u16*)d_in[7];
    const u16* wv = (const u16*)d_in[8];
    (void)hipFuncSetAttribute((const void*)k1_gemm_bin, hipFuncAttributeMaxDynamicSharedMemorySize, K1_LDS);
    k1_gemm_bin<<<256, 512, K1_LDS, stream>>>(x, wa, wb, wu, wv, src, dst, ea, (char*)d_ws, (const char*)d_out, (const char*)wc);
    (void)hipFuncSetAttribute((const void*)k2_edges, hipFuncAttributeMaxDynamicSharedMemorySize, K2_LDS);
    k2_edges<<<256, 1024, K2_LDS, stream>>>(x, wc, src, dst, ea, (const char*)d_ws, (u16*)d_out);
}
```

```cpp
#define MEASURE 0
#define SPAN 1
#include <hip/hip_runtime.h>
#include <hip/hip_bf16.h>
#include <stdint.h>

typedef unsigned short u16;
typedef unsigned int u32;
typedef u32 u32x2 __attribute__((ext_vector_type(2)));
typedef u32 u32x4 __attribute__((ext_vector_type(4)));
typedef float f32x2 __attribute__((ext_vector_type(2)));
typedef float f32x4 __attribute__((ext_vector_type(4)));
typedef float f32x16 __attribute__((ext_vector_type(16)));
typedef short s16x4 __attribute__((ext_vector_type(4)));
typedef short s16x8 __attribute__((ext_vector_type(8)));
typedef __bf16 bf16x8 __attribute__((ext_vector_type(8)));
typedef __bf16 bf16x4 __attribute__((ext_vector_type(4)));
typedef __bf16 bf16x2 __attribute__((ext_vector_type(2)));

constexpr int NN = 10000;
constexpr int NE = 160000;
constexpr int DD = 256;
constexpr int NP = 10048;
constexpr int RG = 320;
constexpr int NBK = 64;
constexpr int NB = 157;
constexpr int NCH = 64;
constexpr int CHE = 2500;
constexpr int CAP = 80;
constexpr int CAPN = 44;
constexpr int LST = 45;
constexpr float NLOG2E = -1.4426950408889634f;
constexpr u32 NEUTRAL_REC = (u32)NN;
constexpr u32 NEUTRAL_BV = 0x00007F00u;

constexpr size_t OFF_BV = 0;
constexpr size_t OFF_AU = OFF_BV + (size_t)4 * NP * 256;
constexpr size_t OFF_R4 = OFF_AU + (size_t)4 * NP * 256;
constexpr size_t OFF_R2 = OFF_R4 + (size_t)NBK * NCH * CAP * 4;
constexpr size_t OFF_CNT = OFF_R2 + (size_t)NBK * NCH * CAP * 2;

#define LDS_AS __attribute__((address_space(3)))
#define GLB_AS __attribute__((address_space(1)))

__device__ __forceinline__ void glds16(const void* g, void* l) {
    __builtin_amdgcn_global_load_lds((const GLB_AS u32*)g, (LDS_AS u32*)l, 16, 0, 0);
}
__device__ __forceinline__ void glds16a(const void* sbase, u32 voff, u32 lds) {
    u32 keep;
    asm volatile("s_mov_b32 %0, m0\n\ts_mov_b32 m0, %3\n\ts_nop 0\n\tglobal_load_lds_dwordx4 %1, %2\n\ts_mov_b32 m0, %0" : "=&s"(keep) : "v"(voff), "s"(sbase), "s"(lds) : "memory");
}
__device__ __forceinline__ u32 cvt_pk_bf16(float lo, float hi) {
    const f32x2 v = {lo, hi}; return __builtin_bit_cast(u32, __builtin_convertvector(v, bf16x2));
}
#define WAIT_V(n) asm volatile("s_waitcnt vmcnt(" #n ")" ::: "memory")
#define WAIT_L(n) asm volatile("s_waitcnt lgkmcnt(" #n ")" ::: "memory")
#define BAR() __builtin_amdgcn_s_barrier()
#define EXIT2_AT 99
#ifndef EXIT_AT
#define EXIT_AT 99
#endif
#define STORE_AUX 16
#define SPAN0() unsigned long long span_t0_ = __builtin_amdgcn_s_memrealtime()
#define MARK(I) do { unsigned long long tm_ = __builtin_amdgcn_s_memrealtime(); if ((threadIdx.x & 63) == 0) ((unsigned long long*)((char*)ws + (200u << 20)))[((I) * 256 + blockIdx.x) * 16 + (threadIdx.x >> 6)] = tm_; } while (0)
#define SPAN1(K) do { asm volatile("s_waitcnt vmcnt(0) lgkmcnt(0)" ::: "memory"); unsigned long long t1_ = __builtin_amdgcn_s_memrealtime(); if ((threadIdx.x & 63) == 0) { unsigned long long* p_ = (unsigned long long*)((char*)ws + (192u << 20)) + (((K) * 256 + blockIdx.x) * 16 + (threadIdx.x >> 6)) * 2; p_[0] = span_t0_; p_[1] = t1_; } } while (0)
#define STAMP(i)
#define STAMPC(i)
#define TICK(v)

constexpr int K1_R0 = 0, K1_R1 = 65536, K1_SCR = 131072;
constexpr int K1_ST4 = K1_SCR, K1_ST2 = K1_ST4 + NBK * CAP * 4, K1_BC = K1_ST2 + NBK * CAP * 2;
constexpr int K1_LDS = K1_SCR + 32768;
static_assert(K1_BC + NBK * 4 <= K1_LDS, "lds");

__global__ __launch_bounds__(512, 2) void k1_gemm_bin(
    const u16* __restrict__ x, const u16* __restrict__ wa, const u16* __restrict__ wb, const u16* __restrict__ wu, const u16* __restrict__ wv,
    const int* __restrict__ src, const int* __restrict__ dst, const u16* __restrict__ ea, char* __restrict__ ws) {
    extern __shared__ __attribute__((aligned(16))) char smem[];
    const int tid = threadIdx.x, lane = tid & 63;
    const int w = __builtin_amdgcn_readfirstlane(tid >> 6);
    const int b = blockIdx.x;
    const int type = (b >> 5) & 1;
    const int ct2 = (b >> 3) & 3;
    const int rg = (b & 7) * 4 + (b >> 6);
    const int row0 = rg * RG;
    const int chunk = (b & 31) | ((b >> 6) << 5) ;
    const bool binner = type == 1 && b < 128;
    constexpr int KOFF = 0;
    SPAN0();
#if EXIT_AT == 0
    if (ws) { SPAN1(0); return; }
#endif
    STAMP(0);
    int bd[5], bs[5]; u32 be[5];
    if (binner) {
#pragma unroll
        for (int i = 0; i < 5; ++i) {
            const int k = tid + 512 * i;
            const int e = chunk * CHE + (k < CHE ? k : 0);
            bd[i] = dst[e]; bs[i] = src[e]; be[i] = ea[e];
        }
        if (tid < NBK) *(u32*)(smem + K1_BC + tid * 4) = 0;
    }
    {
        const int m = w >> 2;
        const u16* W = type == 0 ? (m == 0 ? wb : wv) : (m == 0 ? wa : wu);
        const int kb0 = (w & 3) * 64, kr = lane >> 3;
        const u16* g = W + (size_t)(kb0 + kr) * DD + ct2 * 64 + (((lane & 7) ^ ((kr & 2) << 1)) << 3);
#pragma unroll
        for (int i = 0; i < 8; ++i) glds16(g + (size_t)i * 8 * DD, smem + K1_R0 + m * 32768 + (kb0 / 8 + i) * 1024);
    }
    const int xr = lane >> 5, xp = lane & 31;
    auto stage_x = [&](int tile, int q, int ldsbase) {
        const int r = q * 2 + xr;
        int grow = row0 + tile * 32 + r; grow = grow < NN ? grow : NN - 1;
        glds16((const char*)x + (u32)(grow * 512 + ((xp ^ (r & 15)) << 4)), smem + ldsbase + q * 1024);
    };
#pragma unroll
    for (int i = 0; i < 8; ++i) { const int q = w * 8 + i; stage_x(q >> 4, q & 15, K1_R1 + (q >> 4) * 16384); }
    STAMP(1);
    WAIT_V(8); WAIT_L(0);
    BAR();
    STAMP(2);
    MARK(0);
#if EXIT_AT == 1
    if (ws) { SPAN1(0); return; }
#endif
    const int ch = w >> 2;
    bf16x8 wf0[16], wf1[16];
    {
        const int h = lane >> 5, blk = (lane >> 4) & 1, q = (lane & 15) >> 2, p = lane & 3;
        const u32 base = K1_R0 + (8 * h + q) * 128 + (((ch * 4 + blk * 2 + (p >> 1)) ^ ((q & 2) << 1)) << 4) + (p & 1) * 8;
#define TRRD(S) { u32x2 a0, a1, b0, b1; \
            asm volatile("ds_read_b64_tr_b16 %0, %4 offset:%c5\n\tds_read_b64_tr_b16 %1, %4 offset:%c6\n\t" \
                         "ds_read_b64_tr_b16 %2, %4 offset:%c7\n\tds_read_b64_tr_b16 %3, %4 offset:%c8" \
                         : "=&v"(a0), "=&v"(a1), "=&v"(b0), "=&v"(b1) \
                         : "v"(base), "i"((S) * 2048), "i"((S) * 2048 + 512), "i"(32768 + (S) * 2048), "i"(32768 + (S) * 2048 + 512) : "memory"); \
            wf0[S] = __builtin_bit_cast(bf16x8, __builtin_shufflevector(a0, a1, 0, 1, 2, 3)); \
            wf1[S] = __builtin_bit_cast(bf16x8, __builtin_shufflevector(b0, b1, 0, 1, 2, 3)); }
        TRRD(0) TRRD(1) TRRD(2) TRRD(3) TRRD(4) TRRD(5) TRRD(6) TRRD(7) TRRD(8) TRRD(9) TRRD(10) TRRD(11) TRRD(12) TRRD(13) TRRD(14) TRRD(15)
#undef TRRD
    }
    if (binner) {
#pragma unroll
        for (int i = 0; i < 5; ++i) {
            const int k = tid + 512 * i;
            if (k < CHE) {
                const int bk = (int)(((u32)bd[i] * 53432u) >> 23);
                const int dl = bd[i] - bk * NB;
                const u32 pos = atomicAdd((u32*)(smem + K1_BC + bk * 4), 1u);
                if (pos < CAP) {
                    *(u32*)(smem + K1_ST4 + (bk * CAP + pos) * 4) = (u32)bs[i] | (be[i] << 16);
                    *(u16*)(smem + K1_ST2 + (bk * CAP + pos) * 2) = (u16)dl;
                }
            }
        }
    }
    STAMP(3);
    WAIT_V(0); WAIT_L(0);
    __builtin_amdgcn_sched_barrier(0);
    BAR();
    STAMP(4);
    MARK(1);
#if EXIT_AT == 2
    if (ws) { asm volatile("" :: "v"(wf0[0]), "v"(wf1[15])); SPAN1(0); return; }
#endif
#pragma unroll
    for (int i = 0; i < 8; ++i) { const int q = w * 8 + i; stage_x(4 + (q >> 4), q & 15, K1_R0 + (q >> 4) * 16384); }
    if (binner) {
        u32* R4 = (u32*)(ws + OFF_R4); u16* R2 = (u16*)(ws + OFF_R2); u32* CNT = (u32*)(ws + OFF_CNT);
        u32x4 t4[3]; u32x2 t2[3];
#pragma unroll
        for (int i = 0; i < 3; ++i) { const int q = tid + 512 * i; const int qq = q < NBK * (CAP / 4) ? q : 0;
            t4[i] = *(const u32x4*)(smem + K1_ST4 + qq * 16); t2[i] = *(const u32x2*)(smem + K1_ST2 + qq * 8); }
        const u32 cv = *(const u32*)(smem + K1_BC + (tid & 63) * 4);
#pragma unroll
        for (int i = 0; i < 3; ++i) { const int q = tid + 512 * i;
            if (q < NBK * (CAP / 4)) { const int bk = q / (CAP / 4), o = q - bk * (CAP / 4);
                *(u32x4*)(R4 + ((size_t)bk * NCH + chunk) * CAP + o * 4) = t4[i];
                *(u32x2*)(R2 + ((size_t)bk * NCH + chunk) * CAP + o * 4) = t2[i]; } }
        if (tid < NBK) CNT[tid * NCH + chunk] = cv;
        WAIT_L(0);
        BAR();
    }
    if (rg == 0 && type == 0 && w == 0) ((u32*)(ws + OFF_BV))[((size_t)ct2 * NP + NN) * 64 + lane] = NEUTRAL_BV;

    const int r = lane & 31, h = lane >> 5;
    const int abase = r * 512 + ((((r & 14) | ((h ^ r) & 1))) << 4);
    char* const scr = smem + K1_SCR + w * 4096;
    const __amdgpu_buffer_rsrc_t trs = __builtin_amdgcn_make_buffer_rsrc(ws + (type ? OFF_AU : OFF_BV) + (size_t)ct2 * NP * 256, 0, NN * 256, 0x00020000);
    const u32 svo = (lane >> 3) * 256 + ch * 128 + (lane & 7) * 16;

    const int s4 = w & 3;
    if (ch == 0) __builtin_amdgcn_s_setprio(1);
    STAMP(5);
#pragma unroll 1
    for (int st = 0; st < (EXIT_AT == 3 ? 1 : (EXIT_AT == 4 ? 2 : 3)); ++st) {
        int tile, ldsbase;
        if (st == 0) { tile = s4; ldsbase = K1_R1 + s4 * 16384; }
        else if (st == 1) { tile = 4 + s4; ldsbase = K1_R0 + s4 * 16384; }
        else { const int t = (w < 2) ? w : (w >= 6 ? w - 6 : 99); tile = 8 + t; ldsbase = K1_R1 + (t & 1) * 16384; }
        const int trow = row0 + tile * 32;
        const bool active = tile < 10 && trow < NN;
        unsigned long long tkA = 0, tkB = 0, tkC = 0, tkD = 0;
        if (active) {
            TICK(tkA);
            bf16x8 a[16];
#pragma unroll
            for (int s = 0; s < 8; ++s) a[s] = *(const bf16x8*)(smem + ldsbase + (abase ^ (s << 5)));
            __builtin_amdgcn_sched_barrier(0);
            f32x16 acc0, acc1;
#pragma unroll
            for (int i = 0; i < 16; ++i) { acc0[i] = 0.f; acc1[i] = 0.f; }
#pragma unroll
            for (int s = 0; s < 8; ++s) {
                a[s + 8] = *(const bf16x8*)(smem + ldsbase + (abase ^ (s << 5)) + 256);
                acc0 = __builtin_amdgcn_mfma_f32_32x32x16_bf16(a[s], wf0[s], acc0, 0, 0, 0);
                acc1 = __builtin_amdgcn_mfma_f32_32x32x16_bf16(a[s], wf1[s], acc1, 0, 0, 0);
                __builtin_amdgcn_sched_group_barrier(0x100, 1, 0);
                __builtin_amdgcn_sched_group_barrier(0x8, 2, 0);
            }
            __builtin_amdgcn_sched_barrier(0);
#pragma unroll
            for (int s = 8; s < 16; ++s) {
                acc0 = __builtin_amdgcn_mfma_f32_32x32x16_bf16(a[s], wf0[s], acc0, 0, 0, 0);
                acc1 = __builtin_amdgcn_mfma_f32_32x32x16_bf16(a[s], wf1[s], acc1, 0, 0, 0);
            }
            TICK(tkB);
            char* const sw = scr + h * 512 + r * 4;
            if (type == 0) {
#pragma unroll
                for (int i = 0; i < 16; i += 2) {
                    const u32 bb = cvt_pk_bf16(acc0[i] * NLOG2E, acc0[i + 1] * NLOG2E);
#pragma unroll
                    for (int j = 0; j < 2; ++j) {
                        const u32 vb = __float_as_uint(acc1[i + j]);
                        const u32 bl = j ? (bb >> 16) : (bb & 0xFFFFu);
                        const u32 u = __builtin_elementwise_sub_sat((vb & 0x7FFFFFFFu) + 0x8000u, bl);
                        *(u32*)(sw + (((i + j) & 3) + 8 * ((i + j) >> 2)) * 128) = (vb & 0x80000000u) | (u & 0x7FFF0000u) | bl;
                    }
                }
            } else {
#pragma unroll
                for (int i = 0; i < 16; ++i) *(u32*)(sw + ((i & 3) + 8 * (i >> 2)) * 128) = cvt_pk_bf16(acc0[i] * NLOG2E, acc1[i]);
            }
            TICK(tkC);
#pragma unroll
            for (int i = 0; i < 4; ++i) {
                const u32x4 v = *(const u32x4*)(scr + i * 1024 + lane * 16);
                __builtin_amdgcn_raw_buffer_store_b128(v, trs, svo + (u32)(trow + i * 8) * 256, 0, STORE_AUX);
            }
        }
        TICK(tkD);
        if (st == 0) STAMP(6); else if (st == 1) STAMP(8); else STAMP(10);
        if (st < 2) {
            if (active) WAIT_V(4); else WAIT_V(0);
            BAR();
            if (st == 0) {
#pragma unroll
                for (int i = 0; i < 4; ++i) { const int q = w * 4 + i; stage_x(8 + (q >> 4), q & 15, K1_R1 + (q >> 4) * 16384); }
            }
        }
    }
    SPAN1(0);
}

constexpr int LSTB = LST * 4;
constexpr int K2_X = 0;
constexpr int K2_AU = K2_X + 20480;
constexpr int K2_LIDX = K2_AU + 40960;
constexpr int K2_LSZ = (NB * LSTB + 32 + 256 + 15) / 16 * 16;
constexpr int K2_LE = K2_LIDX + K2_LSZ;
constexpr int K2_CNT = K2_LE + K2_LSZ;
constexpr int K2_HIST = K2_CNT + 160 * 4;
constexpr int K2_ORD = K2_HIST + 128 * 4;
constexpr int K2_MISC = K2_ORD + 80 * 32;
constexpr int K2_LDS = K2_MISC + 16;
constexpr int K2_TRASH = NB * LSTB + 32;
static_assert(K2_TRASH + 256 <= K2_LSZ, "trash");
static_assert(K2_LSZ < 65536 - 64, "ds offset");

__device__ __forceinline__ float dot2sel(u32 d, u32 sel, float c) {
    return __builtin_amdgcn_fdot2_f32_bf16(__builtin_bit_cast(bf16x2, d), __builtin_bit_cast(bf16x2, sel), c, false);
}

struct Acc { f32x2 n01, n23, d01, d23; };

__device__ __forceinline__ void edge_step(Acc& a, const u32x4 d, const u32 rec, const f32x2 a01, const f32x2 a23, const f32x2 w01, const f32x2 w23, const u32 sel) {
    const float e = __uint_as_float(rec & 0xFFFF0000u);
    const f32x2 ee = {e, e};
    const f32x2 t01 = __builtin_elementwise_fma(ee, w01, a01);
    const f32x2 t23 = __builtin_elementwise_fma(ee, w23, a23);
    f32x2 E01, E23;
    E01.x = __builtin_amdgcn_exp2f(dot2sel(d.x, sel, t01.x));
    E01.y = __builtin_amdgcn_exp2f(dot2sel(d.y, sel, t01.y));
    E23.x = __builtin_amdgcn_exp2f(dot2sel(d.z, sel, t23.x));
    E23.y = __builtin_amdgcn_exp2f(dot2sel(d.w, sel, t23.y));
    const f32x2 one = {1.f, 1.f};
    E01 += one; E23 += one;
    f32x2 s01, s23;
    s01.x = __builtin_amdgcn_rcpf(E01.x); s01.y = __builtin_amdgcn_rcpf(E01.y);
    s23.x = __builtin_amdgcn_rcpf(E23.x); s23.y = __builtin_amdgcn_rcpf(E23.y);
    a.d01 += s01; a.d23 += s23;
    const f32x2 v01 = {__uint_as_float(d.x), __uint_as_float(d.y)};
    const f32x2 v23 = {__uint_as_float(d.z), __uint_as_float(d.w)};
    a.n01 = __builtin_elementwise_fma(s01, v01, a.n01);
    a.n23 = __builtin_elementwise_fma(s23, v23, a.n23);
}

__device__ __forceinline__ void node_finish(const Acc& a, const u32x4 au, const u32x2 xv, u16* outp) {
    const float num[4] = {a.n01.x, a.n01.y, a.n23.x, a.n23.y};
    const float den[4] = {a.d01.x, a.d01.y, a.d23.x, a.d23.y};
    const u32 aus[4] = {au.x, au.y, au.z, au.w};
    const float xs[4] = {__uint_as_float(xv.x << 16), __uint_as_float(xv.x & 0xFFFF0000u), __uint_as_float(xv.y << 16), __uint_as_float(xv.y & 0xFFFF0000u)};
    float o[4];
#pragma unroll
    for (int c = 0; c < 4; ++c) {
        const float z = __uint_as_float(aus[c] & 0xFFFF0000u) + num[c] * __builtin_amdgcn_rcpf(den[c] + 1e-6f);
        const float rl = z > 0.f ? z : 0.f * z;
        o[c] = xs[c] + rl;
    }
    u32x2 ov; ov.x = cvt_pk_bf16(o[0], o[1]); ov.y = cvt_pk_bf16(o[2], o[3]);
    *(u32x2*)outp = ov;
}

template <int CTRL, int RM> __device__ __forceinline__ u32 dpp_add(u32 v) {
    return v + (u32)__builtin_amdgcn_update_dpp(0, (int)v, CTRL, RM, 0xf, false);
}

__global__ __launch_bounds__(1024, 4) void k2_edges(
    const u16* __restrict__ x, const u16* __restrict__ wc, const int* __restrict__ src, const int* __restrict__ dst, const u16* __restrict__ ea,
    const char* __restrict__ ws, u16* __restrict__ out) {
    extern __shared__ __attribute__((aligned(16))) char smem[];
    const int tid = threadIdx.x, lane = tid & 63;
    const int w = __builtin_amdgcn_readfirstlane(tid >> 6);
    const int b = blockIdx.x;
    const int slice = (b & 7) >> 1;
    const int bucket = (b >> 3) * 2 + (b & 1);
    const int node0 = bucket * NB;
    const int nvalid = (NN - node0) < NB ? (NN - node0) : NB;
    u32* const cnt = (u32*)(smem + K2_CNT);
    u32* const hist = (u32*)(smem + K2_HIST);
    u32* const misc = (u32*)(smem + K2_MISC);
    constexpr int KOFF = 256 * 16;
    SPAN0();
    STAMP(0);

    const u32* R4 = (const u32*)(ws + OFF_R4) + (size_t)bucket * NCH * CAP;
    const u16* R2 = (const u16*)(ws + OFF_R2) + (size_t)bucket * NCH * CAP;
    const u32* CNT = (const u32*)(ws + OFF_CNT) + bucket * NCH;
    const int ch0 = tid / (CAP / 4), ch1 = (tid + 1024) / (CAP / 4);
    const u32x4 r40 = *(const u32x4*)(R4 + tid * 4);
    const u32x2 r20 = *(const u32x2*)(R2 + tid * 4);
    const u32 c0 = CNT[ch0];
    u32x4 r41 = {0, 0, 0, 0}; u32x2 r21 = {0, 0}; u32 c1 = 0;
    if (w < 4) { r41 = *(const u32x4*)(R4 + (tid + 1024) * 4); r21 = *(const u32x2*)(R2 + (tid + 1024) * 4); c1 = CNT[ch1]; }
    const char* const bvb = ws + OFF_BV + (size_t)slice * NP * 256;
    const char* const aub = ws + OFF_AU + (size_t)slice * NP * 256;
    const int g = lane >> 4, j = lane & 15;
    const u32x2 wcv = *(const u32x2*)(wc + slice * 64 + j * 4);
    for (int i = tid; i < K2_LSZ / 16; i += 1024) {
        *(u32x4*)(smem + K2_LIDX + i * 16) = (u32x4){NEUTRAL_REC, NEUTRAL_REC, NEUTRAL_REC, NEUTRAL_REC};
        *(u32x4*)(smem + K2_LE + i * 16) = (u32x4){0, 0, 0, 0};
    }
    if (tid < 160 + 64) cnt[tid] = 0;
    if (tid == 0) { misc[0] = 16; misc[1] = 0; }
    f32x2 w01 = {__uint_as_float(wcv.x << 16) * NLOG2E, __uint_as_float(wcv.x & 0xFFFF0000u) * NLOG2E};
    f32x2 w23 = {__uint_as_float(wcv.y << 16) * NLOG2E, __uint_as_float(wcv.y & 0xFFFF0000u) * NLOG2E};
    asm volatile("" : "+v"(w01), "+v"(w23));
    STAMP(1);
    WAIT_V(0); WAIT_L(0); BAR();
    {
        const char* ga = aub + (size_t)node0 * 256;
#pragma unroll
        for (int i = 0; i < 3; ++i) { const int q = w + 16 * i; if (q < 40) glds16a(ga, q * 1024 + lane * 16, K2_AU + q * 1024); }
        const char* gx = (const char*)x + slice * 128;
#pragma unroll
        for (int i = 0; i < 2; ++i) {
            const int q = w + 16 * i;
            if (q < 20) { int row = node0 + q * 8 + (lane >> 3); row = row < NN ? row : NN - 1;
                glds16a(gx, row * 512 + (lane & 7) * 16, K2_X + q * 1024); }
        }
    }
    STAMP(2);
    MARK(2);
    {
        const int p0 = (tid - ch0 * (CAP / 4)) * 4;
        const u32 rec0[4] = {r40.x, r40.y, r40.z, r40.w};
        const u32 dl0[4] = {r20.x & 0xFFFF, r20.x >> 16, r20.y & 0xFFFF, r20.y >> 16};
        u32 slot[4];
        bool ovf = c0 > CAP;
#pragma unroll
        for (int k = 0; k < 4; ++k) slot[k] = atomicAdd(&cnt[p0 + k < c0 ? dl0[k] : 224 + lane], 1u);
        if (w < 4) {
            const int p1 = (tid + 1024 - ch1 * (CAP / 4)) * 4;
            const u32 rec1[4] = {r41.x, r41.y, r41.z, r41.w};
            const u32 dl1[4] = {r21.x & 0xFFFF, r21.x >> 16, r21.y & 0xFFFF, r21.y >> 16};
            u32 slot1[4];
            ovf |= c1 > CAP;
#pragma unroll
            for (int k = 0; k < 4; ++k) slot1[k] = atomicAdd(&cnt[p1 + k < c1 ? dl1[k] : 224 + lane], 1u);
#pragma unroll
            for (int k = 0; k < 4; ++k) {
                const bool v = p1 + k < c1, ok = v && slot1[k] < CAPN;
                ovf |= v && !ok;
                const u32 o = ok ? dl1[k] * LSTB + slot1[k] * 4 : K2_TRASH + lane * 4;
                *(u32*)(smem + K2_LIDX + o) = rec1[k] & 0xFFFFu;
                *(u32*)(smem + K2_LE + o) = rec1[k] & 0xFFFF0000u;
            }
        }
#pragma unroll
        for (int k = 0; k < 4; ++k) {
            const bool v = p0 + k < c0, ok = v && slot[k] < CAPN;
            ovf |= v && !ok;
            const u32 o = ok ? dl0[k] * LSTB + slot[k] * 4 : K2_TRASH + lane * 4;
            *(u32*)(smem + K2_LIDX + o) = rec0[k] & 0xFFFFu;
            *(u32*)(smem + K2_LE + o) = rec0[k] & 0xFFFF0000u;
        }
        if (ovf) misc[1] = 1;
    }
    STAMP(3);
    WAIT_L(0); BAR();
    STAMP(4);
    const bool slow = misc[1] != 0;

    const u32 joff = j * 16;

    if (slow) {
        u32 sel = 0x00003F80u;
        asm volatile("" : "+s"(sel));
        for (int task = w; task * 4 < nvalid; task += 16) {
            const int n = task * 4 + g;
            const bool nv = n < nvalid;
            const int node = node0 + (nv ? n : 0);
            const u32x4 au = *(const u32x4*)(aub + (size_t)node * 256 + joff);
            const u32x2 xv = *(const u32x2*)(x + (size_t)node * DD + slice * 64 + j * 4);
            const f32x2 a01 = {__uint_as_float(au.x << 16), __uint_as_float(au.y << 16)};
            const f32x2 a23 = {__uint_as_float(au.z << 16), __uint_as_float(au.w << 16)};
            Acc acc = {{0.f, 0.f}, {0.f, 0.f}, {0.f, 0.f}, {0.f, 0.f}};
            const int lo = node0 + task * 4;
            for (int e0 = 0; e0 < NE; e0 += 64) {
                const int d = dst[e0 + lane];
                unsigned long long m = __ballot((u32)(d - lo) < 4u);
                if (m) {
                    const u32 rc = (u32)src[e0 + lane] | ((u32)ea[e0 + lane] << 16);
                    while (m) {
                        const int l = __builtin_ctzll(m); m &= m - 1;
                        const int dd = __builtin_amdgcn_readlane(d, l);
                        const u32 rr = __builtin_amdgcn_readlane(rc, l);
                        const u32 rec = (nv && dd == node) ? rr : NEUTRAL_REC;
                        const u32x4 dv = *(const u32x4*)(bvb + (size_t)((rec & 0xFFFFu) * 256 + joff));
                        edge_step(acc, dv, rec, a01, a23, w01, w23, sel);
                    }
                }
            }
            if (nv) node_finish(acc, au, xv, out + (size_t)node * DD + slice * 64 + j * 4);
        }
        return;
    }

    u32 mydeg = 0, myrank = 0;
    if (tid < nvalid) { mydeg = cnt[tid]; myrank = atomicAdd(&hist[63 - mydeg], 1u); }
    WAIT_L(0); BAR();
    if (w < 3) {
        const u32 h0 = hist[lane];
        u32 v = h0;
        v = dpp_add<0x111, 0xf>(v); v = dpp_add<0x112, 0xf>(v); v = dpp_add<0x114, 0xf>(v); v = dpp_add<0x118, 0xf>(v);
        v = dpp_add<0x142, 0xa>(v); v = dpp_add<0x143, 0xc>(v);
        const u32 base = (u32)__builtin_amdgcn_ds_bpermute((int)((63 - mydeg) * 4), (int)(v - h0));
        if (tid < nvalid) {
            const u32 pos = base + myrank;
            const u32x2 ent = {(u32)(K2_LIDX + tid * LSTB), (u32)(tid * 256) + mydeg};
            u32x2* o = (u32x2*)(smem + K2_ORD) + pos;
            o[0] = ent;
            if (pos == (u32)nvalid - 1) { o[1] = ent; o[2] = ent; o[3] = ent; }
        }
    }
    WAIT_V(0); WAIT_L(0); BAR();
    STAMP(5);
    MARK(3);
    STAMPC(14);

    const int ntask = (nvalid + 3) >> 2;
    if (w < ntask) {
        u32x4 srdBV, srdO;
        { const size_t p = (size_t)bvb; srdBV = (u32x4){(u32)p, (u32)(p >> 32) | (256u << 16), 0xFFFFFFFFu, 0x00020000u}; }
        { const size_t ob = (size_t)node0 * 512 + slice * 128; const size_t p = (size_t)out + ob; srdO = (u32x4){(u32)p, (u32)(p >> 32), (u32)((size_t)NN * 512 - ob), 0x00020000u}; }
        u32 g8 = K2_ORD + g * 8, ctr = K2_MISC, one = 1, j8 = j * 8;
        unsigned long long eps2 = 0x358637BD358637BDull;
        u32 s_cnt, s_nxt, s_t, s_tc;
#define STAGE(DS0, DS1, DS2, DS3, DSL, DSH, IX, EP, E1, DN, IXN, O1, O2) \
            "s_waitcnt lgkmcnt(0)\n\t" \
            "buffer_load_dwordx4 " DN ", " GIX(IXN) ", %[bv], 0 idxen offen\n\t" \
            "v_pk_fma_f32 v[112:113], " EP ", v[100:101], v[96:97] op_sel_hi:[0,1,1]\n\t" \
            "v_pk_fma_f32 v[114:115], " EP ", v[102:103], v[98:99] op_sel_hi:[0,1,1]\n\t" \
            "ds_read_b32 " IX ", v89 offset:" O1 "\n\t" \
            "ds_read_b32 " E1 ", v89 offset:" O2 "\n\t" \
            "s_waitcnt vmcnt(3)\n\t" \
            "v_dot2c_f32_bf16 v112, %[slo], " DS0 "\n\t" \
            "v_dot2c_f32_bf16 v113, %[slo], " DS1 "\n\t" \
            "v_dot2c_f32_bf16 v114, %[slo], " DS2 "\n\t" \
            "v_dot2c_f32_bf16 v115, %[slo], " DS3 "\n\t" \
            "v_exp_f32 v112, v112\n\t" \
            "v_exp_f32 v113, v113\n\t" \
            "v_exp_f32 v114, v114\n\t" \
            "v_exp_f32 v115, v115\n\t" \
            "v_pk_add_f32 v[112:113], v[112:113], 1.0 op_sel_hi:[1,0]\n\t" \
            "v_pk_add_f32 v[114:115], v[114:115], 1.0 op_sel_hi:[1,0]\n\t" \
            "v_rcp_f32 v112, v112\n\t" \
            "v_rcp_f32 v113, v113\n\t" \
            "v_rcp_f32 v114, v114\n\t" \
            "v_rcp_f32 v115, v115\n\t" \
            "s_sub_u32 %[cnt], %[cnt], 1\n\t" \
            "v_pk_add_f32 v[108:109], v[108:109], v[112:113]\n\t" \
            "v_pk_fma_f32 v[104:105], v[112:113], " DSL ", v[104:105]\n\t" \
            "v_pk_add_f32 v[110:111], v[110:111], v[114:115]\n\t" \
            "v_pk_fma_f32 v[106:107], v[114:115], " DSH ", v[106:107]\n\t" \
            "s_cmp_eq_u32 %[cnt], 0\n\t"
#define GIX(x) x
#define PRIO_ON 1
#define PRIO_INIT "s_setprio 3\n\t"
#define PRIO_STEP \
            "s_add_u32 %[tc], %[tc], 1\n\t" \
            "s_cmp_lg_u32 %[tc], 1\n\t s_cbranch_scc1 L_p1%=\n\t s_setprio 2\n\t L_p1%=:\n\t" \
            "s_cmp_lg_u32 %[tc], 2\n\t s_cbranch_scc1 L_p2%=\n\t s_setprio 1\n\t L_p2%=:\n\t" \
            "s_cmp_lg_u32 %[tc], 3\n\t s_cbranch_scc1 L_p3%=\n\t s_setprio 0\n\t L_p3%=:\n\t"
        asm volatile(
            PRIO_INIT
            "s_mov_b32 %[tc], 0\n\t"
            "s_lshl_b32 %[t], %[task], 5\n\t"
            "v_add_u32 v127, %[t], v91\n\t"
            "ds_read_b64 v[122:123], v127\n\t"
            "s_mov_b64 exec, 1\n\t"
            "ds_add_rtn_u32 v125, v93, v95\n\t"
            "s_mov_b64 exec, -1\n\t"
            "L_task%=:\n\t"
            "s_waitcnt lgkmcnt(0)\n\t"
            "v_readfirstlane_b32 %[nxt], v125\n\t"
            "v_readfirstlane_b32 %[cnt], v123\n\t"
            "v_mov_b32 v89, v122\n\t"
            "v_and_b32 v127, 0xffffff00, v123\n\t"
            "ds_read_b32 v80, v89\n\t"
            "ds_read_b32 v82, v89 offset:4\n\t"
            "ds_read_b32 v84, v89 offset:8\n\t"
            "ds_read_b32 v86, v89 offset:12\n\t"
            "ds_read_b32 v88, v89 offset:%c[le0]\n\t"
            "ds_read_b32 v90, v89 offset:%c[le1]\n\t"
            "ds_read_b32 v92, v89 offset:%c[le2]\n\t"
            "ds_read_b32 v94, v89 offset:%c[le3]\n\t"
            "v_add_u32 v112, v127, v81\n\t"
            "v_lshl_add_u32 v124, v127, 1, v126\n\t"
            "v_lshrrev_b32 v113, 1, v112\n\t"
            "ds_read_b128 v[116:119], v112 offset:%c[au]\n\t"
            "ds_read_b64 v[120:121], v113\n\t"
            "s_and_b32 %[cnt], %[cnt], 0xff\n\t"
            "s_lshl_b32 %[t], %[nxt], 5\n\t"
            "v_add_u32 v127, %[t], v91\n\t"
            "ds_read_b64 v[122:123], v127\n\t"
            "s_mov_b64 exec, 1\n\t"
            "ds_add_rtn_u32 v125, v93, v95\n\t"
            "s_mov_b64 exec, -1\n\t"
            "s_waitcnt lgkmcnt(9)\n\t"
            "buffer_load_dwordx4 v[64:67], v[80:81], %[bv], 0 idxen offen\n\t"
            "buffer_load_dwordx4 v[68:71], v[82:83], %[bv], 0 idxen offen\n\t"
            "buffer_load_dwordx4 v[72:75], v[84:85], %[bv], 0 idxen offen\n\t"
            "v_mov_b64 v[104:105], 0\n\t"
            "v_mov_b64 v[106:107], 0\n\t"
            "v_mov_b64 v[108:109], 0\n\t"
            "v_mov_b64 v[110:111], 0\n\t"
            "s_waitcnt lgkmcnt(2)\n\t"
            "v_lshlrev_b32 v96, 16, v116\n\t"
            "v_lshlrev_b32 v97, 16, v117\n\t"
            "v_lshlrev_b32 v98, 16, v118\n\t"
            "v_lshlrev_b32 v99, 16, v119\n\t"
            "s_cmp_eq_u32 %[cnt], 0\n\t"
            "s_cbranch_scc1 L_fin%=\n\t"
            "L_loop%=:\n\t"
            STAGE("v64", "v65", "v66", "v67", "v[64:65]", "v[66:67]", "v80", "v[88:89]", "v88", "v[76:79]", "v[86:87]", "16", "%c[le4]")
            "s_cbranch_scc1 L_fin%=\n\t"
            STAGE("v68", "v69", "v70", "v71", "v[68:69]", "v[70:71]", "v82", "v[90:91]", "v90", "v[64:67]", "v[80:81]", "20", "%c[le5]")
            "s_cbranch_scc1 L_fin%=\n\t"
            STAGE("v72", "v73", "v74", "v75", "v[72:73]", "v[74:75]", "v84", "v[92:93]", "v92", "v[68:71]", "v[82:83]", "24", "%c[le6]")
            "s_cbranch_scc1 L_fin%=\n\t"
            STAGE("v76", "v77", "v78", "v79", "v[76:77]", "v[78:79]", "v86", "v[94:95]", "v94", "v[72:75]", "v[84:85]", "28", "%c[le7]")
            "v_add_u32 v89, 16, v89\n\t"
            "s_cbranch_scc0 L_loop%=\n\t"
            "L_fin%=:\n\t"
            "v_pk_add_f32 v[108:109], v[108:109], %[eps]\n\t"
            "v_pk_add_f32 v[110:111], v[110:111], %[eps]\n\t"
            "v_rcp_f32 v108, v108\n\t"
            "v_rcp_f32 v109, v109\n\t"
            "v_rcp_f32 v110, v110\n\t"
            "v_rcp_f32 v111, v111\n\t"
            "v_pk_mul_f32 v[104:105], v[104:105], v[108:109]\n\t"
            "v_pk_mul_f32 v[106:107], v[106:107], v[110:111]\n\t"
            "v_dot2c_f32_bf16 v104, %[shi], v116\n\t"
            "v_dot2c_f32_bf16 v105, %[shi], v117\n\t"
            "v_dot2c_f32_bf16 v106, %[shi], v118\n\t"
            "v_dot2c_f32_bf16 v107, %[shi], v119\n\t"
            "v_maximum3_f32 v104, v104, 0, 0\n\t"
            "v_maximum3_f32 v105, v105, 0, 0\n\t"
            "v_maximum3_f32 v106, v106, 0, 0\n\t"
            "v_maximum3_f32 v107, v107, 0, 0\n\t"
            "v_dot2c_f32_bf16 v104, %[slo], v120\n\t"
            "v_dot2c_f32_bf16 v105, %[shi], v120\n\t"
            "v_dot2c_f32_bf16 v106, %[slo], v121\n\t"
            "v_dot2c_f32_bf16 v107, %[shi], v121\n\t"
            "s_nop 1\n\t"
            "v_cvt_pk_bf16_f32 v112, v104, v105\n\t"
            "v_cvt_pk_bf16_f32 v113, v106, v107\n\t"
            "buffer_store_dwordx2 v[112:113], v124, %[o], 0 offen\n\t"
            PRIO_STEP
            "s_cmp_lt_u32 %[nxt], %[ntask]\n\t"
            "s_cbranch_scc1 L_task%=\n\t"
            "s_waitcnt vmcnt(0) lgkmcnt(0)"
            : [cnt] "=&s"(s_cnt), [nxt] "=&s"(s_nxt), [t] "=&s"(s_t), [tc] "=&s"(s_tc)
            : "{v81}"(joff), "{v83}"(joff), "{v85}"(joff), "{v87}"(joff), "{v91}"(g8), "{v93}"(ctr), "{v95}"(one), "{v126}"(j8),
              "{v[100:101]}"(w01), "{v[102:103]}"(w23),
              [task] "s"(w), [ntask] "s"(ntask), [bv] "s"(srdBV), [o] "s"(srdO), [slo] "s"(0x00003F80u), [shi] "s"(0x3F800000u), [eps] "s"(eps2),
              [au] "i"(K2_AU), [le0] "i"(K2_LSZ), [le1] "i"(K2_LSZ + 4), [le2] "i"(K2_LSZ + 8), [le3] "i"(K2_LSZ + 12),
              [le4] "i"(K2_LSZ + 16), [le5] "i"(K2_LSZ + 20), [le6] "i"(K2_LSZ + 24), [le7] "i"(K2_LSZ + 28)
            : "v64", "v65", "v66", "v67", "v68", "v69", "v70", "v71", "v72", "v73", "v74", "v75", "v76", "v77", "v78", "v79",
              "v80", "v82", "v84", "v86", "v88", "v89", "v90", "v92", "v94", "v96", "v97", "v98", "v99",
              "v104", "v105", "v106", "v107", "v108", "v109", "v110", "v111", "v112", "v113", "v114", "v115",
              "v116", "v117", "v118", "v119", "v120", "v121", "v122", "v123", "v124", "v125", "v127", "scc", "memory");
#undef STAGE
    }
    STAMP(10);
    STAMPC(15);
    SPAN1(1);
    if (blockIdx.x == 0 && w == 0) {
        const unsigned long long* sp = (const unsigned long long*)((const char*)ws + (192u << 20));
        const unsigned long long* mk = (const unsigned long long*)((const char*)ws + (200u << 20));
        unsigned long long mn = ~0ull, mx = 0, m0 = 0, m1 = 0;
        for (int i = lane; i < 256 * 8; i += 64) { const int b2 = i >> 3, w2 = i & 7;
            const unsigned long long a = sp[(b2 * 16 + w2) * 2], e = sp[(b2 * 16 + w2) * 2 + 1];
            mn = a < mn ? a : mn; mx = e > mx ? e : mx;
            const unsigned long long k0 = mk[b2 * 16 + w2], k1 = mk[(256 + b2) * 16 + w2];
            m0 = k0 > m0 ? k0 : m0; m1 = k1 > m1 ? k1 : m1; }
        for (int o = 32; o; o >>= 1) { unsigned long long t;
            t = __shfl_xor(mn, o); mn = t < mn ? t : mn; t = __shfl_xor(mx, o); mx = t > mx ? t : mx;
            t = __shfl_xor(m0, o); m0 = t > m0 ? t : m0; t = __shfl_xor(m1, o); m1 = t > m1 ? t : m1; }
        const unsigned long long k2s = span_t0_, k2a = mk[(2 * 256) * 16], k2b = mk[(3 * 256) * 16];
        unsigned long long val = 0;
        if (MEASURE == 1) val = mx - mn;
        if (MEASURE == 2) val = k2s - mx;
        if (MEASURE == 3) val = m0 - mn;
        if (MEASURE == 4) val = m1 - mn;
        if (MEASURE == 5) val = k2a - k2s;
        if (MEASURE == 6) val = k2b - k2s;
        if (MEASURE == 7) val = __builtin_amdgcn_s_memrealtime() - k2s;
        const unsigned long long t0 = __builtin_amdgcn_s_memrealtime();
        while (__builtin_amdgcn_s_memrealtime() - t0 < val * 10ull) __builtin_amdgcn_s_sleep(2);
    }
}

extern "C" void kernel_launch(void* const* d_in, const int* in_sizes, int n_in,
                              void* d_out, int out_size, void* d_ws, size_t ws_size,
                              hipStream_t stream) {
    const u16* x = (const u16*)d_in[0];
    const u16* ea = (const u16*)d_in[1];
    const int* src = (const int*)d_in[2];
    const int* dst = (const int*)d_in[3];
    const u16* wa = (const u16*)d_in[4];
    const u16* wb = (const u16*)d_in[5];
    const u16* wc = (const u16*)d_in[6];
    const u16* wu = (const u16*)d_in[7];
    const u16* wv = (const u16*)d_in[8];
    (void)hipFuncSetAttribute((const void*)k1_gemm_bin, hipFuncAttributeMaxDynamicSharedMemorySize, K1_LDS);
    k1_gemm_bin<<<256, 512, K1_LDS, stream>>>(x, wa, wb, wu, wv, src, dst, ea, (char*)d_ws);
    (void)hipFuncSetAttribute((const void*)k2_edges, hipFuncAttributeMaxDynamicSharedMemorySize, K2_LDS);
    k2_edges<<<256, 1024, K2_LDS, stream>>>(x, wc, src, dst, ea, (const char*)d_ws, (u16*)d_out);
}
```

```cpp
#define MEASURE 0
#define SPAN 1
#include <hip/hip_runtime.h>
#include <hip/hip_bf16.h>
#include <stdint.h>

typedef unsigned short u16;
typedef unsigned int u32;
typedef u32 u32x2 __attribute__((ext_vector_type(2)));
typedef u32 u32x4 __attribute__((ext_vector_type(4)));
typedef float f32x2 __attribute__((ext_vector_type(2)));
typedef float f32x4 __attribute__((ext_vector_type(4)));
typedef float f32x16 __attribute__((ext_vector_type(16)));
typedef short s16x4 __attribute__((ext_vector_type(4)));
typedef short s16x8 __attribute__((ext_vector_type(8)));
typedef __bf16 bf16x8 __attribute__((ext_vector_type(8)));
typedef __bf16 bf16x4 __attribute__((ext_vector_type(4)));
typedef __bf16 bf16x2 __attribute__((ext_vector_type(2)));

constexpr int NN = 10000;
constexpr int NE = 160000;
constexpr int DD = 256;
constexpr int NP = 10048;
constexpr int RG = 320;
constexpr int NBK = 64;
constexpr int NB = 157;
constexpr int NCH = 64;
constexpr int CHE = 2500;
constexpr int CAP = 80;
constexpr int CAPN = 44;
constexpr int LST = 45;
constexpr float NLOG2E = -1.4426950408889634f;
constexpr u32 NEUTRAL_REC = (u32)NN;
constexpr u32 NEUTRAL_BV = 0x00007F00u;

constexpr size_t OFF_BV = 0;
constexpr size_t OFF_AU = OFF_BV + (size_t)4 * NP * 256;
constexpr size_t OFF_R4 = OFF_AU + (size_t)4 * NP * 256;
constexpr size_t OFF_R2 = OFF_R4 + (size_t)NBK * NCH * CAP * 4;
constexpr size_t OFF_CNT = OFF_R2 + (size_t)NBK * NCH * CAP * 2;

#define LDS_AS __attribute__((address_space(3)))
#define GLB_AS __attribute__((address_space(1)))

__device__ __forceinline__ void glds16(const void* g, void* l) {
    __builtin_amdgcn_global_load_lds((const GLB_AS u32*)g, (LDS_AS u32*)l, 16, 0, 0);
}
__device__ __forceinline__ void glds16a(const void* sbase, u32 voff, u32 lds) {
    u32 keep;
    asm volatile("s_mov_b32 %0, m0\n\ts_mov_b32 m0, %3\n\ts_nop 0\n\tglobal_load_lds_dwordx4 %1, %2\n\ts_mov_b32 m0, %0" : "=&s"(keep) : "v"(voff), "s"(sbase), "s"(lds) : "memory");
}
__device__ __forceinline__ u32 cvt_pk_bf16(float lo, float hi) {
    const f32x2 v = {lo, hi}; return __builtin_bit_cast(u32, __builtin_convertvector(v, bf16x2));
}
#define WAIT_V(n) asm volatile("s_waitcnt vmcnt(" #n ")" ::: "memory")
#define WAIT_L(n) asm volatile("s_waitcnt lgkmcnt(" #n ")" ::: "memory")
#define BAR() __builtin_amdgcn_s_barrier()
#define EXIT2_AT 99
#ifndef EXIT_AT
#define EXIT_AT 99
#endif
#define STORE_AUX 16
#define SPAN0() unsigned long long span_t0_ = __builtin_amdgcn_s_memrealtime()
#define MARK(I) do { unsigned long long tm_ = __builtin_amdgcn_s_memrealtime(); if ((threadIdx.x & 63) == 0) ((unsigned long long*)((char*)ws + (200u << 20)))[((I) * 256 + blockIdx.x) * 16 + (threadIdx.x >> 6)] = tm_; } while (0)
#define SPAN1(K) do { asm volatile("s_waitcnt vmcnt(0) lgkmcnt(0)" ::: "memory"); unsigned long long t1_ = __builtin_amdgcn_s_memrealtime(); if ((threadIdx.x & 63) == 0) { unsigned long long* p_ = (unsigned long long*)((char*)ws + (192u << 20)) + (((K) * 256 + blockIdx.x) * 16 + (threadIdx.x >> 6)) * 2; p_[0] = span_t0_; p_[1] = t1_; } } while (0)
#define STAMP(i)
#define STAMPC(i)
#define TICK(v)

constexpr int K1_R0 = 0, K1_R1 = 65536, K1_SCR = 131072;
constexpr int K1_ST4 = K1_SCR, K1_ST2 = K1_ST4 + NBK * CAP * 4, K1_BC = K1_ST2 + NBK * CAP * 2;
constexpr int K1_LDS = K1_SCR + 32768;
static_assert(K1_BC + NBK * 4 <= K1_LDS, "lds");

__global__ __launch_bounds__(512, 2) void k1_gemm_bin(
    const u16* __restrict__ x, const u16* __restrict__ wa, const u16* __restrict__ wb, const u16* __restrict__ wu, const u16* __restrict__ wv,
    const int* __restrict__ src, const int* __restrict__ dst, const u16* __restrict__ ea, char* __restrict__ ws) {
    extern __shared__ __attribute__((aligned(16))) char smem[];
    const int tid = threadIdx.x, lane = tid & 63;
    const int w = __builtin_amdgcn_readfirstlane(tid >> 6);
    const int b = blockIdx.x;
    const int type = (b >> 5) & 1;
    const int ct2 = (b >> 3) & 3;
    const int rg = (b & 7) * 4 + (b >> 6);
    const int row0 = rg * RG;
    const int chunk = (b & 31) | ((b >> 6) << 5) ;
    const bool binner = type == 1 && b < 128;
    constexpr int KOFF = 0;
    SPAN0();
    const unsigned long long cyc1_ = __builtin_amdgcn_s_memtime();
#if EXIT_AT == 0
    if (ws) { SPAN1(0); return; }
#endif
    STAMP(0);
    int bd[5], bs[5]; u32 be[5];
    if (binner) {
#pragma unroll
        for (int i = 0; i < 5; ++i) {
            const int k = tid + 512 * i;
            const int e = chunk * CHE + (k < CHE ? k : 0);
            bd[i] = dst[e]; bs[i] = src[e]; be[i] = ea[e];
        }
        if (tid < NBK) *(u32*)(smem + K1_BC + tid * 4) = 0;
    }
    {
        const int m = w >> 2;
        const u16* W = type == 0 ? (m == 0 ? wb : wv) : (m == 0 ? wa : wu);
        const int kb0 = (w & 3) * 64, kr = lane >> 3;
        const u16* g = W + (size_t)(kb0 + kr) * DD + ct2 * 64 + (((lane & 7) ^ ((kr & 2) << 1)) << 3);
#pragma unroll
        for (int i = 0; i < 8; ++i) glds16(g + (size_t)i * 8 * DD, smem + K1_R0 + m * 32768 + (kb0 / 8 + i) * 1024);
    }
    const int xr = lane >> 5, xp = lane & 31;
    auto stage_x = [&](int tile, int q, int ldsbase) {
        const int r = q * 2 + xr;
        int grow = row0 + tile * 32 + r; grow = grow < NN ? grow : NN - 1;
        glds16((const char*)x + (u32)(grow * 512 + ((xp ^ (r & 15)) << 4)), smem + ldsbase + q * 1024);
    };
#pragma unroll
    for (int i = 0; i < 8; ++i) { const int q = w * 8 + i; stage_x(q >> 4, q & 15, K1_R1 + (q >> 4) * 16384); }
    STAMP(1);
    WAIT_V(8); WAIT_L(0);
    BAR();
    STAMP(2);
    MARK(0);
#if EXIT_AT == 1
    if (ws) { SPAN1(0); return; }
#endif
    const int ch = w >> 2;
    bf16x8 wf0[16], wf1[16];
    {
        const int h = lane >> 5, blk = (lane >> 4) & 1, q = (lane & 15) >> 2, p = lane & 3;
        const u32 base = K1_R0 + (8 * h + q) * 128 + (((ch * 4 + blk * 2 + (p >> 1)) ^ ((q & 2) << 1)) << 4) + (p & 1) * 8;
#define TRRD(S) { u32x2 a0, a1, b0, b1; \
            asm volatile("ds_read_b64_tr_b16 %0, %4 offset:%c5\n\tds_read_b64_tr_b16 %1, %4 offset:%c6\n\t" \
                         "ds_read_b64_tr_b16 %2, %4 offset:%c7\n\tds_read_b64_tr_b16 %3, %4 offset:%c8" \
                         : "=&v"(a0), "=&v"(a1), "=&v"(b0), "=&v"(b1) \
                         : "v"(base), "i"((S) * 2048), "i"((S) * 2048 + 512), "i"(32768 + (S) * 2048), "i"(32768 + (S) * 2048 + 512) : "memory"); \
            wf0[S] = __builtin_bit_cast(bf16x8, __builtin_shufflevector(a0, a1, 0, 1, 2, 3)); \
            wf1[S] = __builtin_bit_cast(bf16x8, __builtin_shufflevector(b0, b1, 0, 1, 2, 3)); }
        TRRD(0) TRRD(1) TRRD(2) TRRD(3) TRRD(4) TRRD(5) TRRD(6) TRRD(7) TRRD(8) TRRD(9) TRRD(10) TRRD(11) TRRD(12) TRRD(13) TRRD(14) TRRD(15)
#undef TRRD
    }
    if (binner) {
#pragma unroll
        for (int i = 0; i < 5; ++i) {
            const int k = tid + 512 * i;
            if (k < CHE) {
                const int bk = (int)(((u32)bd[i] * 53432u) >> 23);
                const int dl = bd[i] - bk * NB;
                const u32 pos = atomicAdd((u32*)(smem + K1_BC + bk * 4), 1u);
                if (pos < CAP) {
                    *(u32*)(smem + K1_ST4 + (bk * CAP + pos) * 4) = (u32)bs[i] | (be[i] << 16);
                    *(u16*)(smem + K1_ST2 + (bk * CAP + pos) * 2) = (u16)dl;
                }
            }
        }
    }
    STAMP(3);
    WAIT_V(0); WAIT_L(0);
    __builtin_amdgcn_sched_barrier(0);
    BAR();
    STAMP(4);
    MARK(1);
#if EXIT_AT == 2
    if (ws) { asm volatile("" :: "v"(wf0[0]), "v"(wf1[15])); SPAN1(0); return; }
#endif
#pragma unroll
    for (int i = 0; i < 8; ++i) { const int q = w * 8 + i; stage_x(4 + (q >> 4), q & 15, K1_R0 + (q >> 4) * 16384); }
    if (binner) {
        u32* R4 = (u32*)(ws + OFF_R4); u16* R2 = (u16*)(ws + OFF_R2); u32* CNT = (u32*)(ws + OFF_CNT);
        u32x4 t4[3]; u32x2 t2[3];
#pragma unroll
        for (int i = 0; i < 3; ++i) { const int q = tid + 512 * i; const int qq = q < NBK * (CAP / 4) ? q : 0;
            t4[i] = *(const u32x4*)(smem + K1_ST4 + qq * 16); t2[i] = *(const u32x2*)(smem + K1_ST2 + qq * 8); }
        const u32 cv = *(const u32*)(smem + K1_BC + (tid & 63) * 4);
#pragma unroll
        for (int i = 0; i < 3; ++i) { const int q = tid + 512 * i;
            if (q < NBK * (CAP / 4)) { const int bk = q / (CAP / 4), o = q - bk * (CAP / 4);
                *(u32x4*)(R4 + ((size_t)bk * NCH + chunk) * CAP + o * 4) = t4[i];
                *(u32x2*)(R2 + ((size_t)bk * NCH + chunk) * CAP + o * 4) = t2[i]; } }
        if (tid < NBK) CNT[tid * NCH + chunk] = cv;
        WAIT_L(0);
        BAR();
    }
    if (rg == 0 && type == 0 && w == 0) ((u32*)(ws + OFF_BV))[((size_t)ct2 * NP + NN) * 64 + lane] = NEUTRAL_BV;

    const int r = lane & 31, h = lane >> 5;
    const int abase = r * 512 + ((((r & 14) | ((h ^ r) & 1))) << 4);
    char* const scr = smem + K1_SCR + w * 4096;
    const __amdgpu_buffer_rsrc_t trs = __builtin_amdgcn_make_buffer_rsrc(ws + (type ? OFF_AU : OFF_BV) + (size_t)ct2 * NP * 256, 0, NN * 256, 0x00020000);
    const u32 svo = (lane >> 3) * 256 + ch * 128 + (lane & 7) * 16;

    const int s4 = w & 3;
    if (ch == 0) __builtin_amdgcn_s_setprio(1);
    STAMP(5);
#pragma unroll 1
    for (int st = 0; st < (EXIT_AT == 3 ? 1 : (EXIT_AT == 4 ? 2 : 3)); ++st) {
        int tile, ldsbase;
        if (st == 0) { tile = s4; ldsbase = K1_R1 + s4 * 16384; }
        else if (st == 1) { tile = 4 + s4; ldsbase = K1_R0 + s4 * 16384; }
        else { const int t = (w < 2) ? w : (w >= 6 ? w - 6 : 99); tile = 8 + t; ldsbase = K1_R1 + (t & 1) * 16384; }
        const int trow = row0 + tile * 32;
        const bool active = tile < 10 && trow < NN;
        unsigned long long tkA = 0, tkB = 0, tkC = 0, tkD = 0;
        if (active) {
            TICK(tkA);
            bf16x8 a[16];
#pragma unroll
            for (int s = 0; s < 8; ++s) a[s] = *(const bf16x8*)(smem + ldsbase + (abase ^ (s << 5)));
            __builtin_amdgcn_sched_barrier(0);
            f32x16 acc0, acc1;
#pragma unroll
            for (int i = 0; i < 16; ++i) { acc0[i] = 0.f; acc1[i] = 0.f; }
#pragma unroll
            for (int s = 0; s < 8; ++s) {
                a[s + 8] = *(const bf16x8*)(smem + ldsbase + (abase ^ (s << 5)) + 256);
                acc0 = __builtin_amdgcn_mfma_f32_32x32x16_bf16(a[s], wf0[s], acc0, 0, 0, 0);
                acc1 = __builtin_amdgcn_mfma_f32_32x32x16_bf16(a[s], wf1[s], acc1, 0, 0, 0);
                __builtin_amdgcn_sched_group_barrier(0x100, 1, 0);
                __builtin_amdgcn_sched_group_barrier(0x8, 2, 0);
            }
            __builtin_amdgcn_sched_barrier(0);
#pragma unroll
            for (int s = 8; s < 16; ++s) {
                acc0 = __builtin_amdgcn_mfma_f32_32x32x16_bf16(a[s], wf0[s], acc0, 0, 0, 0);
                acc1 = __builtin_amdgcn_mfma_f32_32x32x16_bf16(a[s], wf1[s], acc1, 0, 0, 0);
            }
            TICK(tkB);
            char* const sw = scr + h * 512 + r * 4;
            if (type == 0) {
#pragma unroll
                for (int i = 0; i < 16; i += 2) {
                    const u32 bb = cvt_pk_bf16(acc0[i] * NLOG2E, acc0[i + 1] * NLOG2E);
#pragma unroll
                    for (int j = 0; j < 2; ++j) {
                        const u32 vb = __float_as_uint(acc1[i + j]);
                        const u32 bl = j ? (bb >> 16) : (bb & 0xFFFFu);
                        const u32 u = __builtin_elementwise_sub_sat((vb & 0x7FFFFFFFu) + 0x8000u, bl);
                        *(u32*)(sw + (((i + j) & 3) + 8 * ((i + j) >> 2)) * 128) = (vb & 0x80000000u) | (u & 0x7FFF0000u) | bl;
                    }
                }
            } else {
#pragma unroll
                for (int i = 0; i < 16; ++i) *(u32*)(sw + ((i & 3) + 8 * (i >> 2)) * 128) = cvt_pk_bf16(acc0[i] * NLOG2E, acc1[i]);
            }
            TICK(tkC);
#pragma unroll
            for (int i = 0; i < 4; ++i) {
                const u32x4 v = *(const u32x4*)(scr + i * 1024 + lane * 16);
                __builtin_amdgcn_raw_buffer_store_b128(v, trs, svo + (u32)(trow + i * 8) * 256, 0, STORE_AUX);
            }
        }
        TICK(tkD);
        if (st == 0) STAMP(6); else if (st == 1) STAMP(8); else STAMP(10);
        if (st < 2) {
            if (active) WAIT_V(4); else WAIT_V(0);
            BAR();
            if (st == 0) {
#pragma unroll
                for (int i = 0; i < 4; ++i) { const int q = w * 4 + i; stage_x(8 + (q >> 4), q & 15, K1_R1 + (q >> 4) * 16384); }
            }
        }
    }
    SPAN1(0);
    if (blockIdx.x == 0 && threadIdx.x == 0) { unsigned long long* q = (unsigned long long*)(ws + (208u << 20)); q[0] = __builtin_amdgcn_s_memtime() - cyc1_; q[1] = __builtin_amdgcn_s_memrealtime() - span_t0_; }
}

constexpr int LSTB = LST * 4;
constexpr int K2_X = 0;
constexpr int K2_AU = K2_X + 20480;
constexpr int K2_LIDX = K2_AU + 40960;
constexpr int K2_LSZ = (NB * LSTB + 32 + 256 + 15) / 16 * 16;
constexpr int K2_LE = K2_LIDX + K2_LSZ;
constexpr int K2_CNT = K2_LE + K2_LSZ;
constexpr int K2_HIST = K2_CNT + 160 * 4;
constexpr int K2_ORD = K2_HIST + 128 * 4;
constexpr int K2_MISC = K2_ORD + 80 * 32;
constexpr int K2_LDS = K2_MISC + 16;
constexpr int K2_TRASH = NB * LSTB + 32;
static_assert(K2_TRASH + 256 <= K2_LSZ, "trash");
static_assert(K2_LSZ < 65536 - 64, "ds offset");

__device__ __forceinline__ float dot2sel(u32 d, u32 sel, float c) {
    return __builtin_amdgcn_fdot2_f32_bf16(__builtin_bit_cast(bf16x2, d), __builtin_bit_cast(bf16x2, sel), c, false);
}

struct Acc { f32x2 n01, n23, d01, d23; };

__device__ __forceinline__ void edge_step(Acc& a, const u32x4 d, const u32 rec, const f32x2 a01, const f32x2 a23, const f32x2 w01, const f32x2 w23, const u32 sel) {
    const float e = __uint_as_float(rec & 0xFFFF0000u);
    const f32x2 ee = {e, e};
    const f32x2 t01 = __builtin_elementwise_fma(ee, w01, a01);
    const f32x2 t23 = __builtin_elementwise_fma(ee, w23, a23);
    f32x2 E01, E23;
    E01.x = __builtin_amdgcn_exp2f(dot2sel(d.x, sel, t01.x));
    E01.y = __builtin_amdgcn_exp2f(dot2sel(d.y, sel, t01.y));
    E23.x = __builtin_amdgcn_exp2f(dot2sel(d.z, sel, t23.x));
    E23.y = __builtin_amdgcn_exp2f(dot2sel(d.w, sel, t23.y));
    const f32x2 one = {1.f, 1.f};
    E01 += one; E23 += one;
    f32x2 s01, s23;
    s01.x = __builtin_amdgcn_rcpf(E01.x); s01.y = __builtin_amdgcn_rcpf(E01.y);
    s23.x = __builtin_amdgcn_rcpf(E23.x); s23.y = __builtin_amdgcn_rcpf(E23.y);
    a.d01 += s01; a.d23 += s23;
    const f32x2 v01 = {__uint_as_float(d.x), __uint_as_float(d.y)};
    const f32x2 v23 = {__uint_as_float(d.z), __uint_as_float(d.w)};
    a.n01 = __builtin_elementwise_fma(s01, v01, a.n01);
    a.n23 = __builtin_elementwise_fma(s23, v23, a.n23);
}

__device__ __forceinline__ void node_finish(const Acc& a, const u32x4 au, const u32x2 xv, u16* outp) {
    const float num[4] = {a.n01.x, a.n01.y, a.n23.x, a.n23.y};
    const float den[4] = {a.d01.x, a.d01.y, a.d23.x, a.d23.y};
    const u32 aus[4] = {au.x, au.y, au.z, au.w};
    const float xs[4] = {__uint_as_float(xv.x << 16), __uint_as_float(xv.x & 0xFFFF0000u), __uint_as_float(xv.y << 16), __uint_as_float(xv.y & 0xFFFF0000u)};
    float o[4];
#pragma unroll
    for (int c = 0; c < 4; ++c) {
        const float z = __uint_as_float(aus[c] & 0xFFFF0000u) + num[c] * __builtin_amdgcn_rcpf(den[c] + 1e-6f);
        const float rl = z > 0.f ? z : 0.f * z;
        o[c] = xs[c] + rl;
    }
    u32x2 ov; ov.x = cvt_pk_bf16(o[0], o[1]); ov.y = cvt_pk_bf16(o[2], o[3]);
    *(u32x2*)outp = ov;
}

template <int CTRL, int RM> __device__ __forceinline__ u32 dpp_add(u32 v) {
    return v + (u32)__builtin_amdgcn_update_dpp(0, (int)v, CTRL, RM, 0xf, false);
}

__global__ __launch_bounds__(1024, 4) void k2_edges(
    const u16* __restrict__ x, const u16* __restrict__ wc, const int* __restrict__ src, const int* __restrict__ dst, const u16* __restrict__ ea,
    const char* __restrict__ ws, u16* __restrict__ out) {
    extern __shared__ __attribute__((aligned(16))) char smem[];
    const int tid = threadIdx.x, lane = tid & 63;
    const int w = __builtin_amdgcn_readfirstlane(tid >> 6);
    const int b = blockIdx.x;
    const int slice = (b & 7) >> 1;
    const int bucket = (b >> 3) * 2 + (b & 1);
    const int node0 = bucket * NB;
    const int nvalid = (NN - node0) < NB ? (NN - node0) : NB;
    u32* const cnt = (u32*)(smem + K2_CNT);
    u32* const hist = (u32*)(smem + K2_HIST);
    u32* const misc = (u32*)(smem + K2_MISC);
    constexpr int KOFF = 256 * 16;
    SPAN0();
    const unsigned long long cyc0_ = __builtin_amdgcn_s_memtime();
    STAMP(0);

    const u32* R4 = (const u32*)(ws + OFF_R4) + (size_t)bucket * NCH * CAP;
    const u16* R2 = (const u16*)(ws + OFF_R2) + (size_t)bucket * NCH * CAP;
    const u32* CNT = (const u32*)(ws + OFF_CNT) + bucket * NCH;
    const int ch0 = tid / (CAP / 4), ch1 = (tid + 1024) / (CAP / 4);
    const u32x4 r40 = *(const u32x4*)(R4 + tid * 4);
    const u32x2 r20 = *(const u32x2*)(R2 + tid * 4);
    const u32 c0 = CNT[ch0];
    u32x4 r41 = {0, 0, 0, 0}; u32x2 r21 = {0, 0}; u32 c1 = 0;
    if (w < 4) { r41 = *(const u32x4*)(R4 + (tid + 1024) * 4); r21 = *(const u32x2*)(R2 + (tid + 1024) * 4); c1 = CNT[ch1]; }
    const char* const bvb = ws + OFF_BV + (size_t)slice * NP * 256;
    const char* const aub = ws + OFF_AU + (size_t)slice * NP * 256;
    const int g = lane >> 4, j = lane & 15;
    const u32x2 wcv = *(const u32x2*)(wc + slice * 64 + j * 4);
    for (int i = tid; i < K2_LSZ / 16; i += 1024) {
        *(u32x4*)(smem + K2_LIDX + i * 16) = (u32x4){NEUTRAL_REC, NEUTRAL_REC, NEUTRAL_REC, NEUTRAL_REC};
        *(u32x4*)(smem + K2_LE + i * 16) = (u32x4){0, 0, 0, 0};
    }
    if (tid < 160 + 64) cnt[tid] = 0;
    if (tid == 0) { misc[0] = 16; misc[1] = 0; }
    f32x2 w01 = {__uint_as_float(wcv.x << 16) * NLOG2E, __uint_as_float(wcv.x & 0xFFFF0000u) * NLOG2E};
    f32x2 w23 = {__uint_as_float(wcv.y << 16) * NLOG2E, __uint_as_float(wcv.y & 0xFFFF0000u) * NLOG2E};
    asm volatile("" : "+v"(w01), "+v"(w23));
    STAMP(1);
    WAIT_V(0); WAIT_L(0); BAR();
    {
        const char* ga = aub + (size_t)node0 * 256;
#pragma unroll
        for (int i = 0; i < 3; ++i) { const int q = w + 16 * i; if (q < 40) glds16a(ga, q * 1024 + lane * 16, K2_AU + q * 1024); }
        const char* gx = (const char*)x + slice * 128;
#pragma unroll
        for (int i = 0; i < 2; ++i) {
            const int q = w + 16 * i;
            if (q < 20) { int row = node0 + q * 8 + (lane >> 3); row = row < NN ? row : NN - 1;
                glds16a(gx, row * 512 + (lane & 7) * 16, K2_X + q * 1024); }
        }
    }
    STAMP(2);
    MARK(2);
    {
        const int p0 = (tid - ch0 * (CAP / 4)) * 4;
        const u32 rec0[4] = {r40.x, r40.y, r40.z, r40.w};
        const u32 dl0[4] = {r20.x & 0xFFFF, r20.x >> 16, r20.y & 0xFFFF, r20.y >> 16};
        u32 slot[4];
        bool ovf = c0 > CAP;
#pragma unroll
        for (int k = 0; k < 4; ++k) slot[k] = atomicAdd(&cnt[p0 + k < c0 ? dl0[k] : 224 + lane], 1u);
        if (w < 4) {
            const int p1 = (tid + 1024 - ch1 * (CAP / 4)) * 4;
            const u32 rec1[4] = {r41.x, r41.y, r41.z, r41.w};
            const u32 dl1[4] = {r21.x & 0xFFFF, r21.x >> 16, r21.y & 0xFFFF, r21.y >> 16};
            u32 slot1[4];
            ovf |= c1 > CAP;
#pragma unroll
            for (int k = 0; k < 4; ++k) slot1[k] = atomicAdd(&cnt[p1 + k < c1 ? dl1[k] : 224 + lane], 1u);
#pragma unroll
            for (int k = 0; k < 4; ++k) {
                const bool v = p1 + k < c1, ok = v && slot1[k] < CAPN;
                ovf |= v && !ok;
                const u32 o = ok ? dl1[k] * LSTB + slot1[k] * 4 : K2_TRASH + lane * 4;
                *(u32*)(smem + K2_LIDX + o) = rec1[k] & 0xFFFFu;
                *(u32*)(smem + K2_LE + o) = rec1[k] & 0xFFFF0000u;
            }
        }
#pragma unroll
        for (int k = 0; k < 4; ++k) {
            const bool v = p0 + k < c0, ok = v && slot[k] < CAPN;
            ovf |= v && !ok;
            const u32 o = ok ? dl0[k] * LSTB + slot[k] * 4 : K2_TRASH + lane * 4;
            *(u32*)(smem + K2_LIDX + o) = rec0[k] & 0xFFFFu;
            *(u32*)(smem + K2_LE + o) = rec0[k] & 0xFFFF0000u;
        }
        if (ovf) misc[1] = 1;
    }
    STAMP(3);
    WAIT_L(0); BAR();
    STAMP(4);
    const bool slow = misc[1] != 0;

    const u32 joff = j * 16;

    if (slow) {
        u32 sel = 0x00003F80u;
        asm volatile("" : "+s"(sel));
        for (int task = w; task * 4 < nvalid; task += 16) {
            const int n = task * 4 + g;
            const bool nv = n < nvalid;
            const int node = node0 + (nv ? n : 0);
            const u32x4 au = *(const u32x4*)(aub + (size_t)node * 256 + joff);
            const u32x2 xv = *(const u32x2*)(x + (size_t)node * DD + slice * 64 + j * 4);
            const f32x2 a01 = {__uint_as_float(au.x << 16), __uint_as_float(au.y << 16)};
            const f32x2 a23 = {__uint_as_float(au.z << 16), __uint_as_float(au.w << 16)};
            Acc acc = {{0.f, 0.f}, {0.f, 0.f}, {0.f, 0.f}, {0.f, 0.f}};
            const int lo = node0 + task * 4;
            for (int e0 = 0; e0 < NE; e0 += 64) {
                const int d = dst[e0 + lane];
                unsigned long long m = __ballot((u32)(d - lo) < 4u);
                if (m) {
                    const u32 rc = (u32)src[e0 + lane] | ((u32)ea[e0 + lane] << 16);
                    while (m) {
                        const int l = __builtin_ctzll(m); m &= m - 1;
                        const int dd = __builtin_amdgcn_readlane(d, l);
                        const u32 rr = __builtin_amdgcn_readlane(rc, l);
                        const u32 rec = (nv && dd == node) ? rr : NEUTRAL_REC;
                        const u32x4 dv = *(const u32x4*)(bvb + (size_t)((rec & 0xFFFFu) * 256 + joff));
                        edge_step(acc, dv, rec, a01, a23, w01, w23, sel);
                    }
                }
            }
            if (nv) node_finish(acc, au, xv, out + (size_t)node * DD + slice * 64 + j * 4);
        }
        return;
    }

    u32 mydeg = 0, myrank = 0;
    if (tid < nvalid) { mydeg = cnt[tid]; myrank = atomicAdd(&hist[63 - mydeg], 1u); }
    WAIT_L(0); BAR();
    if (w < 3) {
        const u32 h0 = hist[lane];
        u32 v = h0;
        v = dpp_add<0x111, 0xf>(v); v = dpp_add<0x112, 0xf>(v); v = dpp_add<0x114, 0xf>(v); v = dpp_add<0x118, 0xf>(v);
        v = dpp_add<0x142, 0xa>(v); v = dpp_add<0x143, 0xc>(v);
        const u32 base = (u32)__builtin_amdgcn_ds_bpermute((int)((63 - mydeg) * 4), (int)(v - h0));
        if (tid < nvalid) {
            const u32 pos = base + myrank;
            const u32x2 ent = {(u32)(K2_LIDX + tid * LSTB), (u32)(tid * 256) + mydeg};
            u32x2* o = (u32x2*)(smem + K2_ORD) + pos;
            o[0] = ent;
            if (pos == (u32)nvalid - 1) { o[1] = ent; o[2] = ent; o[3] = ent; }
        }
    }
    WAIT_V(0); WAIT_L(0); BAR();
    STAMP(5);
    MARK(3);
    STAMPC(14);

    const int ntask = (nvalid + 3) >> 2;
    if (w < ntask) {
        u32x4 srdBV, srdO;
        { const size_t p = (size_t)bvb; srdBV = (u32x4){(u32)p, (u32)(p >> 32) | (256u << 16), 0xFFFFFFFFu, 0x00020000u}; }
        { const size_t ob = (size_t)node0 * 512 + slice * 128; const size_t p = (size_t)out + ob; srdO = (u32x4){(u32)p, (u32)(p >> 32), (u32)((size_t)NN * 512 - ob), 0x00020000u}; }
        u32 g8 = K2_ORD + g * 8, ctr = K2_MISC, one = 1, j8 = j * 8;
        unsigned long long eps2 = 0x358637BD358637BDull;
        u32 s_cnt, s_nxt, s_t, s_tc;
#define STAGE(DS0, DS1, DS2, DS3, DSL, DSH, IX, EP, E1, DN, IXN, O1, O2) \
            "s_waitcnt lgkmcnt(0)\n\t" \
            "buffer_load_dwordx4 " DN ", " GIX(IXN) ", %[bv], 0 idxen offen\n\t" \
            "v_pk_fma_f32 v[112:113], " EP ", v[100:101], v[96:97] op_sel_hi:[0,1,1]\n\t" \
            "v_pk_fma_f32 v[114:115], " EP ", v[102:103], v[98:99] op_sel_hi:[0,1,1]\n\t" \
            "ds_read_b32 " IX ", v89 offset:" O1 "\n\t" \
            "ds_read_b32 " E1 ", v89 offset:" O2 "\n\t" \
            "s_waitcnt vmcnt(3)\n\t" \
            "v_dot2c_f32_bf16 v112, %[slo], " DS0 "\n\t" \
            "v_dot2c_f32_bf16 v113, %[slo], " DS1 "\n\t" \
            "v_dot2c_f32_bf16 v114, %[slo], " DS2 "\n\t" \
            "v_dot2c_f32_bf16 v115, %[slo], " DS3 "\n\t" \
            "v_exp_f32 v112, v112\n\t" \
            "v_exp_f32 v113, v113\n\t" \
            "v_exp_f32 v114, v114\n\t" \
            "v_exp_f32 v115, v115\n\t" \
            "v_pk_add_f32 v[112:113], v[112:113], 1.0 op_sel_hi:[1,0]\n\t" \
            "v_pk_add_f32 v[114:115], v[114:115], 1.0 op_sel_hi:[1,0]\n\t" \
            "v_rcp_f32 v112, v112\n\t" \
            "v_rcp_f32 v113, v113\n\t" \
            "v_rcp_f32 v114, v114\n\t" \
            "v_rcp_f32 v115, v115\n\t" \
            "s_sub_u32 %[cnt], %[cnt], 1\n\t" \
            "v_pk_add_f32 v[108:109], v[108:109], v[112:113]\n\t" \
            "v_pk_fma_f32 v[104:105], v[112:113], " DSL ", v[104:105]\n\t" \
            "v_pk_add_f32 v[110:111], v[110:111], v[114:115]\n\t" \
            "v_pk_fma_f32 v[106:107], v[114:115], " DSH ", v[106:107]\n\t" \
            "s_cmp_eq_u32 %[cnt], 0\n\t"
#define GIX(x) x
#define PRIO_ON 1
#define PRIO_INIT "s_setprio 3\n\t"
#define PRIO_STEP \
            "s_add_u32 %[tc], %[tc], 1\n\t" \
            "s_cmp_lg_u32 %[tc], 1\n\t s_cbranch_scc1 L_p1%=\n\t s_setprio 2\n\t L_p1%=:\n\t" \
            "s_cmp_lg_u32 %[tc], 2\n\t s_cbranch_scc1 L_p2%=\n\t s_setprio 1\n\t L_p2%=:\n\t" \
            "s_cmp_lg_u32 %[tc], 3\n\t s_cbranch_scc1 L_p3%=\n\t s_setprio 0\n\t L_p3%=:\n\t"
        asm volatile(
            PRIO_INIT
            "s_mov_b32 %[tc], 0\n\t"
            "s_lshl_b32 %[t], %[task], 5\n\t"
            "v_add_u32 v127, %[t], v91\n\t"
            "ds_read_b64 v[122:123], v127\n\t"
            "s_mov_b64 exec, 1\n\t"
            "ds_add_rtn_u32 v125, v93, v95\n\t"
            "s_mov_b64 exec, -1\n\t"
            "L_task%=:\n\t"
            "s_waitcnt lgkmcnt(0)\n\t"
            "v_readfirstlane_b32 %[nxt], v125\n\t"
            "v_readfirstlane_b32 %[cnt], v123\n\t"
            "v_mov_b32 v89, v122\n\t"
            "v_and_b32 v127, 0xffffff00, v123\n\t"
            "ds_read_b32 v80, v89\n\t"
            "ds_read_b32 v82, v89 offset:4\n\t"
            "ds_read_b32 v84, v89 offset:8\n\t"
            "ds_read_b32 v86, v89 offset:12\n\t"
            "ds_read_b32 v88, v89 offset:%c[le0]\n\t"
            "ds_read_b32 v90, v89 offset:%c[le1]\n\t"
            "ds_read_b32 v92, v89 offset:%c[le2]\n\t"
            "ds_read_b32 v94, v89 offset:%c[le3]\n\t"
            "v_add_u32 v112, v127, v81\n\t"
            "v_lshl_add_u32 v124, v127, 1, v126\n\t"
            "v_lshrrev_b32 v113, 1, v112\n\t"
            "ds_read_b128 v[116:119], v112 offset:%c[au]\n\t"
            "ds_read_b64 v[120:121], v113\n\t"
            "s_and_b32 %[cnt], %[cnt], 0xff\n\t"
            "s_lshl_b32 %[t], %[nxt], 5\n\t"
            "v_add_u32 v127, %[t], v91\n\t"
            "ds_read_b64 v[122:123], v127\n\t"
            "s_mov_b64 exec, 1\n\t"
            "ds_add_rtn_u32 v125, v93, v95\n\t"
            "s_mov_b64 exec, -1\n\t"
            "s_waitcnt lgkmcnt(9)\n\t"
            "buffer_load_dwordx4 v[64:67], v[80:81], %[bv], 0 idxen offen\n\t"
            "buffer_load_dwordx4 v[68:71], v[82:83], %[bv], 0 idxen offen\n\t"
            "buffer_load_dwordx4 v[72:75], v[84:85], %[bv], 0 idxen offen\n\t"
            "v_mov_b64 v[104:105], 0\n\t"
            "v_mov_b64 v[106:107], 0\n\t"
            "v_mov_b64 v[108:109], 0\n\t"
            "v_mov_b64 v[110:111], 0\n\t"
            "s_waitcnt lgkmcnt(2)\n\t"
            "v_lshlrev_b32 v96, 16, v116\n\t"
            "v_lshlrev_b32 v97, 16, v117\n\t"
            "v_lshlrev_b32 v98, 16, v118\n\t"
            "v_lshlrev_b32 v99, 16, v119\n\t"
            "s_cmp_eq_u32 %[cnt], 0\n\t"
            "s_cbranch_scc1 L_fin%=\n\t"
            "L_loop%=:\n\t"
            STAGE("v64", "v65", "v66", "v67", "v[64:65]", "v[66:67]", "v80", "v[88:89]", "v88", "v[76:79]", "v[86:87]", "16", "%c[le4]")
            "s_cbranch_scc1 L_fin%=\n\t"
            STAGE("v68", "v69", "v70", "v71", "v[68:69]", "v[70:71]", "v82", "v[90:91]", "v90", "v[64:67]", "v[80:81]", "20", "%c[le5]")
            "s_cbranch_scc1 L_fin%=\n\t"
            STAGE("v72", "v73", "v74", "v75", "v[72:73]", "v[74:75]", "v84", "v[92:93]", "v92", "v[68:71]", "v[82:83]", "24", "%c[le6]")
            "s_cbranch_scc1 L_fin%=\n\t"
            STAGE("v76", "v77", "v78", "v79", "v[76:77]", "v[78:79]", "v86", "v[94:95]", "v94", "v[72:75]", "v[84:85]", "28", "%c[le7]")
            "v_add_u32 v89, 16, v89\n\t"
            "s_cbranch_scc0 L_loop%=\n\t"
            "L_fin%=:\n\t"
            "v_pk_add_f32 v[108:109], v[108:109], %[eps]\n\t"
            "v_pk_add_f32 v[110:111], v[110:111], %[eps]\n\t"
            "v_rcp_f32 v108, v108\n\t"
            "v_rcp_f32 v109, v109\n\t"
            "v_rcp_f32 v110, v110\n\t"
            "v_rcp_f32 v111, v111\n\t"
            "v_pk_mul_f32 v[104:105], v[104:105], v[108:109]\n\t"
            "v_pk_mul_f32 v[106:107], v[106:107], v[110:111]\n\t"
            "v_dot2c_f32_bf16 v104, %[shi], v116\n\t"
            "v_dot2c_f32_bf16 v105, %[shi], v117\n\t"
            "v_dot2c_f32_bf16 v106, %[shi], v118\n\t"
            "v_dot2c_f32_bf16 v107, %[shi], v119\n\t"
            "v_maximum3_f32 v104, v104, 0, 0\n\t"
            "v_maximum3_f32 v105, v105, 0, 0\n\t"
            "v_maximum3_f32 v106, v106, 0, 0\n\t"
            "v_maximum3_f32 v107, v107, 0, 0\n\t"
            "v_dot2c_f32_bf16 v104, %[slo], v120\n\t"
            "v_dot2c_f32_bf16 v105, %[shi], v120\n\t"
            "v_dot2c_f32_bf16 v106, %[slo], v121\n\t"
            "v_dot2c_f32_bf16 v107, %[shi], v121\n\t"
            "s_nop 1\n\t"
            "v_cvt_pk_bf16_f32 v112, v104, v105\n\t"
            "v_cvt_pk_bf16_f32 v113, v106, v107\n\t"
            "buffer_store_dwordx2 v[112:113], v124, %[o], 0 offen\n\t"
            PRIO_STEP
            "s_cmp_lt_u32 %[nxt], %[ntask]\n\t"
            "s_cbranch_scc1 L_task%=\n\t"
            "s_waitcnt vmcnt(0) lgkmcnt(0)"
            : [cnt] "=&s"(s_cnt), [nxt] "=&s"(s_nxt), [t] "=&s"(s_t), [tc] "=&s"(s_tc)
            : "{v81}"(joff), "{v83}"(joff), "{v85}"(joff), "{v87}"(joff), "{v91}"(g8), "{v93}"(ctr), "{v95}"(one), "{v126}"(j8),
              "{v[100:101]}"(w01), "{v[102:103]}"(w23),
              [task] "s"(w), [ntask] "s"(ntask), [bv] "s"(srdBV), [o] "s"(srdO), [slo] "s"(0x00003F80u), [shi] "s"(0x3F800000u), [eps] "s"(eps2),
              [au] "i"(K2_AU), [le0] "i"(K2_LSZ), [le1] "i"(K2_LSZ + 4), [le2] "i"(K2_LSZ + 8), [le3] "i"(K2_LSZ + 12),
              [le4] "i"(K2_LSZ + 16), [le5] "i"(K2_LSZ + 20), [le6] "i"(K2_LSZ + 24), [le7] "i"(K2_LSZ + 28)
            : "v64", "v65", "v66", "v67", "v68", "v69", "v70", "v71", "v72", "v73", "v74", "v75", "v76", "v77", "v78", "v79",
              "v80", "v82", "v84", "v86", "v88", "v89", "v90", "v92", "v94", "v96", "v97", "v98", "v99",
              "v104", "v105", "v106", "v107", "v108", "v109", "v110", "v111", "v112", "v113", "v114", "v115",
              "v116", "v117", "v118", "v119", "v120", "v121", "v122", "v123", "v124", "v125", "v127", "scc", "memory");
#undef STAGE
    }
    STAMP(10);
    STAMPC(15);
    SPAN1(1);
    if (blockIdx.x == 0 && w == 0) {
        const unsigned long long* sp = (const unsigned long long*)((const char*)ws + (192u << 20));
        const unsigned long long* mk = (const unsigned long long*)((const char*)ws + (200u << 20));
        unsigned long long mn = ~0ull, mx = 0, m0 = 0, m1 = 0;
        for (int i = lane; i < 256 * 8; i += 64) { const int b2 = i >> 3, w2 = i & 7;
            const unsigned long long a = sp[(b2 * 16 + w2) * 2], e = sp[(b2 * 16 + w2) * 2 + 1];
            mn = a < mn ? a : mn; mx = e > mx ? e : mx;
            const unsigned long long k0 = mk[b2 * 16 + w2], k1 = mk[(256 + b2) * 16 + w2];
            m0 = k0 > m0 ? k0 : m0; m1 = k1 > m1 ? k1 : m1; }
        for (int o = 32; o; o >>= 1) { unsigned long long t;
            t = __shfl_xor(mn, o); mn = t < mn ? t : mn; t = __shfl_xor(mx, o); mx = t > mx ? t : mx;
            t = __shfl_xor(m0, o); m0 = t > m0 ? t : m0; t = __shfl_xor(m1, o); m1 = t > m1 ? t : m1; }
        const unsigned long long k2s = span_t0_, k2a = mk[(2 * 256) * 16], k2b = mk[(3 * 256) * 16];
        unsigned long long val = 0;
        if (MEASURE == 1) val = mx - mn;
        if (MEASURE == 2) val = k2s - mx;
        if (MEASURE == 3) val = m0 - mn;
        if (MEASURE == 4) val = m1 - mn;
        if (MEASURE == 5) val = k2a - k2s;
        if (MEASURE == 6) val = k2b - k2s;
        if (MEASURE == 9) val = ((const unsigned long long*)((const char*)ws + (208u << 20)))[0] / 20;
        if (MEASURE == 10) val = ((const unsigned long long*)((const char*)ws + (208u << 20)))[1];
        if (MEASURE == 8) val = (__builtin_amdgcn_s_memtime() - cyc0_) / 20;
        if (MEASURE == 7) val = __builtin_amdgcn_s_memrealtime() - k2s;
        const unsigned long long t0 = __builtin_amdgcn_s_memrealtime();
        while (__builtin_amdgcn_s_memrealtime() - t0 < val * 10ull) __builtin_amdgcn_s_sleep(2);
    }
}

extern "C" void kernel_launch(void* const* d_in, const int* in_sizes, int n_in,
                              void* d_out, int out_size, void* d_ws, size_t ws_size,
                              hipStream_t stream) {
    const u16* x = (const u16*)d_in[0];
    const u16* ea = (const u16*)d_in[1];
    const int* src = (const int*)d_in[2];
    const int* dst = (const int*)d_in[3];
    const u16* wa = (const u16*)d_in[4];
    const u16* wb = (const u16*)d_in[5];
    const u16* wc = (const u16*)d_in[6];
    const u16* wu = (const u16*)d_in[7];
    const u16* wv = (const u16*)d_in[8];
    (void)hipFuncSetAttribute((const void*)k1_gemm_bin, hipFuncAttributeMaxDynamicSharedMemorySize, K1_LDS);
    k1_gemm_bin<<<256, 512, K1_LDS, stream>>>(x, wa, wb, wu, wv, src, dst, ea, (char*)d_ws);
    (void)hipFuncSetAttribute((const void*)k2_edges, hipFuncAttributeMaxDynamicSharedMemorySize, K2_LDS);
    k2_edges<<<256, 1024, K2_LDS, stream>>>(x, wc, src, dst, ea, (const char*)d_ws, (u16*)d_out);
}
```

```cpp
#include <hip/hip_runtime.h>
#include <hip/hip_bf16.h>
#include <stdint.h>

typedef unsigned short u16;
typedef unsigned int u32;
typedef u32 u32x2 __attribute__((ext_vector_type(2)));
typedef u32 u32x4 __attribute__((ext_vector_type(4)));
typedef float f32x2 __attribute__((ext_vector_type(2)));
typedef float f32x4 __attribute__((ext_vector_type(4)));
typedef float f32x16 __attribute__((ext_vector_type(16)));
typedef short s16x4 __attribute__((ext_vector_type(4)));
typedef short s16x8 __attribute__((ext_vector_type(8)));
typedef __bf16 bf16x8 __attribute__((ext_vector_type(8)));
typedef __bf16 bf16x4 __attribute__((ext_vector_type(4)));
typedef __bf16 bf16x2 __attribute__((ext_vector_type(2)));

constexpr int NN = 10000;
constexpr int NE = 160000;
constexpr int DD = 256;
constexpr int NP = 10048;
constexpr int RG = 320;
constexpr int NBK = 64;
constexpr int NB = 157;
constexpr int NCH = 64;
constexpr int CHE = 2500;
constexpr int CAP = 80;
constexpr int CAPN = 44;
constexpr int LST = 45;
constexpr float NLOG2E = -1.4426950408889634f;
constexpr u32 NEUTRAL_REC = (u32)NN;
constexpr u32 NEUTRAL_BV = 0x00007F00u;

constexpr size_t OFF_BV = 0;
constexpr size_t OFF_AU = OFF_BV + (size_t)4 * NP * 256;
constexpr size_t OFF_R4 = OFF_AU + (size_t)4 * NP * 256;
constexpr size_t OFF_R2 = OFF_R4 + (size_t)NBK * NCH * CAP * 4;
constexpr size_t OFF_CNT = OFF_R2 + (size_t)NBK * NCH * CAP * 2;

#define LDS_AS __attribute__((address_space(3)))
#define GLB_AS __attribute__((address_space(1)))

__device__ __forceinline__ void glds16(const void* g, void* l) {
    __builtin_amdgcn_global_load_lds((const GLB_AS u32*)g, (LDS_AS u32*)l, 16, 0, 0);
}
__device__ __forceinline__ void glds16a(const void* sbase, u32 voff, u32 lds) {
    u32 keep;
    asm volatile("s_mov_b32 %0, m0\n\ts_mov_b32 m0, %3\n\ts_nop 0\n\tglobal_load_lds_dwordx4 %1, %2\n\ts_mov_b32 m0, %0" : "=&s"(keep) : "v"(voff), "s"(sbase), "s"(lds) : "memory");
}
__device__ __forceinline__ u32 cvt_pk_bf16(float lo, float hi) {
    const f32x2 v = {lo, hi}; return __builtin_bit_cast(u32, __builtin_convertvector(v, bf16x2));
}
#define WAIT_V(n) asm volatile("s_waitcnt vmcnt(" #n ")" ::: "memory")
#define WAIT_L(n) asm volatile("s_waitcnt lgkmcnt(" #n ")" ::: "memory")
#define BAR() __builtin_amdgcn_s_barrier()
#define EXIT2_AT 99
#ifndef EXIT_AT
#define EXIT_AT 99
#endif
#define STORE_AUX 16
#define SPAN0()
#define SPAN1(K)
#define STAMP(i)
#define STAMPC(i)
#define TICK(v)

constexpr int K1_R0 = 0, K1_R1 = 65536, K1_SCR = 131072;
constexpr int K1_ST4 = K1_SCR, K1_ST2 = K1_ST4 + NBK * CAP * 4, K1_BC = K1_ST2 + NBK * CAP * 2;
constexpr int K1_LDS = K1_SCR + 32768;
static_assert(K1_BC + NBK * 4 <= K1_LDS, "lds");

__global__ __launch_bounds__(512, 2) void k1_gemm_bin(
    const u16* __restrict__ x, const u16* __restrict__ wa, const u16* __restrict__ wb, const u16* __restrict__ wu, const u16* __restrict__ wv,
    const int* __restrict__ src, const int* __restrict__ dst, const u16* __restrict__ ea, char* __restrict__ ws) {
    extern __shared__ __attribute__((aligned(16))) char smem[];
    const int tid = threadIdx.x, lane = tid & 63;
    const int w = __builtin_amdgcn_readfirstlane(tid >> 6);
    const int b = blockIdx.x;
    const int type = (b >> 5) & 1;
    const int ct2 = (b >> 3) & 3;
    const int rg = (b & 7) * 4 + (b >> 6);
    const int row0 = rg * RG;
    const int chunk = (b & 31) | ((b >> 6) << 5) ;
    const bool binner = type == 1 && b < 128;
    constexpr int KOFF = 0;
    SPAN0();
#if EXIT_AT == 0
    if (ws) { SPAN1(0); return; }
#endif
    STAMP(0);
    int bd[5], bs[5]; u32 be[5];
    if (binner) {
#pragma unroll
        for (int i = 0; i < 5; ++i) {
            const int k = tid + 512 * i;
            const int e = chunk * CHE + (k < CHE ? k : 0);
            bd[i] = dst[e]; bs[i] = src[e]; be[i] = ea[e];
        }
        if (tid < NBK) *(u32*)(smem + K1_BC + tid * 4) = 0;
    }
    {
        const int m = w >> 2;
        const u16* W = type == 0 ? (m == 0 ? wb : wv) : (m == 0 ? wa : wu);
        const int kb0 = (w & 3) * 64, kr = lane >> 3;
        const u16* g = W + (size_t)(kb0 + kr) * DD + ct2 * 64 + (((lane & 7) ^ ((kr & 2) << 1)) << 3);
#pragma unroll
        for (int i = 0; i < 8; ++i) glds16(g + (size_t)i * 8 * DD, smem + K1_R0 + m * 32768 + (kb0 / 8 + i) * 1024);
    }
    const int xr = lane >> 5, xp = lane & 31;
    auto stage_x = [&](int tile, int q, int ldsbase) {
        const int r = q * 2 + xr;
        int grow = row0 + tile * 32 + r; grow = grow < NN ? grow : NN - 1;
        glds16((const char*)x + (u32)(grow * 512 + ((xp ^ (r & 15)) << 4)), smem + ldsbase + q * 1024);
    };
#pragma unroll
    for (int i = 0; i < 8; ++i) { const int q = w * 8 + i; stage_x(q >> 4, q & 15, K1_R1 + (q >> 4) * 16384); }
    STAMP(1);
    WAIT_V(8); WAIT_L(0);
    BAR();
    STAMP(2);
#if EXIT_AT == 1
    if (ws) { SPAN1(0); return; }
#endif
    const int ch = w >> 2;
    bf16x8 wf0[16], wf1[16];
    {
        const int h = lane >> 5, blk = (lane >> 4) & 1, q = (lane & 15) >> 2, p = lane & 3;
        const u32 base = K1_R0 + (8 * h + q) * 128 + (((ch * 4 + blk * 2 + (p >> 1)) ^ ((q & 2) << 1)) << 4) + (p & 1) * 8;
#define TRRD(S) { u32x2 a0, a1, b0, b1; \
            asm volatile("ds_read_b64_tr_b16 %0, %4 offset:%c5\n\tds_read_b64_tr_b16 %1, %4 offset:%c6\n\t" \
                         "ds_read_b64_tr_b16 %2, %4 offset:%c7\n\tds_read_b64_tr_b16 %3, %4 offset:%c8" \
                         : "=&v"(a0), "=&v"(a1), "=&v"(b0), "=&v"(b1) \
                         : "v"(base), "i"((S) * 2048), "i"((S) * 2048 + 512), "i"(32768 + (S) * 2048), "i"(32768 + (S) * 2048 + 512) : "memory"); \
            wf0[S] = __builtin_bit_cast(bf16x8, __builtin_shufflevector(a0, a1, 0, 1, 2, 3)); \
            wf1[S] = __builtin_bit_cast(bf16x8, __builtin_shufflevector(b0, b1, 0, 1, 2, 3)); }
        TRRD(0) TRRD(1) TRRD(2) TRRD(3) TRRD(4) TRRD(5) TRRD(6) TRRD(7) TRRD(8) TRRD(9) TRRD(10) TRRD(11) TRRD(12) TRRD(13) TRRD(14) TRRD(15)
#undef TRRD
    }
    if (binner) {
#pragma unroll
        for (int i = 0; i < 5; ++i) {
            const int k = tid + 512 * i;
            if (k < CHE) {
                const int bk = (int)(((u32)bd[i] * 53432u) >> 23);
                const int dl = bd[i] - bk * NB;
                const u32 pos = atomicAdd((u32*)(smem + K1_BC + bk * 4), 1u);
                if (pos < CAP) {
                    *(u32*)(smem + K1_ST4 + (bk * CAP + pos) * 4) = (u32)bs[i] | (be[i] << 16);
                    *(u16*)(smem + K1_ST2 + (bk * CAP + pos) * 2) = (u16)dl;
                }
            }
        }
    }
    STAMP(3);
    WAIT_V(0); WAIT_L(0);
    __builtin_amdgcn_sched_barrier(0);
    BAR();
    STAMP(4);
#if EXIT_AT == 2
    if (ws) { asm volatile("" :: "v"(wf0[0]), "v"(wf1[15])); SPAN1(0); return; }
#endif
#pragma unroll
    for (int i = 0; i < 8; ++i) { const int q = w * 8 + i; stage_x(4 + (q >> 4), q & 15, K1_R0 + (q >> 4) * 16384); }
    if (binner) {
        u32* R4 = (u32*)(ws + OFF_R4); u16* R2 = (u16*)(ws + OFF_R2); u32* CNT = (u32*)(ws + OFF_CNT);
        u32x4 t4[3]; u32x2 t2[3];
#pragma unroll
        for (int i = 0; i < 3; ++i) { const int q = tid + 512 * i; const int qq = q < NBK * (CAP / 4) ? q : 0;
            t4[i] = *(const u32x4*)(smem + K1_ST4 + qq * 16); t2[i] = *(const u32x2*)(smem + K1_ST2 + qq * 8); }
        const u32 cv = *(const u32*)(smem + K1_BC + (tid & 63) * 4);
#pragma unroll
        for (int i = 0; i < 3; ++i) { const int q = tid + 512 * i;
            if (q < NBK * (CAP / 4)) { const int bk = q / (CAP / 4), o = q - bk * (CAP / 4);
                *(u32x4*)(R4 + ((size_t)bk * NCH + chunk) * CAP + o * 4) = t4[i];
                *(u32x2*)(R2 + ((size_t)bk * NCH + chunk) * CAP + o * 4) = t2[i]; } }
        if (tid < NBK) CNT[tid * NCH + chunk] = cv;
        WAIT_L(0);
        BAR();
    }
    if (rg == 0 && type == 0 && w == 0) ((u32*)(ws + OFF_BV))[((size_t)ct2 * NP + NN) * 64 + lane] = NEUTRAL_BV;

    const int r = lane & 31, h = lane >> 5;
    const int abase = r * 512 + ((((r & 14) | ((h ^ r) & 1))) << 4);
    char* const scr = smem + K1_SCR + w * 4096;
    const __amdgpu_buffer_rsrc_t trs = __builtin_amdgcn_make_buffer_rsrc(ws + (type ? OFF_AU : OFF_BV) + (size_t)ct2 * NP * 256, 0, NN * 256, 0x00020000);
    const u32 svo = (lane >> 3) * 256 + ch * 128 + (lane & 7) * 16;

    const int s4 = w & 3;
    if (ch == 0) __builtin_amdgcn_s_setprio(1);
    STAMP(5);
#pragma unroll 1
    for (int st = 0; st < (EXIT_AT == 3 ? 1 : (EXIT_AT == 4 ? 2 : 3)); ++st) {
        int tile, ldsbase;
        if (st == 0) { tile = s4; ldsbase = K1_R1 + s4 * 16384; }
        else if (st == 1) { tile = 4 + s4; ldsbase = K1_R0 + s4 * 16384; }
        else { const int t = (w < 2) ? w : (w >= 6 ? w - 6 : 99); tile = 8 + t; ldsbase = K1_R1 + (t & 1) * 16384; }
        const int trow = row0 + tile * 32;
        const bool active = tile < 10 && trow < NN;
        unsigned long long tkA = 0, tkB = 0, tkC = 0, tkD = 0;
        if (active) {
            TICK(tkA);
            bf16x8 a[16];
#pragma unroll
            for (int s = 0; s < 8; ++s) a[s] = *(const bf16x8*)(smem + ldsbase + (abase ^ (s << 5)));
            __builtin_amdgcn_sched_barrier(0);
            f32x16 acc0, acc1;
#pragma unroll
            for (int i = 0; i < 16; ++i) { acc0[i] = 0.f; acc1[i] = 0.f; }
#pragma unroll
            for (int s = 0; s < 8; ++s) {
                a[s + 8] = *(const bf16x8*)(smem + ldsbase + (abase ^ (s << 5)) + 256);
                acc0 = __builtin_amdgcn_mfma_f32_32x32x16_bf16(a[s], wf0[s], acc0, 0, 0, 0);
                acc1 = __builtin_amdgcn_mfma_f32_32x32x16_bf16(a[s], wf1[s], acc1, 0, 0, 0);
                __builtin_amdgcn_sched_group_barrier(0x100, 1, 0);
                __builtin_amdgcn_sched_group_barrier(0x8, 2, 0);
            }
            __builtin_amdgcn_sched_barrier(0);
#pragma unroll
            for (int s = 8; s < 16; ++s) {
                acc0 = __builtin_amdgcn_mfma_f32_32x32x16_bf16(a[s], wf0[s], acc0, 0, 0, 0);
                acc1 = __builtin_amdgcn_mfma_f32_32x32x16_bf16(a[s], wf1[s], acc1, 0, 0, 0);
            }
            TICK(tkB);
            char* const sw = scr + h * 512 + r * 4;
            if (type == 0) {
#pragma unroll
                for (int i = 0; i < 16; i += 2) {
                    const u32 bb = cvt_pk_bf16(acc0[i] * NLOG2E, acc0[i + 1] * NLOG2E);
#pragma unroll
                    for (int j = 0; j < 2; ++j) {
                        const u32 vb = __float_as_uint(acc1[i + j]);
                        const u32 bl = j ? (bb >> 16) : (bb & 0xFFFFu);
                        const u32 u = __builtin_elementwise_sub_sat((vb & 0x7FFFFFFFu) + 0x8000u, bl);
                        *(u32*)(sw + (((i + j) & 3) + 8 * ((i + j) >> 2)) * 128) = (vb & 0x80000000u) | (u & 0x7FFF0000u) | bl;
                    }
                }
            } else {
#pragma unroll
                for (int i = 0; i < 16; ++i) *(u32*)(sw + ((i & 3) + 8 * (i >> 2)) * 128) = cvt_pk_bf16(acc0[i] * NLOG2E, acc1[i]);
            }
            TICK(tkC);
#pragma unroll
            for (int i = 0; i < 4; ++i) {
                const u32x4 v = *(const u32x4*)(scr + i * 1024 + lane * 16);
                __builtin_amdgcn_raw_buffer_store_b128(v, trs, svo + (u32)(trow + i * 8) * 256, 0, STORE_AUX);
            }
        }
        TICK(tkD);
        if (st == 0) STAMP(6); else if (st == 1) STAMP(8); else STAMP(10);
        if (st < 2) {
            if (active) WAIT_V(4); else WAIT_V(0);
            BAR();
            if (st == 0) {
#pragma unroll
                for (int i = 0; i < 4; ++i) { const int q = w * 4 + i; stage_x(8 + (q >> 4), q & 15, K1_R1 + (q >> 4) * 16384); }
            }
        }
    }
    SPAN1(0);
}

constexpr int LSTB = LST * 4;
constexpr int K2_X = 0;
constexpr int K2_AU = K2_X + 20480;
constexpr int K2_LIDX = K2_AU + 40960;
constexpr int K2_LSZ = (NB * LSTB + 32 + 256 + 15) / 16 * 16;
constexpr int K2_LE = K2_LIDX + K2_LSZ;
constexpr int K2_CNT = K2_LE + K2_LSZ;
constexpr int K2_HIST = K2_CNT + 160 * 4;
constexpr int K2_ORD = K2_HIST + 128 * 4;
constexpr int K2_MISC = K2_ORD + 80 * 32;
constexpr int K2_LDS = K2_MISC + 16;
constexpr int K2_TRASH = NB * LSTB + 32;
static_assert(K2_TRASH + 256 <= K2_LSZ, "trash");
static_assert(K2_LSZ < 65536 - 64, "ds offset");

__device__ __forceinline__ float dot2sel(u32 d, u32 sel, float c) {
    return __builtin_amdgcn_fdot2_f32_bf16(__builtin_bit_cast(bf16x2, d), __builtin_bit_cast(bf16x2, sel), c, false);
}

struct Acc { f32x2 n01, n23, d01, d23; };

__device__ __forceinline__ void edge_step(Acc& a, const u32x4 d, const u32 rec, const f32x2 a01, const f32x2 a23, const f32x2 w01, const f32x2 w23, const u32 sel) {
    const float e = __uint_as_float(rec & 0xFFFF0000u);
    const f32x2 ee = {e, e};
    const f32x2 t01 = __builtin_elementwise_fma(ee, w01, a01);
    const f32x2 t23 = __builtin_elementwise_fma(ee, w23, a23);
    f32x2 E01, E23;
    E01.x = __builtin_amdgcn_exp2f(dot2sel(d.x, sel, t01.x));
    E01.y = __builtin_amdgcn_exp2f(dot2sel(d.y, sel, t01.y));
    E23.x = __builtin_amdgcn_exp2f(dot2sel(d.z, sel, t23.x));
    E23.y = __builtin_amdgcn_exp2f(dot2sel(d.w, sel, t23.y));
    const f32x2 one = {1.f, 1.f};
    E01 += one; E23 += one;
    f32x2 s01, s23;
    s01.x = __builtin_amdgcn_rcpf(E01.x); s01.y = __builtin_amdgcn_rcpf(E01.y);
    s23.x = __builtin_amdgcn_rcpf(E23.x); s23.y = __builtin_amdgcn_rcpf(E23.y);
    a.d01 += s01; a.d23 += s23;
    const f32x2 v01 = {__uint_as_float(d.x), __uint_as_float(d.y)};
    const f32x2 v23 = {__uint_as_float(d.z), __uint_as_float(d.w)};
    a.n01 = __builtin_elementwise_fma(s01, v01, a.n01);
    a.n23 = __builtin_elementwise_fma(s23, v23, a.n23);
}

__device__ __forceinline__ void node_finish(const Acc& a, const u32x4 au, const u32x2 xv, u16* outp) {
    const float num[4] = {a.n01.x, a.n01.y, a.n23.x, a.n23.y};
    const float den[4] = {a.d01.x, a.d01.y, a.d23.x, a.d23.y};
    const u32 aus[4] = {au.x, au.y, au.z, au.w};
    const float xs[4] = {__uint_as_float(xv.x << 16), __uint_as_float(xv.x & 0xFFFF0000u), __uint_as_float(xv.y << 16), __uint_as_float(xv.y & 0xFFFF0000u)};
    float o[4];
#pragma unroll
    for (int c = 0; c < 4; ++c) {
        const float z = __uint_as_float(aus[c] & 0xFFFF0000u) + num[c] * __builtin_amdgcn_rcpf(den[c] + 1e-6f);
        const float rl = z > 0.f ? z : 0.f * z;
        o[c] = xs[c] + rl;
    }
    u32x2 ov; ov.x = cvt_pk_bf16(o[0], o[1]); ov.y = cvt_pk_bf16(o[2], o[3]);
    *(u32x2*)outp = ov;
}

template <int CTRL, int RM> __device__ __forceinline__ u32 dpp_add(u32 v) {
    return v + (u32)__builtin_amdgcn_update_dpp(0, (int)v, CTRL, RM, 0xf, false);
}

__global__ __launch_bounds__(1024, 4) void k2_edges(
    const u16* __restrict__ x, const u16* __restrict__ wc, const int* __restrict__ src, const int* __restrict__ dst, const u16* __restrict__ ea,
    const char* __restrict__ ws, u16* __restrict__ out) {
    extern __shared__ __attribute__((aligned(16))) char smem[];
    const int tid = threadIdx.x, lane = tid & 63;
    const int w = __builtin_amdgcn_readfirstlane(tid >> 6);
    const int b = blockIdx.x;
    const int slice = (b & 7) >> 1;
    const int bucket = (b >> 3) * 2 + (b & 1);
    const int node0 = bucket * NB;
    const int nvalid = (NN - node0) < NB ? (NN - node0) : NB;
    u32* const cnt = (u32*)(smem + K2_CNT);
    u32* const hist = (u32*)(smem + K2_HIST);
    u32* const misc = (u32*)(smem + K2_MISC);
    constexpr int KOFF = 256 * 16;
    SPAN0();
    STAMP(0);

    const u32* R4 = (const u32*)(ws + OFF_R4) + (size_t)bucket * NCH * CAP;
    const u16* R2 = (const u16*)(ws + OFF_R2) + (size_t)bucket * NCH * CAP;
    const u32* CNT = (const u32*)(ws + OFF_CNT) + bucket * NCH;
    const int ch0 = tid / (CAP / 4), ch1 = (tid + 1024) / (CAP / 4);
    const u32x4 r40 = *(const u32x4*)(R4 + tid * 4);
    const u32x2 r20 = *(const u32x2*)(R2 + tid * 4);
    const u32 c0 = CNT[ch0];
    u32x4 r41 = {0, 0, 0, 0}; u32x2 r21 = {0, 0}; u32 c1 = 0;
    if (w < 4) { r41 = *(const u32x4*)(R4 + (tid + 1024) * 4); r21 = *(const u32x2*)(R2 + (tid + 1024) * 4); c1 = CNT[ch1]; }
    const char* const bvb = ws + OFF_BV + (size_t)slice * NP * 256;
    const char* const aub = ws + OFF_AU + (size_t)slice * NP * 256;
    const int g = lane >> 4, j = lane & 15;
    const u32x2 wcv = *(const u32x2*)(wc + slice * 64 + j * 4);
    for (int i = tid; i < K2_LSZ / 16; i += 1024) {
        *(u32x4*)(smem + K2_LIDX + i * 16) = (u32x4){NEUTRAL_REC, NEUTRAL_REC, NEUTRAL_REC, NEUTRAL_REC};
        *(u32x4*)(smem + K2_LE + i * 16) = (u32x4){0, 0, 0, 0};
    }
    if (tid < 160 + 64) cnt[tid] = 0;
    if (tid == 0) { misc[0] = 16; misc[1] = 0; }
    f32x2 w01 = {__uint_as_float(wcv.x << 16) * NLOG2E, __uint_as_float(wcv.x & 0xFFFF0000u) * NLOG2E};
    f32x2 w23 = {__uint_as_float(wcv.y << 16) * NLOG2E, __uint_as_float(wcv.y & 0xFFFF0000u) * NLOG2E};
    asm volatile("" : "+v"(w01), "+v"(w23));
    STAMP(1);
    WAIT_V(0); WAIT_L(0); BAR();
    {
        const char* ga = aub + (size_t)node0 * 256;
#pragma unroll
        for (int i = 0; i < 3; ++i) { const int q = w + 16 * i; if (q < 40) glds16a(ga, q * 1024 + lane * 16, K2_AU + q * 1024); }
        const char* gx = (const char*)x + slice * 128;
#pragma unroll
        for (int i = 0; i < 2; ++i) {
            const int q = w + 16 * i;
            if (q < 20) { int row = node0 + q * 8 + (lane >> 3); row = row < NN ? row : NN - 1;
                glds16a(gx, row * 512 + (lane & 7) * 16, K2_X + q * 1024); }
        }
    }
    STAMP(2);
    {
        const int p0 = (tid - ch0 * (CAP / 4)) * 4;
        const u32 rec0[4] = {r40.x, r40.y, r40.z, r40.w};
        const u32 dl0[4] = {r20.x & 0xFFFF, r20.x >> 16, r20.y & 0xFFFF, r20.y >> 16};
        u32 slot[4];
        bool ovf = c0 > CAP;
#pragma unroll
        for (int k = 0; k < 4; ++k) slot[k] = atomicAdd(&cnt[p0 + k < c0 ? dl0[k] : 224 + lane], 1u);
        if (w < 4) {
            const int p1 = (tid + 1024 - ch1 * (CAP / 4)) * 4;
            const u32 rec1[4] = {r41.x, r41.y, r41.z, r41.w};
            const u32 dl1[4] = {r21.x & 0xFFFF, r21.x >> 16, r21.y & 0xFFFF, r21.y >> 16};
            u32 slot1[4];
            ovf |= c1 > CAP;
#pragma unroll
            for (int k = 0; k < 4; ++k) slot1[k] = atomicAdd(&cnt[p1 + k < c1 ? dl1[k] : 224 + lane], 1u);
#pragma unroll
            for (int k = 0; k < 4; ++k) {
                const bool v = p1 + k < c1, ok = v && slot1[k] < CAPN;
                ovf |= v && !ok;
                const u32 o = ok ? dl1[k] * LSTB + slot1[k] * 4 : K2_TRASH + lane * 4;
                *(u32*)(smem + K2_LIDX + o) = rec1[k] & 0xFFFFu;
                *(u32*)(smem + K2_LE + o) = rec1[k] & 0xFFFF0000u;
            }
        }
#pragma unroll
        for (int k = 0; k < 4; ++k) {
            const bool v = p0 + k < c0, ok = v && slot[k] < CAPN;
            ovf |= v && !ok;
            const u32 o = ok ? dl0[k] * LSTB + slot[k] * 4 : K2_TRASH + lane * 4;
            *(u32*)(smem + K2_LIDX + o) = rec0[k] & 0xFFFFu;
            *(u32*)(smem + K2_LE + o) = rec0[k] & 0xFFFF0000u;
        }
        if (ovf) misc[1] = 1;
    }
    STAMP(3);
    WAIT_L(0); BAR();
    STAMP(4);
    const bool slow = misc[1] != 0;

    const u32 joff = j * 16;

    if (slow) {
        u32 sel = 0x00003F80u;
        asm volatile("" : "+s"(sel));
        for (int task = w; task * 4 < nvalid; task += 16) {
            const int n = task * 4 + g;
            const bool nv = n < nvalid;
            const int node = node0 + (nv ? n : 0);
            const u32x4 au = *(const u32x4*)(aub + (size_t)node * 256 + joff);
            const u32x2 xv = *(const u32x2*)(x + (size_t)node * DD + slice * 64 + j * 4);
            const f32x2 a01 = {__uint_as_float(au.x << 16), __uint_as_float(au.y << 16)};
            const f32x2 a23 = {__uint_as_float(au.z << 16), __uint_as_float(au.w << 16)};
            Acc acc = {{0.f, 0.f}, {0.f, 0.f}, {0.f, 0.f}, {0.f, 0.f}};
            const int lo = node0 + task * 4;
            for (int e0 = 0; e0 < NE; e0 += 64) {
                const int d = dst[e0 + lane];
                unsigned long long m = __ballot((u32)(d - lo) < 4u);
                if (m) {
                    const u32 rc = (u32)src[e0 + lane] | ((u32)ea[e0 + lane] << 16);
                    while (m) {
                        const int l = __builtin_ctzll(m); m &= m - 1;
                        const int dd = __builtin_amdgcn_readlane(d, l);
                        const u32 rr = __builtin_amdgcn_readlane(rc, l);
                        const u32 rec = (nv && dd == node) ? rr : NEUTRAL_REC;
                        const u32x4 dv = *(const u32x4*)(bvb + (size_t)((rec & 0xFFFFu) * 256 + joff));
                        edge_step(acc, dv, rec, a01, a23, w01, w23, sel);
                    }
                }
            }
            if (nv) node_finish(acc, au, xv, out + (size_t)node * DD + slice * 64 + j * 4);
        }
        return;
    }

    u32 mydeg = 0, myrank = 0;
    if (tid < nvalid) { mydeg = cnt[tid]; myrank = atomicAdd(&hist[63 - mydeg], 1u); }
    WAIT_L(0); BAR();
    if (w < 3) {
        const u32 h0 = hist[lane];
        u32 v = h0;
        v = dpp_add<0x111, 0xf>(v); v = dpp_add<0x112, 0xf>(v); v = dpp_add<0x114, 0xf>(v); v = dpp_add<0x118, 0xf>(v);
        v = dpp_add<0x142, 0xa>(v); v = dpp_add<0x143, 0xc>(v);
        const u32 base = (u32)__builtin_amdgcn_ds_bpermute((int)((63 - mydeg) * 4), (int)(v - h0));
        if (tid < nvalid) {
            const u32 pos = base + myrank;
            const u32x2 ent = {(u32)(K2_LIDX + tid * LSTB), (u32)(tid * 256) + mydeg};
            u32x2* o = (u32x2*)(smem + K2_ORD) + pos;
            o[0] = ent;
            if (pos == (u32)nvalid - 1) { o[1] = ent; o[2] = ent; o[3] = ent; }
        }
    }
    WAIT_V(0); WAIT_L(0); BAR();
    STAMP(5);
    STAMPC(14);

    const int ntask = (nvalid + 3) >> 2;
    if (w < ntask) {
        u32x4 srdBV, srdO;
        { const size_t p = (size_t)bvb; srdBV = (u32x4){(u32)p, (u32)(p >> 32) | (256u << 16), 0xFFFFFFFFu, 0x00020000u}; }
        { const size_t ob = (size_t)node0 * 512 + slice * 128; const size_t p = (size_t)out + ob; srdO = (u32x4){(u32)p, (u32)(p >> 32), (u32)((size_t)NN * 512 - ob), 0x00020000u}; }
        u32 g8 = K2_ORD + g * 8, ctr = K2_MISC, one = 1, j8 = j * 8;
        unsigned long long eps2 = 0x358637BD358637BDull;
        u32 s_cnt, s_nxt, s_t, s_tc;
#define STAGE(DS0, DS1, DS2, DS3, DSL, DSH, IX, EP, E1, DN, IXN, O1, O2) \
            "s_waitcnt lgkmcnt(0)\n\t" \
            "buffer_load_dwordx4 " DN ", " GIX(IXN) ", %[bv], 0 idxen offen\n\t" \
            "v_pk_fma_f32 v[112:113], " EP ", v[100:101], v[96:97] op_sel_hi:[0,1,1]\n\t" \
            "v_pk_fma_f32 v[114:115], " EP ", v[102:103], v[98:99] op_sel_hi:[0,1,1]\n\t" \
            "ds_read_b32 " IX ", v89 offset:" O1 "\n\t" \
            "ds_read_b32 " E1 ", v89 offset:" O2 "\n\t" \
            "s_waitcnt vmcnt(3)\n\t" \
            "v_dot2c_f32_bf16 v112, %[slo], " DS0 "\n\t" \
            "v_dot2c_f32_bf16 v113, %[slo], " DS1 "\n\t" \
            "v_dot2c_f32_bf16 v114, %[slo], " DS2 "\n\t" \
            "v_dot2c_f32_bf16 v115, %[slo], " DS3 "\n\t" \
            "v_exp_f32 v112, v112\n\t" \
            "v_exp_f32 v113, v113\n\t" \
            "v_exp_f32 v114, v114\n\t" \
            "v_exp_f32 v115, v115\n\t" \
            "v_pk_add_f32 v[112:113], v[112:113], 1.0 op_sel_hi:[1,0]\n\t" \
            "v_pk_add_f32 v[114:115], v[114:115], 1.0 op_sel_hi:[1,0]\n\t" \
            "v_rcp_f32 v112, v112\n\t" \
            "v_rcp_f32 v113, v113\n\t" \
            "v_rcp_f32 v114, v114\n\t" \
            "v_rcp_f32 v115, v115\n\t" \
            "s_sub_u32 %[cnt], %[cnt], 1\n\t" \
            "v_pk_add_f32 v[108:109], v[108:109], v[112:113]\n\t" \
            "v_pk_fma_f32 v[104:105], v[112:113], " DSL ", v[104:105]\n\t" \
            "v_pk_add_f32 v[110:111], v[110:111], v[114:115]\n\t" \
            "v_pk_fma_f32 v[106:107], v[114:115], " DSH ", v[106:107]\n\t" \
            "s_cmp_eq_u32 %[cnt], 0\n\t"
#define GIX(x) x
#define PRIO_ON 1
#define PRIO_INIT "s_setprio 3\n\t"
#define PRIO_STEP \
            "s_add_u32 %[tc], %[tc], 1\n\t" \
            "s_cmp_lg_u32 %[tc], 1\n\t s_cbranch_scc1 L_p1%=\n\t s_setprio 2\n\t L_p1%=:\n\t" \
            "s_cmp_lg_u32 %[tc], 2\n\t s_cbranch_scc1 L_p2%=\n\t s_setprio 1\n\t L_p2%=:\n\t" \
            "s_cmp_lg_u32 %[tc], 3\n\t s_cbranch_scc1 L_p3%=\n\t s_setprio 0\n\t L_p3%=:\n\t"
        asm volatile(
            PRIO_INIT
            "s_mov_b32 %[tc], 0\n\t"
            "s_lshl_b32 %[t], %[task], 5\n\t"
            "v_add_u32 v127, %[t], v91\n\t"
            "ds_read_b64 v[122:123], v127\n\t"
            "s_mov_b64 exec, 1\n\t"
            "ds_add_rtn_u32 v125, v93, v95\n\t"
            "s_mov_b64 exec, -1\n\t"
            "L_task%=:\n\t"
            "s_waitcnt lgkmcnt(0)\n\t"
            "v_readfirstlane_b32 %[nxt], v125\n\t"
            "v_readfirstlane_b32 %[cnt], v123\n\t"
            "v_mov_b32 v89, v122\n\t"
            "v_and_b32 v127, 0xffffff00, v123\n\t"
            "ds_read_b32 v80, v89\n\t"
            "ds_read_b32 v82, v89 offset:4\n\t"
            "ds_read_b32 v84, v89 offset:8\n\t"
            "ds_read_b32 v86, v89 offset:12\n\t"
            "ds_read_b32 v88, v89 offset:%c[le0]\n\t"
            "ds_read_b32 v90, v89 offset:%c[le1]\n\t"
            "ds_read_b32 v92, v89 offset:%c[le2]\n\t"
            "ds_read_b32 v94, v89 offset:%c[le3]\n\t"
            "v_add_u32 v112, v127, v81\n\t"
            "v_lshl_add_u32 v124, v127, 1, v126\n\t"
            "v_lshrrev_b32 v113, 1, v112\n\t"
            "ds_read_b128 v[116:119], v112 offset:%c[au]\n\t"
            "ds_read_b64 v[120:121], v113\n\t"
            "s_and_b32 %[cnt], %[cnt], 0xff\n\t"
            "s_lshl_b32 %[t], %[nxt], 5\n\t"
            "v_add_u32 v127, %[t], v91\n\t"
            "ds_read_b64 v[122:123], v127\n\t"
            "s_mov_b64 exec, 1\n\t"
            "ds_add_rtn_u32 v125, v93, v95\n\t"
            "s_mov_b64 exec, -1\n\t"
            "s_waitcnt lgkmcnt(9)\n\t"
            "buffer_load_dwordx4 v[64:67], v[80:81], %[bv], 0 idxen offen\n\t"
            "buffer_load_dwordx4 v[68:71], v[82:83], %[bv], 0 idxen offen\n\t"
            "buffer_load_dwordx4 v[72:75], v[84:85], %[bv], 0 idxen offen\n\t"
            "v_mov_b64 v[104:105], 0\n\t"
            "v_mov_b64 v[106:107], 0\n\t"
            "v_mov_b64 v[108:109], 0\n\t"
            "v_mov_b64 v[110:111], 0\n\t"
            "s_waitcnt lgkmcnt(2)\n\t"
            "v_lshlrev_b32 v96, 16, v116\n\t"
            "v_lshlrev_b32 v97, 16, v117\n\t"
            "v_lshlrev_b32 v98, 16, v118\n\t"
            "v_lshlrev_b32 v99, 16, v119\n\t"
            "s_cmp_eq_u32 %[cnt], 0\n\t"
            "s_cbranch_scc1 L_fin%=\n\t"
            "L_loop%=:\n\t"
            STAGE("v64", "v65", "v66", "v67", "v[64:65]", "v[66:67]", "v80", "v[88:89]", "v88", "v[76:79]", "v[86:87]", "16", "%c[le4]")
            "s_cbranch_scc1 L_fin%=\n\t"
            STAGE("v68", "v69", "v70", "v71", "v[68:69]", "v[70:71]", "v82", "v[90:91]", "v90", "v[64:67]", "v[80:81]", "20", "%c[le5]")
            "s_cbranch_scc1 L_fin%=\n\t"
            STAGE("v72", "v73", "v74", "v75", "v[72:73]", "v[74:75]", "v84", "v[92:93]", "v92", "v[68:71]", "v[82:83]", "24", "%c[le6]")
            "s_cbranch_scc1 L_fin%=\n\t"
            STAGE("v76", "v77", "v78", "v79", "v[76:77]", "v[78:79]", "v86", "v[94:95]", "v94", "v[72:75]", "v[84:85]", "28", "%c[le7]")
            "v_add_u32 v89, 16, v89\n\t"
            "s_cbranch_scc0 L_loop%=\n\t"
            "L_fin%=:\n\t"
            "v_pk_add_f32 v[108:109], v[108:109], %[eps]\n\t"
            "v_pk_add_f32 v[110:111], v[110:111], %[eps]\n\t"
            "v_rcp_f32 v108, v108\n\t"
            "v_rcp_f32 v109, v109\n\t"
            "v_rcp_f32 v110, v110\n\t"
            "v_rcp_f32 v111, v111\n\t"
            "v_pk_mul_f32 v[104:105], v[104:105], v[108:109]\n\t"
            "v_pk_mul_f32 v[106:107], v[106:107], v[110:111]\n\t"
            "v_dot2c_f32_bf16 v104, %[shi], v116\n\t"
            "v_dot2c_f32_bf16 v105, %[shi], v117\n\t"
            "v_dot2c_f32_bf16 v106, %[shi], v118\n\t"
            "v_dot2c_f32_bf16 v107, %[shi], v119\n\t"
            "v_maximum3_f32 v104, v104, 0, 0\n\t"
            "v_maximum3_f32 v105, v105, 0, 0\n\t"
            "v_maximum3_f32 v106, v106, 0, 0\n\t"
            "v_maximum3_f32 v107, v107, 0, 0\n\t"
            "v_dot2c_f32_bf16 v104, %[slo], v120\n\t"
            "v_dot2c_f32_bf16 v105, %[shi], v120\n\t"
            "v_dot2c_f32_bf16 v106, %[slo], v121\n\t"
            "v_dot2c_f32_bf16 v107, %[shi], v121\n\t"
            "s_nop 1\n\t"
            "v_cvt_pk_bf16_f32 v112, v104, v105\n\t"
            "v_cvt_pk_bf16_f32 v113, v106, v107\n\t"
            "buffer_store_dwordx2 v[112:113], v124, %[o], 0 offen nt\n\t"
            PRIO_STEP
            "s_cmp_lt_u32 %[nxt], %[ntask]\n\t"
            "s_cbranch_scc1 L_task%=\n\t"
            "s_waitcnt vmcnt(0) lgkmcnt(0)"
            : [cnt] "=&s"(s_cnt), [nxt] "=&s"(s_nxt), [t] "=&s"(s_t), [tc] "=&s"(s_tc)
            : "{v81}"(joff), "{v83}"(joff), "{v85}"(joff), "{v87}"(joff), "{v91}"(g8), "{v93}"(ctr), "{v95}"(one), "{v126}"(j8),
              "{v[100:101]}"(w01), "{v[102:103]}"(w23),
              [task] "s"(w), [ntask] "s"(ntask), [bv] "s"(srdBV), [o] "s"(srdO), [slo] "s"(0x00003F80u), [shi] "s"(0x3F800000u), [eps] "s"(eps2),
              [au] "i"(K2_AU), [le0] "i"(K2_LSZ), [le1] "i"(K2_LSZ + 4), [le2] "i"(K2_LSZ + 8), [le3] "i"(K2_LSZ + 12),
              [le4] "i"(K2_LSZ + 16), [le5] "i"(K2_LSZ + 20), [le6] "i"(K2_LSZ + 24), [le7] "i"(K2_LSZ + 28)
            : "v64", "v65", "v66", "v67", "v68", "v69", "v70", "v71", "v72", "v73", "v74", "v75", "v76", "v77", "v78", "v79",
              "v80", "v82", "v84", "v86", "v88", "v89", "v90", "v92", "v94", "v96", "v97", "v98", "v99",
              "v104", "v105", "v106", "v107", "v108", "v109", "v110", "v111", "v112", "v113", "v114", "v115",
              "v116", "v117", "v118", "v119", "v120", "v121", "v122", "v123", "v124", "v125", "v127", "scc", "memory");
#undef STAGE
    }
    STAMP(10);
    STAMPC(15);
    SPAN1(1);
}

extern "C" void kernel_launch(void* const* d_in, const int* in_sizes, int n_in,
                              void* d_out, int out_size, void* d_ws, size_t ws_size,
                              hipStream_t stream) {
    const u16* x = (const u16*)d_in[0];
    const u16* ea = (const u16*)d_in[1];
    const int* src = (const int*)d_in[2];
    const int* dst = (const int*)d_in[3];
    const u16* wa = (const u16*)d_in[4];
    const u16* wb = (const u16*)d_in[5];
    const u16* wc = (const u16*)d_in[6];
    const u16* wu = (const u16*)d_in[7];
    const u16* wv = (const u16*)d_in[8];
    (void)hipFuncSetAttribute((const void*)k1_gemm_bin, hipFuncAttributeMaxDynamicSharedMemorySize, K1_LDS);
    k1_gemm_bin<<<256, 512, K1_LDS, stream>>>(x, wa, wb, wu, wv, src, dst, ea, (char*)d_ws);
    (void)hipFuncSetAttribute((const void*)k2_edges, hipFuncAttributeMaxDynamicSharedMemorySize, K2_LDS);
    k2_edges<<<256, 1024, K2_LDS, stream>>>(x, wc, src, dst, ea, (const char*)d_ws, (u16*)d_out);
}
```

```cpp
#define MEASURE 0
#define SPAN 1
#include <hip/hip_runtime.h>
#include <hip/hip_bf16.h>
#include <stdint.h>

typedef unsigned short u16;
typedef unsigned int u32;
typedef u32 u32x2 __attribute__((ext_vector_type(2)));
typedef u32 u32x4 __attribute__((ext_vector_type(4)));
typedef float f32x2 __attribute__((ext_vector_type(2)));
typedef float f32x4 __attribute__((ext_vector_type(4)));
typedef float f32x16 __attribute__((ext_vector_type(16)));
typedef short s16x4 __attribute__((ext_vector_type(4)));
typedef short s16x8 __attribute__((ext_vector_type(8)));
typedef __bf16 bf16x8 __attribute__((ext_vector_type(8)));
typedef __bf16 bf16x4 __attribute__((ext_vector_type(4)));
typedef __bf16 bf16x2 __attribute__((ext_vector_type(2)));

constexpr int NN = 10000;
constexpr int NE = 160000;
constexpr int DD = 256;
constexpr int NP = 10048;
constexpr int RG = 320;
constexpr int NBK = 64;
constexpr int NB = 157;
constexpr int NCH = 64;
constexpr int CHE = 2500;
constexpr int CAP = 80;
constexpr int CAPN = 44;
constexpr int LST = 45;
constexpr float NLOG2E = -1.4426950408889634f;
constexpr u32 NEUTRAL_REC = (u32)NN;
constexpr u32 NEUTRAL_BV = 0x00007F00u;

constexpr size_t OFF_BV = 0;
constexpr size_t OFF_AU = OFF_BV + (size_t)4 * NP * 256;
constexpr size_t OFF_R4 = OFF_AU + (size_t)4 * NP * 256;
constexpr size_t OFF_R2 = OFF_R4 + (size_t)NBK * NCH * CAP * 4;
constexpr size_t OFF_CNT = OFF_R2 + (size_t)NBK * NCH * CAP * 2;

#define LDS_AS __attribute__((address_space(3)))
#define GLB_AS __attribute__((address_space(1)))

__device__ __forceinline__ void glds16(const void* g, void* l) {
    __builtin_amdgcn_global_load_lds((const GLB_AS u32*)g, (LDS_AS u32*)l, 16, 0, 0);
}
__device__ __forceinline__ void glds16a(const void* sbase, u32 voff, u32 lds) {
    u32 keep;
    asm volatile("s_mov_b32 %0, m0\n\ts_mov_b32 m0, %3\n\ts_nop 0\n\tglobal_load_lds_dwordx4 %1, %2\n\ts_mov_b32 m0, %0" : "=&s"(keep) : "v"(voff), "s"(sbase), "s"(lds) : "memory");
}
__device__ __forceinline__ u32 cvt_pk_bf16(float lo, float hi) {
    const f32x2 v = {lo, hi}; return __builtin_bit_cast(u32, __builtin_convertvector(v, bf16x2));
}
#define WAIT_V(n) asm volatile("s_waitcnt vmcnt(" #n ")" ::: "memory")
#define WAIT_L(n) asm volatile("s_waitcnt lgkmcnt(" #n ")" ::: "memory")
#define BAR() __builtin_amdgcn_s_barrier()
#define EXIT2_AT 99
#ifndef EXIT_AT
#define EXIT_AT 99
#endif
#define STORE_AUX 16
#define SPAN0() unsigned long long span_t0_ = __builtin_amdgcn_s_memrealtime()
#define MARK(I) do { unsigned long long tm_ = __builtin_amdgcn_s_memrealtime(); if ((threadIdx.x & 63) == 0) ((unsigned long long*)((char*)ws + (200u << 20)))[((I) * 256 + blockIdx.x) * 16 + (threadIdx.x >> 6)] = tm_; } while (0)
#define SPAN1(K) do { asm volatile("s_waitcnt vmcnt(0) lgkmcnt(0)" ::: "memory"); unsigned long long t1_ = __builtin_amdgcn_s_memrealtime(); if ((threadIdx.x & 63) == 0) { unsigned long long* p_ = (unsigned long long*)((char*)ws + (192u << 20)) + (((K) * 256 + blockIdx.x) * 16 + (threadIdx.x >> 6)) * 2; p_[0] = span_t0_; p_[1] = t1_; } } while (0)
#define STAMP(i)
#define STAMPC(i)
#define TICK(v)

constexpr int K1_R0 = 0, K1_R1 = 65536, K1_SCR = 131072;
constexpr int K1_ST4 = K1_SCR, K1_ST2 = K1_ST4 + NBK * CAP * 4, K1_BC = K1_ST2 + NBK * CAP * 2;
constexpr int K1_LDS = K1_SCR + 32768;
static_assert(K1_BC + NBK * 4 <= K1_LDS, "lds");

__global__ __launch_bounds__(512, 2) void k1_gemm_bin(
    const u16* __restrict__ x, const u16* __restrict__ wa, const u16* __restrict__ wb, const u16* __restrict__ wu, const u16* __restrict__ wv,
    const int* __restrict__ src, const int* __restrict__ dst, const u16* __restrict__ ea, char* __restrict__ ws) {
    extern __shared__ __attribute__((aligned(16))) char smem[];
    const int tid = threadIdx.x, lane = tid & 63;
    const int w = __builtin_amdgcn_readfirstlane(tid >> 6);
    const int b = blockIdx.x;
    const int type = (b >> 5) & 1;
    const int ct2 = (b >> 3) & 3;
    const int rg = (b & 7) * 4 + (b >> 6);
    const int row0 = rg * RG;
    const int chunk = (b & 31) | ((b >> 6) << 5) ;
    const bool binner = type == 1 && b < 128;
    constexpr int KOFF = 0;
    SPAN0();
    const unsigned long long cyc1_ = __builtin_amdgcn_s_memtime();
#if EXIT_AT == 0
    if (ws) { SPAN1(0); return; }
#endif
    STAMP(0);
    int bd[5], bs[5]; u32 be[5];
    if (binner) {
#pragma unroll
        for (int i = 0; i < 5; ++i) {
            const int k = tid + 512 * i;
            const int e = chunk * CHE + (k < CHE ? k : 0);
            bd[i] = dst[e]; bs[i] = src[e]; be[i] = ea[e];
        }
        if (tid < NBK) *(u32*)(smem + K1_BC + tid * 4) = 0;
    }
    {
        const int m = w >> 2;
        const u16* W = type == 0 ? (m == 0 ? wb : wv) : (m == 0 ? wa : wu);
        const int kb0 = (w & 3) * 64, kr = lane >> 3;
        const u16* g = W + (size_t)(kb0 + kr) * DD + ct2 * 64 + (((lane & 7) ^ ((kr & 2) << 1)) << 3);
#pragma unroll
        for (int i = 0; i < 8; ++i) glds16(g + (size_t)i * 8 * DD, smem + K1_R0 + m * 32768 + (kb0 / 8 + i) * 1024);
    }
    const int xr = lane >> 5, xp = lane & 31;
    auto stage_x = [&](int tile, int q, int ldsbase) {
        const int r = q * 2 + xr;
        int grow = row0 + tile * 32 + r; grow = grow < NN ? grow : NN - 1;
        glds16((const char*)x + (u32)(grow * 512 + ((xp ^ (r & 15)) << 4)), smem + ldsbase + q * 1024);
    };
#pragma unroll
    for (int i = 0; i < 8; ++i) { const int q = w * 8 + i; stage_x(q >> 4, q & 15, K1_R1 + (q >> 4) * 16384); }
    STAMP(1);
    WAIT_V(8); WAIT_L(0);
    BAR();
    STAMP(2);
    MARK(0);
#if EXIT_AT == 1
    if (ws) { SPAN1(0); return; }
#endif
    const int ch = w >> 2;
    bf16x8 wf0[16], wf1[16];
    {
        const int h = lane >> 5, blk = (lane >> 4) & 1, q = (lane & 15) >> 2, p = lane & 3;
        const u32 base = K1_R0 + (8 * h + q) * 128 + (((ch * 4 + blk * 2 + (p >> 1)) ^ ((q & 2) << 1)) << 4) + (p & 1) * 8;
#define TRRD(S) { u32x2 a0, a1, b0, b1; \
            asm volatile("ds_read_b64_tr_b16 %0, %4 offset:%c5\n\tds_read_b64_tr_b16 %1, %4 offset:%c6\n\t" \
                         "ds_read_b64_tr_b16 %2, %4 offset:%c7\n\tds_read_b64_tr_b16 %3, %4 offset:%c8" \
                         : "=&v"(a0), "=&v"(a1), "=&v"(b0), "=&v"(b1) \
                         : "v"(base), "i"((S) * 2048), "i"((S) * 2048 + 512), "i"(32768 + (S) * 2048), "i"(32768 + (S) * 2048 + 512) : "memory"); \
            wf0[S] = __builtin_bit_cast(bf16x8, __builtin_shufflevector(a0, a1, 0, 1, 2, 3)); \
            wf1[S] = __builtin_bit_cast(bf16x8, __builtin_shufflevector(b0, b1, 0, 1, 2, 3)); }
        TRRD(0) TRRD(1) TRRD(2) TRRD(3) TRRD(4) TRRD(5) TRRD(6) TRRD(7) TRRD(8) TRRD(9) TRRD(10) TRRD(11) TRRD(12) TRRD(13) TRRD(14) TRRD(15)
#undef TRRD
    }
    if (binner) {
#pragma unroll
        for (int i = 0; i < 5; ++i) {
            const int k = tid + 512 * i;
            if (k < CHE) {
                const int bk = (int)(((u32)bd[i] * 53432u) >> 23);
                const int dl = bd[i] - bk * NB;
                const u32 pos = atomicAdd((u32*)(smem + K1_BC + bk * 4), 1u);
                if (pos < CAP) {
                    *(u32*)(smem + K1_ST4 + (bk * CAP + pos) * 4) = (u32)bs[i] | (be[i] << 16);
                    *(u16*)(smem + K1_ST2 + (bk * CAP + pos) * 2) = (u16)dl;
                }
            }
        }
    }
    STAMP(3);
    WAIT_V(0); WAIT_L(0);
    __builtin_amdgcn_sched_barrier(0);
    BAR();
    STAMP(4);
    MARK(1);
#if EXIT_AT == 2
    if (ws) { asm volatile("" :: "v"(wf0[0]), "v"(wf1[15])); SPAN1(0); return; }
#endif
#pragma unroll
    for (int i = 0; i < 8; ++i) { const int q = w * 8 + i; stage_x(4 + (q >> 4), q & 15, K1_R0 + (q >> 4) * 16384); }
    if (binner) {
        u32* R4 = (u32*)(ws + OFF_R4); u16* R2 = (u16*)(ws + OFF_R2); u32* CNT = (u32*)(ws + OFF_CNT);
        u32x4 t4[3]; u32x2 t2[3];
#pragma unroll
        for (int i = 0; i < 3; ++i) { const int q = tid + 512 * i; const int qq = q < NBK * (CAP / 4) ? q : 0;
            t4[i] = *(const u32x4*)(smem + K1_ST4 + qq * 16); t2[i] = *(const u32x2*)(smem + K1_ST2 + qq * 8); }
        const u32 cv = *(const u32*)(smem + K1_BC + (tid & 63) * 4);
#pragma unroll
        for (int i = 0; i < 3; ++i) { const int q = tid + 512 * i;
            if (q < NBK * (CAP / 4)) { const int bk = q / (CAP / 4), o = q - bk * (CAP / 4);
                *(u32x4*)(R4 + ((size_t)bk * NCH + chunk) * CAP + o * 4) = t4[i];
                *(u32x2*)(R2 + ((size_t)bk * NCH + chunk) * CAP + o * 4) = t2[i]; } }
        if (tid < NBK) CNT[tid * NCH + chunk] = cv;
        WAIT_L(0);
        BAR();
    }
    if (rg == 0 && type == 0 && w == 0) ((u32*)(ws + OFF_BV))[((size_t)ct2 * NP + NN) * 64 + lane] = NEUTRAL_BV;

    const int r = lane & 31, h = lane >> 5;
    const int abase = r * 512 + ((((r & 14) | ((h ^ r) & 1))) << 4);
    char* const scr = smem + K1_SCR + w * 4096;
    const __amdgpu_buffer_rsrc_t trs = __builtin_amdgcn_make_buffer_rsrc(ws + (type ? OFF_AU : OFF_BV) + (size_t)ct2 * NP * 256, 0, NN * 256, 0x00020000);
    const u32 svo = (lane >> 3) * 256 + ch * 128 + (lane & 7) * 16;

    const int s4 = w & 3;
    if (ch == 0) __builtin_amdgcn_s_setprio(1);
    STAMP(5);
#pragma unroll 1
    for (int st = 0; st < (EXIT_AT == 3 ? 1 : (EXIT_AT == 4 ? 2 : 3)); ++st) {
        int tile, ldsbase;
        if (st == 0) { tile = s4; ldsbase = K1_R1 + s4 * 16384; }
        else if (st == 1) { tile = 4 + s4; ldsbase = K1_R0 + s4 * 16384; }
        else { const int t = (w < 2) ? w : (w >= 6 ? w - 6 : 99); tile = 8 + t; ldsbase = K1_R1 + (t & 1) * 16384; }
        const int trow = row0 + tile * 32;
        const bool active = tile < 10 && trow < NN;
        unsigned long long tkA = 0, tkB = 0, tkC = 0, tkD = 0;
        if (active) {
            TICK(tkA);
            bf16x8 a[16];
#pragma unroll
            for (int s = 0; s < 8; ++s) a[s] = *(const bf16x8*)(smem + ldsbase + (abase ^ (s << 5)));
            __builtin_amdgcn_sched_barrier(0);
            f32x16 acc0, acc1;
#pragma unroll
            for (int i = 0; i < 16; ++i) { acc0[i] = 0.f; acc1[i] = 0.f; }
#pragma unroll
            for (int s = 0; s < 8; ++s) {
                a[s + 8] = *(const bf16x8*)(smem + ldsbase + (abase ^ (s << 5)) + 256);
                acc0 = __builtin_amdgcn_mfma_f32_32x32x16_bf16(a[s], wf0[s], acc0, 0, 0, 0);
                acc1 = __builtin_amdgcn_mfma_f32_32x32x16_bf16(a[s], wf1[s], acc1, 0, 0, 0);
                __builtin_amdgcn_sched_group_barrier(0x100, 1, 0);
                __builtin_amdgcn_sched_group_barrier(0x8, 2, 0);
            }
            __builtin_amdgcn_sched_barrier(0);
#pragma unroll
            for (int s = 8; s < 16; ++s) {
                acc0 = __builtin_amdgcn_mfma_f32_32x32x16_bf16(a[s], wf0[s], acc0, 0, 0, 0);
                acc1 = __builtin_amdgcn_mfma_f32_32x32x16_bf16(a[s], wf1[s], acc1, 0, 0, 0);
            }
            TICK(tkB);
            char* const sw = scr + h * 512 + r * 4;
            if (type == 0) {
#pragma unroll
                for (int i = 0; i < 16; i += 2) {
                    const u32 bb = cvt_pk_bf16(acc0[i] * NLOG2E, acc0[i + 1] * NLOG2E);
#pragma unroll
                    for (int j = 0; j < 2; ++j) {
                        const u32 vb = __float_as_uint(acc1[i + j]);
                        const u32 bl = j ? (bb >> 16) : (bb & 0xFFFFu);
                        const u32 u = __builtin_elementwise_sub_sat((vb & 0x7FFFFFFFu) + 0x8000u, bl);
                        *(u32*)(sw + (((i + j) & 3) + 8 * ((i + j) >> 2)) * 128) = (vb & 0x80000000u) | (u & 0x7FFF0000u) | bl;
                    }
                }
            } else {
#pragma unroll
                for (int i = 0; i < 16; ++i) *(u32*)(sw + ((i & 3) + 8 * (i >> 2)) * 128) = cvt_pk_bf16(acc0[i] * NLOG2E, acc1[i]);
            }
            TICK(tkC);
#pragma unroll
            for (int i = 0; i < 4; ++i) {
                const u32x4 v = *(const u32x4*)(scr + i * 1024 + lane * 16);
                __builtin_amdgcn_raw_buffer_store_b128(v, trs, svo + (u32)(trow + i * 8) * 256, 0, STORE_AUX);
            }
        }
        TICK(tkD);
        if (st == 0) STAMP(6); else if (st == 1) STAMP(8); else STAMP(10);
        if (st < 2) {
            if (active) WAIT_V(4); else WAIT_V(0);
            BAR();
            if (st == 0) {
#pragma unroll
                for (int i = 0; i < 4; ++i) { const int q = w * 4 + i; stage_x(8 + (q >> 4), q & 15, K1_R1 + (q >> 4) * 16384); }
            }
        }
    }
    SPAN1(0);
    if (blockIdx.x == 0 && threadIdx.x == 0) { unsigned long long* q = (unsigned long long*)(ws + (208u << 20)); q[0] = __builtin_amdgcn_s_memtime() - cyc1_; q[1] = __builtin_amdgcn_s_memrealtime() - span_t0_; }
}

constexpr int LSTB = LST * 4;
constexpr int K2_X = 0;
constexpr int K2_AU = K2_X + 20480;
constexpr int K2_LIDX = K2_AU + 40960;
constexpr int K2_LSZ = (NB * LSTB + 32 + 256 + 15) / 16 * 16;
constexpr int K2_LE = K2_LIDX + K2_LSZ;
constexpr int K2_CNT = K2_LE + K2_LSZ;
constexpr int K2_HIST = K2_CNT + 160 * 4;
constexpr int K2_ORD = K2_HIST + 128 * 4;
constexpr int K2_MISC = K2_ORD + 80 * 32;
constexpr int K2_LDS = K2_MISC + 16;
constexpr int K2_TRASH = NB * LSTB + 32;
static_assert(K2_TRASH + 256 <= K2_LSZ, "trash");
static_assert(K2_LSZ < 65536 - 64, "ds offset");

__device__ __forceinline__ float dot2sel(u32 d, u32 sel, float c) {
    return __builtin_amdgcn_fdot2_f32_bf16(__builtin_bit_cast(bf16x2, d), __builtin_bit_cast(bf16x2, sel), c, false);
}

struct Acc { f32x2 n01, n23, d01, d23; };

__device__ __forceinline__ void edge_step(Acc& a, const u32x4 d, const u32 rec, const f32x2 a01, const f32x2 a23, const f32x2 w01, const f32x2 w23, const u32 sel) {
    const float e = __uint_as_float(rec & 0xFFFF0000u);
    const f32x2 ee = {e, e};
    const f32x2 t01 = __builtin_elementwise_fma(ee, w01, a01);
    const f32x2 t23 = __builtin_elementwise_fma(ee, w23, a23);
    f32x2 E01, E23;
    E01.x = __builtin_amdgcn_exp2f(dot2sel(d.x, sel, t01.x));
    E01.y = __builtin_amdgcn_exp2f(dot2sel(d.y, sel, t01.y));
    E23.x = __builtin_amdgcn_exp2f(dot2sel(d.z, sel, t23.x));
    E23.y = __builtin_amdgcn_exp2f(dot2sel(d.w, sel, t23.y));
    const f32x2 one = {1.f, 1.f};
    E01 += one; E23 += one;
    f32x2 s01, s23;
    s01.x = __builtin_amdgcn_rcpf(E01.x); s01.y = __builtin_amdgcn_rcpf(E01.y);
    s23.x = __builtin_amdgcn_rcpf(E23.x); s23.y = __builtin_amdgcn_rcpf(E23.y);
    a.d01 += s01; a.d23 += s23;
    const f32x2 v01 = {__uint_as_float(d.x), __uint_as_float(d.y)};
    const f32x2 v23 = {__uint_as_float(d.z), __uint_as_float(d.w)};
    a.n01 = __builtin_elementwise_fma(s01, v01, a.n01);
    a.n23 = __builtin_elementwise_fma(s23, v23, a.n23);
}

__device__ __forceinline__ void node_finish(const Acc& a, const u32x4 au, const u32x2 xv, u16* outp) {
    const float num[4] = {a.n01.x, a.n01.y, a.n23.x, a.n23.y};
    const float den[4] = {a.d01.x, a.d01.y, a.d23.x, a.d23.y};
    const u32 aus[4] = {au.x, au.y, au.z, au.w};
    const float xs[4] = {__uint_as_float(xv.x << 16), __uint_as_float(xv.x & 0xFFFF0000u), __uint_as_float(xv.y << 16), __uint_as_float(xv.y & 0xFFFF0000u)};
    float o[4];
#pragma unroll
    for (int c = 0; c < 4; ++c) {
        const float z = __uint_as_float(aus[c] & 0xFFFF0000u) + num[c] * __builtin_amdgcn_rcpf(den[c] + 1e-6f);
        const float rl = z > 0.f ? z : 0.f * z;
        o[c] = xs[c] + rl;
    }
    u32x2 ov; ov.x = cvt_pk_bf16(o[0], o[1]); ov.y = cvt_pk_bf16(o[2], o[3]);
    *(u32x2*)outp = ov;
}

template <int CTRL, int RM> __device__ __forceinline__ u32 dpp_add(u32 v) {
    return v + (u32)__builtin_amdgcn_update_dpp(0, (int)v, CTRL, RM, 0xf, false);
}

__global__ __launch_bounds__(1024, 4) void k2_edges(
    const u16* __restrict__ x, const u16* __restrict__ wc, const int* __restrict__ src, const int* __restrict__ dst, const u16* __restrict__ ea,
    const char* __restrict__ ws, u16* __restrict__ out) {
    extern __shared__ __attribute__((aligned(16))) char smem[];
    const int tid = threadIdx.x, lane = tid & 63;
    const int w = __builtin_amdgcn_readfirstlane(tid >> 6);
    const int b = blockIdx.x;
    const int slice = (b & 7) >> 1;
    const int bucket = (b >> 3) * 2 + (b & 1);
    const int node0 = bucket * NB;
    const int nvalid = (NN - node0) < NB ? (NN - node0) : NB;
    u32* const cnt = (u32*)(smem + K2_CNT);
    u32* const hist = (u32*)(smem + K2_HIST);
    u32* const misc = (u32*)(smem + K2_MISC);
    constexpr int KOFF = 256 * 16;
    SPAN0();
    const unsigned long long cyc0_ = __builtin_amdgcn_s_memtime();
    STAMP(0);

    const u32* R4 = (const u32*)(ws + OFF_R4) + (size_t)bucket * NCH * CAP;
    const u16* R2 = (const u16*)(ws + OFF_R2) + (size_t)bucket * NCH * CAP;
    const u32* CNT = (const u32*)(ws + OFF_CNT) + bucket * NCH;
    const int ch0 = tid / (CAP / 4), ch1 = (tid + 1024) / (CAP / 4);
    const u32x4 r40 = *(const u32x4*)(R4 + tid * 4);
    const u32x2 r20 = *(const u32x2*)(R2 + tid * 4);
    const u32 c0 = CNT[ch0];
    u32x4 r41 = {0, 0, 0, 0}; u32x2 r21 = {0, 0}; u32 c1 = 0;
    if (w < 4) { r41 = *(const u32x4*)(R4 + (tid + 1024) * 4); r21 = *(const u32x2*)(R2 + (tid + 1024) * 4); c1 = CNT[ch1]; }
    const char* const bvb = ws + OFF_BV + (size_t)slice * NP * 256;
    const char* const aub = ws + OFF_AU + (size_t)slice * NP * 256;
    const int g = lane >> 4, j = lane & 15;
    const u32x2 wcv = *(const u32x2*)(wc + slice * 64 + j * 4);
    for (int i = tid; i < K2_LSZ / 16; i += 1024) {
        *(u32x4*)(smem + K2_LIDX + i * 16) = (u32x4){NEUTRAL_REC, NEUTRAL_REC, NEUTRAL_REC, NEUTRAL_REC};
        *(u32x4*)(smem + K2_LE + i * 16) = (u32x4){0, 0, 0, 0};
    }
    if (tid < 160 + 64) cnt[tid] = 0;
    if (tid == 0) { misc[0] = 16; misc[1] = 0; }
    f32x2 w01 = {__uint_as_float(wcv.x << 16) * NLOG2E, __uint_as_float(wcv.x & 0xFFFF0000u) * NLOG2E};
    f32x2 w23 = {__uint_as_float(wcv.y << 16) * NLOG2E, __uint_as_float(wcv.y & 0xFFFF0000u) * NLOG2E};
    asm volatile("" : "+v"(w01), "+v"(w23));
    STAMP(1);
    WAIT_V(0); WAIT_L(0); BAR();
    {
        const char* ga = aub + (size_t)node0 * 256;
#pragma unroll
        for (int i = 0; i < 3; ++i) { const int q = w + 16 * i; if (q < 40) glds16a(ga, q * 1024 + lane * 16, K2_AU + q * 1024); }
        const char* gx = (const char*)x + slice * 128;
#pragma unroll
        for (int i = 0; i < 2; ++i) {
            const int q = w + 16 * i;
            if (q < 20) { int row = node0 + q * 8 + (lane >> 3); row = row < NN ? row : NN - 1;
                glds16a(gx, row * 512 + (lane & 7) * 16, K2_X + q * 1024); }
        }
    }
    STAMP(2);
    MARK(2);
    {
        const int p0 = (tid - ch0 * (CAP / 4)) * 4;
        const u32 rec0[4] = {r40.x, r40.y, r40.z, r40.w};
        const u32 dl0[4] = {r20.x & 0xFFFF, r20.x >> 16, r20.y & 0xFFFF, r20.y >> 16};
        u32 slot[4];
        bool ovf = c0 > CAP;
#pragma unroll
        for (int k = 0; k < 4; ++k) slot[k] = atomicAdd(&cnt[p0 + k < c0 ? dl0[k] : 224 + lane], 1u);
        if (w < 4) {
            const int p1 = (tid + 1024 - ch1 * (CAP / 4)) * 4;
            const u32 rec1[4] = {r41.x, r41.y, r41.z, r41.w};
            const u32 dl1[4] = {r21.x & 0xFFFF, r21.x >> 16, r21.y & 0xFFFF, r21.y >> 16};
            u32 slot1[4];
            ovf |= c1 > CAP;
#pragma unroll
            for (int k = 0; k < 4; ++k) slot1[k] = atomicAdd(&cnt[p1 + k < c1 ? dl1[k] : 224 + lane], 1u);
#pragma unroll
            for (int k = 0; k < 4; ++k) {
                const bool v = p1 + k < c1, ok = v && slot1[k] < CAPN;
                ovf |= v && !ok;
                const u32 o = ok ? dl1[k] * LSTB + slot1[k] * 4 : K2_TRASH + lane * 4;
                *(u32*)(smem + K2_LIDX + o) = rec1[k] & 0xFFFFu;
                *(u32*)(smem + K2_LE + o) = rec1[k] & 0xFFFF0000u;
            }
        }
#pragma unroll
        for (int k = 0; k < 4; ++k) {
            const bool v = p0 + k < c0, ok = v && slot[k] < CAPN;
            ovf |= v && !ok;
            const u32 o = ok ? dl0[k] * LSTB + slot[k] * 4 : K2_TRASH + lane * 4;
            *(u32*)(smem + K2_LIDX + o) = rec0[k] & 0xFFFFu;
            *(u32*)(smem + K2_LE + o) = rec0[k] & 0xFFFF0000u;
        }
        if (ovf) misc[1] = 1;
    }
    STAMP(3);
    WAIT_L(0); BAR();
    STAMP(4);
    const bool slow = misc[1] != 0;

    const u32 joff = j * 16;

    if (slow) {
        u32 sel = 0x00003F80u;
        asm volatile("" : "+s"(sel));
        for (int task = w; task * 4 < nvalid; task += 16) {
            const int n = task * 4 + g;
            const bool nv = n < nvalid;
            const int node = node0 + (nv ? n : 0);
            const u32x4 au = *(const u32x4*)(aub + (size_t)node * 256 + joff);
            const u32x2 xv = *(const u32x2*)(x + (size_t)node * DD + slice * 64 + j * 4);
            const f32x2 a01 = {__uint_as_float(au.x << 16), __uint_as_float(au.y << 16)};
            const f32x2 a23 = {__uint_as_float(au.z << 16), __uint_as_float(au.w << 16)};
            Acc acc = {{0.f, 0.f}, {0.f, 0.f}, {0.f, 0.f}, {0.f, 0.f}};
            const int lo = node0 + task * 4;
            for (int e0 = 0; e0 < NE; e0 += 64) {
                const int d = dst[e0 + lane];
                unsigned long long m = __ballot((u32)(d - lo) < 4u);
                if (m) {
                    const u32 rc = (u32)src[e0 + lane] | ((u32)ea[e0 + lane] << 16);
                    while (m) {
                        const int l = __builtin_ctzll(m); m &= m - 1;
                        const int dd = __builtin_amdgcn_readlane(d, l);
                        const u32 rr = __builtin_amdgcn_readlane(rc, l);
                        const u32 rec = (nv && dd == node) ? rr : NEUTRAL_REC;
                        const u32x4 dv = *(const u32x4*)(bvb + (size_t)((rec & 0xFFFFu) * 256 + joff));
                        edge_step(acc, dv, rec, a01, a23, w01, w23, sel);
                    }
                }
            }
            if (nv) node_finish(acc, au, xv, out + (size_t)node * DD + slice * 64 + j * 4);
        }
        return;
    }

    u32 mydeg = 0, myrank = 0;
    if (tid < nvalid) { mydeg = cnt[tid]; myrank = atomicAdd(&hist[63 - mydeg], 1u); }
    WAIT_L(0); BAR();
    if (w < 3) {
        const u32 h0 = hist[lane];
        u32 v = h0;
        v = dpp_add<0x111, 0xf>(v); v = dpp_add<0x112, 0xf>(v); v = dpp_add<0x114, 0xf>(v); v = dpp_add<0x118, 0xf>(v);
        v = dpp_add<0x142, 0xa>(v); v = dpp_add<0x143, 0xc>(v);
        const u32 base = (u32)__builtin_amdgcn_ds_bpermute((int)((63 - mydeg) * 4), (int)(v - h0));
        if (tid < nvalid) {
            const u32 pos = base + myrank;
            const u32x2 ent = {(u32)(K2_LIDX + tid * LSTB), (u32)(tid * 256) + mydeg};
            u32x2* o = (u32x2*)(smem + K2_ORD) + pos;
            o[0] = ent;
            if (pos == (u32)nvalid - 1) { o[1] = ent; o[2] = ent; o[3] = ent; }
        }
    }
    WAIT_V(0); WAIT_L(0); BAR();
    STAMP(5);
    MARK(3);
    STAMPC(14);

    const int ntask = (nvalid + 3) >> 2;
    if (w < ntask) {
        u32x4 srdBV, srdO;
        { const size_t p = (size_t)bvb; srdBV = (u32x4){(u32)p, (u32)(p >> 32) | (256u << 16), 0xFFFFFFFFu, 0x00020000u}; }
        { const size_t ob = (size_t)node0 * 512 + slice * 128; const size_t p = (size_t)out + ob; srdO = (u32x4){(u32)p, (u32)(p >> 32), (u32)((size_t)NN * 512 - ob), 0x00020000u}; }
        u32 g8 = K2_ORD + g * 8, ctr = K2_MISC, one = 1, j8 = j * 8;
        unsigned long long eps2 = 0x358637BD358637BDull;
        u32 s_cnt, s_nxt, s_t, s_tc;
#define STAGE(DS0, DS1, DS2, DS3, DSL, DSH, IX, EP, E1, DN, IXN, O1, O2) \
            "s_waitcnt lgkmcnt(0)\n\t" \
            "buffer_load_dwordx4 " DN ", " GIX(IXN) ", %[bv], 0 idxen offen\n\t" \
            "v_pk_fma_f32 v[112:113], " EP ", v[100:101], v[96:97] op_sel_hi:[0,1,1]\n\t" \
            "v_pk_fma_f32 v[114:115], " EP ", v[102:103], v[98:99] op_sel_hi:[0,1,1]\n\t" \
            "ds_read_b32 " IX ", v89 offset:" O1 "\n\t" \
            "ds_read_b32 " E1 ", v89 offset:" O2 "\n\t" \
            "s_waitcnt vmcnt(3)\n\t" \
            "v_dot2c_f32_bf16 v112, %[slo], " DS0 "\n\t" \
            "v_dot2c_f32_bf16 v113, %[slo], " DS1 "\n\t" \
            "v_dot2c_f32_bf16 v114, %[slo], " DS2 "\n\t" \
            "v_dot2c_f32_bf16 v115, %[slo], " DS3 "\n\t" \
            "v_exp_f32 v112, v112\n\t" \
            "v_exp_f32 v113, v113\n\t" \
            "v_exp_f32 v114, v114\n\t" \
            "v_exp_f32 v115, v115\n\t" \
            "v_pk_add_f32 v[112:113], v[112:113], 1.0 op_sel_hi:[1,0]\n\t" \
            "v_pk_add_f32 v[114:115], v[114:115], 1.0 op_sel_hi:[1,0]\n\t" \
            "v_rcp_f32 v112, v112\n\t" \
            "v_rcp_f32 v113, v113\n\t" \
            "v_rcp_f32 v114, v114\n\t" \
            "v_rcp_f32 v115, v115\n\t" \
            "s_sub_u32 %[cnt], %[cnt], 1\n\t" \
            "v_pk_add_f32 v[108:109], v[108:109], v[112:113]\n\t" \
            "v_pk_fma_f32 v[104:105], v[112:113], " DSL ", v[104:105]\n\t" \
            "v_pk_add_f32 v[110:111], v[110:111], v[114:115]\n\t" \
            "v_pk_fma_f32 v[106:107], v[114:115], " DSH ", v[106:107]\n\t" \
            "s_cmp_eq_u32 %[cnt], 0\n\t"
#define GIX(x) x
#define PRIO_ON 1
#define PRIO_INIT "s_setprio 3\n\t"
#define PRIO_STEP \
            "s_add_u32 %[tc], %[tc], 1\n\t" \
            "s_cmp_lg_u32 %[tc], 1\n\t s_cbranch_scc1 L_p1%=\n\t s_setprio 2\n\t L_p1%=:\n\t" \
            "s_cmp_lg_u32 %[tc], 2\n\t s_cbranch_scc1 L_p2%=\n\t s_setprio 1\n\t L_p2%=:\n\t" \
            "s_cmp_lg_u32 %[tc], 3\n\t s_cbranch_scc1 L_p3%=\n\t s_setprio 0\n\t L_p3%=:\n\t"
        asm volatile(
            PRIO_INIT
            "s_mov_b32 %[tc], 0\n\t"
            "s_lshl_b32 %[t], %[task], 5\n\t"
            "v_add_u32 v127, %[t], v91\n\t"
            "ds_read_b64 v[122:123], v127\n\t"
            "s_mov_b64 exec, 1\n\t"
            "ds_add_rtn_u32 v125, v93, v95\n\t"
            "s_mov_b64 exec, -1\n\t"
            "L_task%=:\n\t"
            "s_waitcnt lgkmcnt(0)\n\t"
            "v_readfirstlane_b32 %[nxt], v125\n\t"
            "v_readfirstlane_b32 %[cnt], v123\n\t"
            "v_mov_b32 v89, v122\n\t"
            "v_and_b32 v127, 0xffffff00, v123\n\t"
            "ds_read_b32 v80, v89\n\t"
            "ds_read_b32 v82, v89 offset:4\n\t"
            "ds_read_b32 v84, v89 offset:8\n\t"
            "ds_read_b32 v86, v89 offset:12\n\t"
            "ds_read_b32 v88, v89 offset:%c[le0]\n\t"
            "ds_read_b32 v90, v89 offset:%c[le1]\n\t"
            "ds_read_b32 v92, v89 offset:%c[le2]\n\t"
            "ds_read_b32 v94, v89 offset:%c[le3]\n\t"
            "v_add_u32 v112, v127, v81\n\t"
            "v_lshl_add_u32 v124, v127, 1, v126\n\t"
            "v_lshrrev_b32 v113, 1, v112\n\t"
            "ds_read_b128 v[116:119], v112 offset:%c[au]\n\t"
            "ds_read_b64 v[120:121], v113\n\t"
            "s_and_b32 %[cnt], %[cnt], 0xff\n\t"
            "s_lshl_b32 %[t], %[nxt], 5\n\t"
            "v_add_u32 v127, %[t], v91\n\t"
            "ds_read_b64 v[122:123], v127\n\t"
            "s_mov_b64 exec, 1\n\t"
            "ds_add_rtn_u32 v125, v93, v95\n\t"
            "s_mov_b64 exec, -1\n\t"
            "s_waitcnt lgkmcnt(9)\n\t"
            "buffer_load_dwordx4 v[64:67], v[80:81], %[bv], 0 idxen offen\n\t"
            "buffer_load_dwordx4 v[68:71], v[82:83], %[bv], 0 idxen offen\n\t"
            "buffer_load_dwordx4 v[72:75], v[84:85], %[bv], 0 idxen offen\n\t"
            "v_mov_b64 v[104:105], 0\n\t"
            "v_mov_b64 v[106:107], 0\n\t"
            "v_mov_b64 v[108:109], 0\n\t"
            "v_mov_b64 v[110:111], 0\n\t"
            "s_waitcnt lgkmcnt(2)\n\t"
            "v_lshlrev_b32 v96, 16, v116\n\t"
            "v_lshlrev_b32 v97, 16, v117\n\t"
            "v_lshlrev_b32 v98, 16, v118\n\t"
            "v_lshlrev_b32 v99, 16, v119\n\t"
            "s_cmp_eq_u32 %[cnt], 0\n\t"
            "s_cbranch_scc1 L_fin%=\n\t"
            "L_loop%=:\n\t"
            STAGE("v64", "v65", "v66", "v67", "v[64:65]", "v[66:67]", "v80", "v[88:89]", "v88", "v[76:79]", "v[86:87]", "16", "%c[le4]")
            "s_cbranch_scc1 L_fin%=\n\t"
            STAGE("v68", "v69", "v70", "v71", "v[68:69]", "v[70:71]", "v82", "v[90:91]", "v90", "v[64:67]", "v[80:81]", "20", "%c[le5]")
            "s_cbranch_scc1 L_fin%=\n\t"
            STAGE("v72", "v73", "v74", "v75", "v[72:73]", "v[74:75]", "v84", "v[92:93]", "v92", "v[68:71]", "v[82:83]", "24", "%c[le6]")
            "s_cbranch_scc1 L_fin%=\n\t"
            STAGE("v76", "v77", "v78", "v79", "v[76:77]", "v[78:79]", "v86", "v[94:95]", "v94", "v[72:75]", "v[84:85]", "28", "%c[le7]")
            "v_add_u32 v89, 16, v89\n\t"
            "s_cbranch_scc0 L_loop%=\n\t"
            "L_fin%=:\n\t"
            "v_pk_add_f32 v[108:109], v[108:109], %[eps]\n\t"
            "v_pk_add_f32 v[110:111], v[110:111], %[eps]\n\t"
            "v_rcp_f32 v108, v108\n\t"
            "v_rcp_f32 v109, v109\n\t"
            "v_rcp_f32 v110, v110\n\t"
            "v_rcp_f32 v111, v111\n\t"
            "v_pk_mul_f32 v[104:105], v[104:105], v[108:109]\n\t"
            "v_pk_mul_f32 v[106:107], v[106:107], v[110:111]\n\t"
            "v_dot2c_f32_bf16 v104, %[shi], v116\n\t"
            "v_dot2c_f32_bf16 v105, %[shi], v117\n\t"
            "v_dot2c_f32_bf16 v106, %[shi], v118\n\t"
            "v_dot2c_f32_bf16 v107, %[shi], v119\n\t"
            "v_maximum3_f32 v104, v104, 0, 0\n\t"
            "v_maximum3_f32 v105, v105, 0, 0\n\t"
            "v_maximum3_f32 v106, v106, 0, 0\n\t"
            "v_maximum3_f32 v107, v107, 0, 0\n\t"
            "v_dot2c_f32_bf16 v104, %[slo], v120\n\t"
            "v_dot2c_f32_bf16 v105, %[shi], v120\n\t"
            "v_dot2c_f32_bf16 v106, %[slo], v121\n\t"
            "v_dot2c_f32_bf16 v107, %[shi], v121\n\t"
            "s_nop 1\n\t"
            "v_cvt_pk_bf16_f32 v112, v104, v105\n\t"
            "v_cvt_pk_bf16_f32 v113, v106, v107\n\t"
            "buffer_store_dwordx2 v[112:113], v124, %[o], 0 offen\n\t"
            PRIO_STEP
            "s_cmp_lt_u32 %[nxt], %[ntask]\n\t"
            "s_cbranch_scc1 L_task%=\n\t"
            "s_waitcnt vmcnt(0) lgkmcnt(0)"
            : [cnt] "=&s"(s_cnt), [nxt] "=&s"(s_nxt), [t] "=&s"(s_t), [tc] "=&s"(s_tc)
            : "{v81}"(joff), "{v83}"(joff), "{v85}"(joff), "{v87}"(joff), "{v91}"(g8), "{v93}"(ctr), "{v95}"(one), "{v126}"(j8),
              "{v[100:101]}"(w01), "{v[102:103]}"(w23),
              [task] "s"(w), [ntask] "s"(ntask), [bv] "s"(srdBV), [o] "s"(srdO), [slo] "s"(0x00003F80u), [shi] "s"(0x3F800000u), [eps] "s"(eps2),
              [au] "i"(K2_AU), [le0] "i"(K2_LSZ), [le1] "i"(K2_LSZ + 4), [le2] "i"(K2_LSZ + 8), [le3] "i"(K2_LSZ + 12),
              [le4] "i"(K2_LSZ + 16), [le5] "i"(K2_LSZ + 20), [le6] "i"(K2_LSZ + 24), [le7] "i"(K2_LSZ + 28)
            : "v64", "v65", "v66", "v67", "v68", "v69", "v70", "v71", "v72", "v73", "v74", "v75", "v76", "v77", "v78", "v79",
              "v80", "v82", "v84", "v86", "v88", "v89", "v90", "v92", "v94", "v96", "v97", "v98", "v99",
              "v104", "v105", "v106", "v107", "v108", "v109", "v110", "v111", "v112", "v113", "v114", "v115",
              "v116", "v117", "v118", "v119", "v120", "v121", "v122", "v123", "v124", "v125", "v127", "scc", "memory");
#undef STAGE
    }
    STAMP(10);
    STAMPC(15);
    SPAN1(1);
    if (blockIdx.x == 0 && w == 0) {
        const unsigned long long* sp = (const unsigned long long*)((const char*)ws + (192u << 20));
        const unsigned long long* mk = (const unsigned long long*)((const char*)ws + (200u << 20));
        unsigned long long mn = ~0ull, mx = 0, m0 = 0, m1 = 0;
        for (int i = lane; i < 256 * 8; i += 64) { const int b2 = i >> 3, w2 = i & 7;
            const unsigned long long a = sp[(b2 * 16 + w2) * 2], e = sp[(b2 * 16 + w2) * 2 + 1];
            mn = a < mn ? a : mn; mx = e > mx ? e : mx;
            const unsigned long long k0 = mk[b2 * 16 + w2], k1 = mk[(256 + b2) * 16 + w2];
            m0 = k0 > m0 ? k0 : m0; m1 = k1 > m1 ? k1 : m1; }
        for (int o = 32; o; o >>= 1) { unsigned long long t;
            t = __shfl_xor(mn, o); mn = t < mn ? t : mn; t = __shfl_xor(mx, o); mx = t > mx ? t : mx;
            t = __shfl_xor(m0, o); m0 = t > m0 ? t : m0; t = __shfl_xor(m1, o); m1 = t > m1 ? t : m1; }
        const unsigned long long k2s = span_t0_, k2a = mk[(2 * 256) * 16], k2b = mk[(3 * 256) * 16];
        unsigned long long val = 0;
        if (MEASURE == 1) val = mx - mn;
        if (MEASURE == 2) val = k2s - mx;
        if (MEASURE == 3) val = m0 - mn;
        if (MEASURE == 4) val = m1 - mn;
        if (MEASURE == 5) val = k2a - k2s;
        if (MEASURE == 6) val = k2b - k2s;
        if (MEASURE == 11) val = mn - *(const unsigned long long*)((const char*)ws + (210u << 20));
        if (MEASURE == 9) val = ((const unsigned long long*)((const char*)ws + (208u << 20)))[0] / 20;
        if (MEASURE == 10) val = ((const unsigned long long*)((const char*)ws + (208u << 20)))[1];
        if (MEASURE == 8) val = (__builtin_amdgcn_s_memtime() - cyc0_) / 20;
        if (MEASURE == 7) val = __builtin_amdgcn_s_memrealtime() - k2s;
        const unsigned long long t0 = __builtin_amdgcn_s_memrealtime();
        while (__builtin_amdgcn_s_memrealtime() - t0 < val * 10ull) __builtin_amdgcn_s_sleep(2);
    }
}

__global__ void k_first(char* ws) { if (blockIdx.x == 0 && threadIdx.x == 0) *(unsigned long long*)(ws + (210u << 20)) = __builtin_amdgcn_s_memrealtime(); }
extern "C" void kernel_launch(void* const* d_in, const int* in_sizes, int n_in,
                              void* d_out, int out_size, void* d_ws, size_t ws_size,
                              hipStream_t stream) {
    const u16* x = (const u16*)d_in[0];
    const u16* ea = (const u16*)d_in[1];
    const int* src = (const int*)d_in[2];
    const int* dst = (const int*)d_in[3];
    const u16* wa = (const u16*)d_in[4];
    const u16* wb = (const u16*)d_in[5];
    const u16* wc = (const u16*)d_in[6];
    const u16* wu = (const u16*)d_in[7];
    const u16* wv = (const u16*)d_in[8];
    k_first<<<1, 64, 0, stream>>>((char*)d_ws);
    (void)hipFuncSetAttribute((const void*)k1_gemm_bin, hipFuncAttributeMaxDynamicSharedMemorySize, K1_LDS);
    k1_gemm_bin<<<256, 512, K1_LDS, stream>>>(x, wa, wb, wu, wv, src, dst, ea, (char*)d_ws);
    (void)hipFuncSetAttribute((const void*)k2_edges, hipFuncAttributeMaxDynamicSharedMemorySize, K2_LDS);
    k2_edges<<<256, 1024, K2_LDS, stream>>>(x, wc, src, dst, ea, (const char*)d_ws, (u16*)d_out);
}
```

```cpp
#define MEASURE 0
#define SPAN 1
#include <hip/hip_runtime.h>
#include <hip/hip_bf16.h>
#include <stdint.h>

typedef unsigned short u16;
typedef unsigned int u32;
typedef u32 u32x2 __attribute__((ext_vector_type(2)));
typedef u32 u32x4 __attribute__((ext_vector_type(4)));
typedef float f32x2 __attribute__((ext_vector_type(2)));
typedef float f32x4 __attribute__((ext_vector_type(4)));
typedef float f32x16 __attribute__((ext_vector_type(16)));
typedef short s16x4 __attribute__((ext_vector_type(4)));
typedef short s16x8 __attribute__((ext_vector_type(8)));
typedef __bf16 bf16x8 __attribute__((ext_vector_type(8)));
typedef __bf16 bf16x4 __attribute__((ext_vector_type(4)));
typedef __bf16 bf16x2 __attribute__((ext_vector_type(2)));

constexpr int NN = 10000;
constexpr int NE = 160000;
constexpr int DD = 256;
constexpr int NP = 10048;
constexpr int RG = 320;
constexpr int NBK = 64;
constexpr int NB = 157;
constexpr int NCH = 64;
constexpr int CHE = 2500;
constexpr int CAP = 80;
constexpr int CAPN = 44;
constexpr int LST = 45;
constexpr float NLOG2E = -1.4426950408889634f;
constexpr u32 NEUTRAL_REC = (u32)NN;
constexpr u32 NEUTRAL_BV = 0x00007F00u;

constexpr size_t OFF_BV = 0;
constexpr size_t OFF_AU = OFF_BV + (size_t)4 * NP * 256;
constexpr size_t OFF_R4 = OFF_AU + (size_t)4 * NP * 256;
constexpr size_t OFF_R2 = OFF_R4 + (size_t)NBK * NCH * CAP * 4;
constexpr size_t OFF_CNT = OFF_R2 + (size_t)NBK * NCH * CAP * 2;

#define LDS_AS __attribute__((address_space(3)))
#define GLB_AS __attribute__((address_space(1)))

__device__ __forceinline__ void glds16(const void* g, void* l) {
    __builtin_amdgcn_global_load_lds((const GLB_AS u32*)g, (LDS_AS u32*)l, 16, 0, 0);
}
__device__ __forceinline__ void glds16a(const void* sbase, u32 voff, u32 lds) {
    u32 keep;
    asm volatile("s_mov_b32 %0, m0\n\ts_mov_b32 m0, %3\n\ts_nop 0\n\tglobal_load_lds_dwordx4 %1, %2\n\ts_mov_b32 m0, %0" : "=&s"(keep) : "v"(voff), "s"(sbase), "s"(lds) : "memory");
}
__device__ __forceinline__ u32 cvt_pk_bf16(float lo, float hi) {
    const f32x2 v = {lo, hi}; return __builtin_bit_cast(u32, __builtin_convertvector(v, bf16x2));
}
#define WAIT_V(n) asm volatile("s_waitcnt vmcnt(" #n ")" ::: "memory")
#define WAIT_L(n) asm volatile("s_waitcnt lgkmcnt(" #n ")" ::: "memory")
#define BAR() __builtin_amdgcn_s_barrier()
#define EXIT2_AT 99
#ifndef EXIT_AT
#define EXIT_AT 99
#endif
#define STORE_AUX 16
#define SPAN0() unsigned long long span_t0_ = __builtin_amdgcn_s_memrealtime()
#define SPAN1(K) do { asm volatile("s_waitcnt vmcnt(0) lgkmcnt(0)" ::: "memory"); unsigned long long t1_ = __builtin_amdgcn_s_memrealtime(); if ((threadIdx.x & 63) == 0) { unsigned long long* p_ = (unsigned long long*)((char*)ws + (192u << 20)) + (((K) * 256 + blockIdx.x) * 16 + (threadIdx.x >> 6)) * 2; p_[0] = span_t0_; p_[1] = t1_; } } while (0)
#define STAMP(i)
#define STAMPC(i)
#define TICK(v)

constexpr int K1_R0 = 0, K1_R1 = 65536, K1_SCR = 131072;
constexpr int K1_ST4 = K1_SCR, K1_ST2 = K1_ST4 + NBK * CAP * 4, K1_BC = K1_ST2 + NBK * CAP * 2;
constexpr int K1_LDS = K1_SCR + 32768;
static_assert(K1_BC + NBK * 4 <= K1_LDS, "lds");

__global__ __launch_bounds__(512, 2) void k1_gemm_bin(
    const u16* __restrict__ x, const u16* __restrict__ wa, const u16* __restrict__ wb, const u16* __restrict__ wu, const u16* __restrict__ wv,
    const int* __restrict__ src, const int* __restrict__ dst, const u16* __restrict__ ea, char* __restrict__ ws) {
    extern __shared__ __attribute__((aligned(16))) char smem[];
    const int tid = threadIdx.x, lane = tid & 63;
    const int w = __builtin_amdgcn_readfirstlane(tid >> 6);
    const int b = blockIdx.x;
    const int type = (b >> 5) & 1;
    const int ct2 = (b >> 3) & 3;
    const int rg = (b & 7) * 4 + (b >> 6);
    const int row0 = rg * RG;
    const int chunk = (b & 31) | ((b >> 6) << 5) ;
    const bool binner = type == 1 && b < 128;
    constexpr int KOFF = 0;
    SPAN0();
#if EXIT_AT == 0
    if (ws) { SPAN1(0); return; }
#endif
    STAMP(0);
    int bd[5], bs[5]; u32 be[5];
    if (binner) {
#pragma unroll
        for (int i = 0; i < 5; ++i) {
            const int k = tid + 512 * i;
            const int e = chunk * CHE + (k < CHE ? k : 0);
            bd[i] = dst[e]; bs[i] = src[e]; be[i] = ea[e];
        }
        if (tid < NBK) *(u32*)(smem + K1_BC + tid * 4) = 0;
    }
    {
        const int m = w >> 2;
        const u16* W = type == 0 ? (m == 0 ? wb : wv) : (m == 0 ? wa : wu);
        const int kb0 = (w & 3) * 64, kr = lane >> 3;
        const u16* g = W + (size_t)(kb0 + kr) * DD + ct2 * 64 + (((lane & 7) ^ ((kr & 2) << 1)) << 3);
#pragma unroll
        for (int i = 0; i < 8; ++i) glds16(g + (size_t)i * 8 * DD, smem + K1_R0 + m * 32768 + (kb0 / 8 + i) * 1024);
    }
    const int xr = lane >> 5, xp = lane & 31;
    auto stage_x = [&](int tile, int q, int ldsbase) {
        const int r = q * 2 + xr;
        int grow = row0 + tile * 32 + r; grow = grow < NN ? grow : NN - 1;
        glds16((const char*)x + (u32)(grow * 512 + ((xp ^ (r & 15)) << 4)), smem + ldsbase + q * 1024);
    };
#pragma unroll
    for (int i = 0; i < 8; ++i) { const int q = w * 8 + i; stage_x(q >> 4, q & 15, K1_R1 + (q >> 4) * 16384); }
    STAMP(1);
    WAIT_V(8); WAIT_L(0);
    BAR();
    STAMP(2);
#if EXIT_AT == 1
    if (ws) { SPAN1(0); return; }
#endif
    const int ch = w >> 2;
    bf16x8 wf0[16], wf1[16];
    {
        const int h = lane >> 5, blk = (lane >> 4) & 1, q = (lane & 15) >> 2, p = lane & 3;
        const u32 base = K1_R0 + (8 * h + q) * 128 + (((ch * 4 + blk * 2 + (p >> 1)) ^ ((q & 2) << 1)) << 4) + (p & 1) * 8;
#define TRRD(S) { u32x2 a0, a1, b0, b1; \
            asm volatile("ds_read_b64_tr_b16 %0, %4 offset:%c5\n\tds_read_b64_tr_b16 %1, %4 offset:%c6\n\t" \
                         "ds_read_b64_tr_b16 %2, %4 offset:%c7\n\tds_read_b64_tr_b16 %3, %4 offset:%c8" \
                         : "=&v"(a0), "=&v"(a1), "=&v"(b0), "=&v"(b1) \
                         : "v"(base), "i"((S) * 2048), "i"((S) * 2048 + 512), "i"(32768 + (S) * 2048), "i"(32768 + (S) * 2048 + 512) : "memory"); \
            wf0[S] = __builtin_bit_cast(bf16x8, __builtin_shufflevector(a0, a1, 0, 1, 2, 3)); \
            wf1[S] = __builtin_bit_cast(bf16x8, __builtin_shufflevector(b0, b1, 0, 1, 2, 3)); }
        TRRD(0) TRRD(1) TRRD(2) TRRD(3) TRRD(4) TRRD(5) TRRD(6) TRRD(7) TRRD(8) TRRD(9) TRRD(10) TRRD(11) TRRD(12) TRRD(13) TRRD(14) TRRD(15)
#undef TRRD
    }
    if (binner) {
#pragma unroll
        for (int i = 0; i < 5; ++i) {
            const int k = tid + 512 * i;
            if (k < CHE) {
                const int bk = (int)(((u32)bd[i] * 53432u) >> 23);
                const int dl = bd[i] - bk * NB;
                const u32 pos = atomicAdd((u32*)(smem + K1_BC + bk * 4), 1u);
                if (pos < CAP) {
                    *(u32*)(smem + K1_ST4 + (bk * CAP + pos) * 4) = (u32)bs[i] | (be[i] << 16);
                    *(u16*)(smem + K1_ST2 + (bk * CAP + pos) * 2) = (u16)dl;
                }
            }
        }
    }
    STAMP(3);
    WAIT_V(0); WAIT_L(0);
    __builtin_amdgcn_sched_barrier(0);
    BAR();
    STAMP(4);
#if EXIT_AT == 2
    if (ws) { asm volatile("" :: "v"(wf0[0]), "v"(wf1[15])); SPAN1(0); return; }
#endif
#pragma unroll
    for (int i = 0; i < 8; ++i) { const int q = w * 8 + i; stage_x(4 + (q >> 4), q & 15, K1_R0 + (q >> 4) * 16384); }
    if (binner) {
        u32* R4 = (u32*)(ws + OFF_R4); u16* R2 = (u16*)(ws + OFF_R2); u32* CNT = (u32*)(ws + OFF_CNT);
        u32x4 t4[3]; u32x2 t2[3];
#pragma unroll
        for (int i = 0; i < 3; ++i) { const int q = tid + 512 * i; const int qq = q < NBK * (CAP / 4) ? q : 0;
            t4[i] = *(const u32x4*)(smem + K1_ST4 + qq * 16); t2[i] = *(const u32x2*)(smem + K1_ST2 + qq * 8); }
        const u32 cv = *(const u32*)(smem + K1_BC + (tid & 63) * 4);
#pragma unroll
        for (int i = 0; i < 3; ++i) { const int q = tid + 512 * i;
            if (q < NBK * (CAP / 4)) { const int bk = q / (CAP / 4), o = q - bk * (CAP / 4);
                *(u32x4*)(R4 + ((size_t)bk * NCH + chunk) * CAP + o * 4) = t4[i];
                *(u32x2*)(R2 + ((size_t)bk * NCH + chunk) * CAP + o * 4) = t2[i]; } }
        if (tid < NBK) CNT[tid * NCH + chunk] = cv;
        WAIT_L(0);
        BAR();
    }
    if (rg == 0 && type == 0 && w == 0) ((u32*)(ws + OFF_BV))[((size_t)ct2 * NP + NN) * 64 + lane] = NEUTRAL_BV;

    const int r = lane & 31, h = lane >> 5;
    const int abase = r * 512 + ((((r & 14) | ((h ^ r) & 1))) << 4);
    char* const scr = smem + K1_SCR + w * 4096;
    const __amdgpu_buffer_rsrc_t trs = __builtin_amdgcn_make_buffer_rsrc(ws + (type ? OFF_AU : OFF_BV) + (size_t)ct2 * NP * 256, 0, NN * 256, 0x00020000);
    const u32 svo = (lane >> 3) * 256 + ch * 128 + (lane & 7) * 16;

    const int s4 = w & 3;
    if (ch == 0) __builtin_amdgcn_s_setprio(1);
    STAMP(5);
#pragma unroll 1
    for (int st = 0; st < (EXIT_AT == 3 ? 1 : (EXIT_AT == 4 ? 2 : 3)); ++st) {
        int tile, ldsbase;
        if (st == 0) { tile = s4; ldsbase = K1_R1 + s4 * 16384; }
        else if (st == 1) { tile = 4 + s4; ldsbase = K1_R0 + s4 * 16384; }
        else { const int t = (w < 2) ? w : (w >= 6 ? w - 6 : 99); tile = 8 + t; ldsbase = K1_R1 + (t & 1) * 16384; }
        const int trow = row0 + tile * 32;
        const bool active = tile < 10 && trow < NN;
        unsigned long long tkA = 0, tkB = 0, tkC = 0, tkD = 0;
        if (active) {
            TICK(tkA);
            bf16x8 a[16];
#pragma unroll
            for (int s = 0; s < 8; ++s) a[s] = *(const bf16x8*)(smem + ldsbase + (abase ^ (s << 5)));
            __builtin_amdgcn_sched_barrier(0);
            f32x16 acc0, acc1;
#pragma unroll
            for (int i = 0; i < 16; ++i) { acc0[i] = 0.f; acc1[i] = 0.f; }
#pragma unroll
            for (int s = 0; s < 8; ++s) {
                a[s + 8] = *(const bf16x8*)(smem + ldsbase + (abase ^ (s << 5)) + 256);
                acc0 = __builtin_amdgcn_mfma_f32_32x32x16_bf16(a[s], wf0[s], acc0, 0, 0, 0);
                acc1 = __builtin_amdgcn_mfma_f32_32x32x16_bf16(a[s], wf1[s], acc1, 0, 0, 0);
                __builtin_amdgcn_sched_group_barrier(0x100, 1, 0);
                __builtin_amdgcn_sched_group_barrier(0x8, 2, 0);
            }
            __builtin_amdgcn_sched_barrier(0);
#pragma unroll
            for (int s = 8; s < 16; ++s) {
                acc0 = __builtin_amdgcn_mfma_f32_32x32x16_bf16(a[s], wf0[s], acc0, 0, 0, 0);
                acc1 = __builtin_amdgcn_mfma_f32_32x32x16_bf16(a[s], wf1[s], acc1, 0, 0, 0);
            }
            TICK(tkB);
            char* const sw = scr + h * 512 + r * 4;
            if (type == 0) {
#pragma unroll
                for (int i = 0; i < 16; i += 2) {
                    const u32 bb = cvt_pk_bf16(acc0[i] * NLOG2E, acc0[i + 1] * NLOG2E);
#pragma unroll
                    for (int j = 0; j < 2; ++j) {
                        const u32 vb = __float_as_uint(acc1[i + j]);
                        const u32 bl = j ? (bb >> 16) : (bb & 0xFFFFu);
                        const u32 u = __builtin_elementwise_sub_sat((vb & 0x7FFFFFFFu) + 0x8000u, bl);
                        *(u32*)(sw + (((i + j) & 3) + 8 * ((i + j) >> 2)) * 128) = (vb & 0x80000000u) | (u & 0x7FFF0000u) | bl;
                    }
                }
            } else {
#pragma unroll
                for (int i = 0; i < 16; ++i) *(u32*)(sw + ((i & 3) + 8 * (i >> 2)) * 128) = cvt_pk_bf16(acc0[i] * NLOG2E, acc1[i]);
            }
            TICK(tkC);
#pragma unroll
            for (int i = 0; i < 4; ++i) {
                const u32x4 v = *(const u32x4*)(scr + i * 1024 + lane * 16);
                __builtin_amdgcn_raw_buffer_store_b128(v, trs, svo + (u32)(trow + i * 8) * 256, 0, STORE_AUX);
            }
        }
        TICK(tkD);
        if (st == 0) STAMP(6); else if (st == 1) STAMP(8); else STAMP(10);
        if (st < 2) {
            if (active) WAIT_V(4); else WAIT_V(0);
            BAR();
            if (st == 0) {
#pragma unroll
                for (int i = 0; i < 4; ++i) { const int q = w * 4 + i; stage_x(8 + (q >> 4), q & 15, K1_R1 + (q >> 4) * 16384); }
            }
        }
    }
    SPAN1(0);
}

constexpr int LSTB = LST * 4;
constexpr int K2_X = 0;
constexpr int K2_AU = K2_X + 20480;
constexpr int K2_LIDX = K2_AU + 40960;
constexpr int K2_LSZ = (NB * LSTB + 32 + 256 + 15) / 16 * 16;
constexpr int K2_LE = K2_LIDX + K2_LSZ;
constexpr int K2_CNT = K2_LE + K2_LSZ;
constexpr int K2_HIST = K2_CNT + 160 * 4;
constexpr int K2_ORD = K2_HIST + 128 * 4;
constexpr int K2_MISC = K2_ORD + 80 * 32;
constexpr int K2_LDS = K2_MISC + 16;
constexpr int K2_TRASH = NB * LSTB + 32;
static_assert(K2_TRASH + 256 <= K2_LSZ, "trash");
static_assert(K2_LSZ < 65536 - 64, "ds offset");

__device__ __forceinline__ float dot2sel(u32 d, u32 sel, float c) {
    return __builtin_amdgcn_fdot2_f32_bf16(__builtin_bit_cast(bf16x2, d), __builtin_bit_cast(bf16x2, sel), c, false);
}

struct Acc { f32x2 n01, n23, d01, d23; };

__device__ __forceinline__ void edge_step(Acc& a, const u32x4 d, const u32 rec, const f32x2 a01, const f32x2 a23, const f32x2 w01, const f32x2 w23, const u32 sel) {
    const float e = __uint_as_float(rec & 0xFFFF0000u);
    const f32x2 ee = {e, e};
    const f32x2 t01 = __builtin_elementwise_fma(ee, w01, a01);
    const f32x2 t23 = __builtin_elementwise_fma(ee, w23, a23);
    f32x2 E01, E23;
    E01.x = __builtin_amdgcn_exp2f(dot2sel(d.x, sel, t01.x));
    E01.y = __builtin_amdgcn_exp2f(dot2sel(d.y, sel, t01.y));
    E23.x = __builtin_amdgcn_exp2f(dot2sel(d.z, sel, t23.x));
    E23.y = __builtin_amdgcn_exp2f(dot2sel(d.w, sel, t23.y));
    const f32x2 one = {1.f, 1.f};
    E01 += one; E23 += one;
    f32x2 s01, s23;
    s01.x = __builtin_amdgcn_rcpf(E01.x); s01.y = __builtin_amdgcn_rcpf(E01.y);
    s23.x = __builtin_amdgcn_rcpf(E23.x); s23.y = __builtin_amdgcn_rcpf(E23.y);
    a.d01 += s01; a.d23 += s23;
    const f32x2 v01 = {__uint_as_float(d.x), __uint_as_float(d.y)};
    const f32x2 v23 = {__uint_as_float(d.z), __uint_as_float(d.w)};
    a.n01 = __builtin_elementwise_fma(s01, v01, a.n01);
    a.n23 = __builtin_elementwise_fma(s23, v23, a.n23);
}

__device__ __forceinline__ void node_finish(const Acc& a, const u32x4 au, const u32x2 xv, u16* outp) {
    const float num[4] = {a.n01.x, a.n01.y, a.n23.x, a.n23.y};
    const float den[4] = {a.d01.x, a.d01.y, a.d23.x, a.d23.y};
    const u32 aus[4] = {au.x, au.y, au.z, au.w};
    const float xs[4] = {__uint_as_float(xv.x << 16), __uint_as_float(xv.x & 0xFFFF0000u), __uint_as_float(xv.y << 16), __uint_as_float(xv.y & 0xFFFF0000u)};
    float o[4];
#pragma unroll
    for (int c = 0; c < 4; ++c) {
        const float z = __uint_as_float(aus[c] & 0xFFFF0000u) + num[c] * __builtin_amdgcn_rcpf(den[c] + 1e-6f);
        const float rl = z > 0.f ? z : 0.f * z;
        o[c] = xs[c] + rl;
    }
    u32x2 ov; ov.x = cvt_pk_bf16(o[0], o[1]); ov.y = cvt_pk_bf16(o[2], o[3]);
    *(u32x2*)outp = ov;
}

template <int CTRL, int RM> __device__ __forceinline__ u32 dpp_add(u32 v) {
    return v + (u32)__builtin_amdgcn_update_dpp(0, (int)v, CTRL, RM, 0xf, false);
}

__global__ __launch_bounds__(1024, 4) void k2_edges(
    const u16* __restrict__ x, const u16* __restrict__ wc, const int* __restrict__ src, const int* __restrict__ dst, const u16* __restrict__ ea,
    const char* __restrict__ ws, u16* __restrict__ out) {
    extern __shared__ __attribute__((aligned(16))) char smem[];
    const int tid = threadIdx.x, lane = tid & 63;
    const int w = __builtin_amdgcn_readfirstlane(tid >> 6);
    const int b = blockIdx.x;
    const int slice = (b & 7) >> 1;
    const int bucket = (b >> 3) * 2 + (b & 1);
    const int node0 = bucket * NB;
    const int nvalid = (NN - node0) < NB ? (NN - node0) : NB;
    u32* const cnt = (u32*)(smem + K2_CNT);
    u32* const hist = (u32*)(smem + K2_HIST);
    u32* const misc = (u32*)(smem + K2_MISC);
    constexpr int KOFF = 256 * 16;
    SPAN0();
    STAMP(0);

    const u32* R4 = (const u32*)(ws + OFF_R4) + (size_t)bucket * NCH * CAP;
    const u16* R2 = (const u16*)(ws + OFF_R2) + (size_t)bucket * NCH * CAP;
    const u32* CNT = (const u32*)(ws + OFF_CNT) + bucket * NCH;
    const int ch0 = tid / (CAP / 4), ch1 = (tid + 1024) / (CAP / 4);
    const u32x4 r40 = *(const u32x4*)(R4 + tid * 4);
    const u32x2 r20 = *(const u32x2*)(R2 + tid * 4);
    const u32 c0 = CNT[ch0];
    u32x4 r41 = {0, 0, 0, 0}; u32x2 r21 = {0, 0}; u32 c1 = 0;
    if (w < 4) { r41 = *(const u32x4*)(R4 + (tid + 1024) * 4); r21 = *(const u32x2*)(R2 + (tid + 1024) * 4); c1 = CNT[ch1]; }
    const char* const bvb = ws + OFF_BV + (size_t)slice * NP * 256;
    const char* const aub = ws + OFF_AU + (size_t)slice * NP * 256;
    const int g = lane >> 4, j = lane & 15;
    const u32x2 wcv = *(const u32x2*)(wc + slice * 64 + j * 4);
    for (int i = tid; i < K2_LSZ / 16; i += 1024) {
        *(u32x4*)(smem + K2_LIDX + i * 16) = (u32x4){NEUTRAL_REC, NEUTRAL_REC, NEUTRAL_REC, NEUTRAL_REC};
        *(u32x4*)(smem + K2_LE + i * 16) = (u32x4){0, 0, 0, 0};
    }
    if (tid < 160 + 64) cnt[tid] = 0;
    if (tid == 0) { misc[0] = 16; misc[1] = 0; }
    f32x2 w01 = {__uint_as_float(wcv.x << 16) * NLOG2E, __uint_as_float(wcv.x & 0xFFFF0000u) * NLOG2E};
    f32x2 w23 = {__uint_as_float(wcv.y << 16) * NLOG2E, __uint_as_float(wcv.y & 0xFFFF0000u) * NLOG2E};
    asm volatile("" : "+v"(w01), "+v"(w23));
    STAMP(1);
    WAIT_V(0); WAIT_L(0); BAR();
    {
        const char* ga = aub + (size_t)node0 * 256;
#pragma unroll
        for (int i = 0; i < 3; ++i) { const int q = w + 16 * i; if (q < 40) glds16a(ga, q * 1024 + lane * 16, K2_AU + q * 1024); }
        const char* gx = (const char*)x + slice * 128;
#pragma unroll
        for (int i = 0; i < 2; ++i) {
            const int q = w + 16 * i;
            if (q < 20) { int row = node0 + q * 8 + (lane >> 3); row = row < NN ? row : NN - 1;
                glds16a(gx, row * 512 + (lane & 7) * 16, K2_X + q * 1024); }
        }
    }
    STAMP(2);
    {
        const int p0 = (tid - ch0 * (CAP / 4)) * 4;
        const u32 rec0[4] = {r40.x, r40.y, r40.z, r40.w};
        const u32 dl0[4] = {r20.x & 0xFFFF, r20.x >> 16, r20.y & 0xFFFF, r20.y >> 16};
        u32 slot[4];
        bool ovf = c0 > CAP;
#pragma unroll
        for (int k = 0; k < 4; ++k) slot[k] = atomicAdd(&cnt[p0 + k < c0 ? dl0[k] : 224 + lane], 1u);
        if (w < 4) {
            const int p1 = (tid + 1024 - ch1 * (CAP / 4)) * 4;
            const u32 rec1[4] = {r41.x, r41.y, r41.z, r41.w};
            const u32 dl1[4] = {r21.x & 0xFFFF, r21.x >> 16, r21.y & 0xFFFF, r21.y >> 16};
            u32 slot1[4];
            ovf |= c1 > CAP;
#pragma unroll
            for (int k = 0; k < 4; ++k) slot1[k] = atomicAdd(&cnt[p1 + k < c1 ? dl1[k] : 224 + lane], 1u);
#pragma unroll
            for (int k = 0; k < 4; ++k) {
                const bool v = p1 + k < c1, ok = v && slot1[k] < CAPN;
                ovf |= v && !ok;
                const u32 o = ok ? dl1[k] * LSTB + slot1[k] * 4 : K2_TRASH + lane * 4;
                *(u32*)(smem + K2_LIDX + o) = rec1[k] & 0xFFFFu;
                *(u32*)(smem + K2_LE + o) = rec1[k] & 0xFFFF0000u;
            }
        }
#pragma unroll
        for (int k = 0; k < 4; ++k) {
            const bool v = p0 + k < c0, ok = v && slot[k] < CAPN;
            ovf |= v && !ok;
            const u32 o = ok ? dl0[k] * LSTB + slot[k] * 4 : K2_TRASH + lane * 4;
            *(u32*)(smem + K2_LIDX + o) = rec0[k] & 0xFFFFu;
            *(u32*)(smem + K2_LE + o) = rec0[k] & 0xFFFF0000u;
        }
        if (ovf) misc[1] = 1;
    }
    STAMP(3);
    WAIT_L(0); BAR();
    STAMP(4);
    const bool slow = misc[1] != 0;

    const u32 joff = j * 16;

    if (slow) {
        u32 sel = 0x00003F80u;
        asm volatile("" : "+s"(sel));
        for (int task = w; task * 4 < nvalid; task += 16) {
            const int n = task * 4 + g;
            const bool nv = n < nvalid;
            const int node = node0 + (nv ? n : 0);
            const u32x4 au = *(const u32x4*)(aub + (size_t)node * 256 + joff);
            const u32x2 xv = *(const u32x2*)(x + (size_t)node * DD + slice * 64 + j * 4);
            const f32x2 a01 = {__uint_as_float(au.x << 16), __uint_as_float(au.y << 16)};
            const f32x2 a23 = {__uint_as_float(au.z << 16), __uint_as_float(au.w << 16)};
            Acc acc = {{0.f, 0.f}, {0.f, 0.f}, {0.f, 0.f}, {0.f, 0.f}};
            const int lo = node0 + task * 4;
            for (int e0 = 0; e0 < NE; e0 += 64) {
                const int d = dst[e0 + lane];
                unsigned long long m = __ballot((u32)(d - lo) < 4u);
                if (m) {
                    const u32 rc = (u32)src[e0 + lane] | ((u32)ea[e0 + lane] << 16);
                    while (m) {
                        const int l = __builtin_ctzll(m); m &= m - 1;
                        const int dd = __builtin_amdgcn_readlane(d, l);
                        const u32 rr = __builtin_amdgcn_readlane(rc, l);
                        const u32 rec = (nv && dd == node) ? rr : NEUTRAL_REC;
                        const u32x4 dv = *(const u32x4*)(bvb + (size_t)((rec & 0xFFFFu) * 256 + joff));
                        edge_step(acc, dv, rec, a01, a23, w01, w23, sel);
                    }
                }
            }
            if (nv) node_finish(acc, au, xv, out + (size_t)node * DD + slice * 64 + j * 4);
        }
        return;
    }

    u32 mydeg = 0, myrank = 0;
    if (tid < nvalid) { mydeg = cnt[tid]; myrank = atomicAdd(&hist[63 - mydeg], 1u); }
    WAIT_L(0); BAR();
    if (w < 3) {
        const u32 h0 = hist[lane];
        u32 v = h0;
        v = dpp_add<0x111, 0xf>(v); v = dpp_add<0x112, 0xf>(v); v = dpp_add<0x114, 0xf>(v); v = dpp_add<0x118, 0xf>(v);
        v = dpp_add<0x142, 0xa>(v); v = dpp_add<0x143, 0xc>(v);
        const u32 base = (u32)__builtin_amdgcn_ds_bpermute((int)((63 - mydeg) * 4), (int)(v - h0));
        if (tid < nvalid) {
            const u32 pos = base + myrank;
            const u32x2 ent = {(u32)(K2_LIDX + tid * LSTB), (u32)(tid * 256) + mydeg};
            u32x2* o = (u32x2*)(smem + K2_ORD) + pos;
            o[0] = ent;
            if (pos == (u32)nvalid - 1) { o[1] = ent; o[2] = ent; o[3] = ent; }
        }
    }
    WAIT_V(0); WAIT_L(0); BAR();
    STAMP(5);
    STAMPC(14);

    const int ntask = (nvalid + 3) >> 2;
    if (w < ntask) {
        u32x4 srdBV, srdO;
        { const size_t p = (size_t)bvb; srdBV = (u32x4){(u32)p, (u32)(p >> 32) | (256u << 16), 0xFFFFFFFFu, 0x00020000u}; }
        { const size_t ob = (size_t)node0 * 512 + slice * 128; const size_t p = (size_t)out + ob; srdO = (u32x4){(u32)p, (u32)(p >> 32), (u32)((size_t)NN * 512 - ob), 0x00020000u}; }
        u32 g8 = K2_ORD + g * 8, ctr = K2_MISC, one = 1, j8 = j * 8;
        unsigned long long eps2 = 0x358637BD358637BDull;
        u32 s_cnt, s_nxt, s_t, s_tc;
#define STAGE(DS0, DS1, DS2, DS3, DSL, DSH, IX, EP, E1, DN, IXN, O1, O2) \
            "s_waitcnt lgkmcnt(0)\n\t" \
            "buffer_load_dwordx4 " DN ", " GIX(IXN) ", %[bv], 0 idxen offen\n\t" \
            "v_pk_fma_f32 v[112:113], " EP ", v[100:101], v[96:97] op_sel_hi:[0,1,1]\n\t" \
            "v_pk_fma_f32 v[114:115], " EP ", v[102:103], v[98:99] op_sel_hi:[0,1,1]\n\t" \
            "ds_read_b32 " IX ", v89 offset:" O1 "\n\t" \
            "ds_read_b32 " E1 ", v89 offset:" O2 "\n\t" \
            "s_waitcnt vmcnt(3)\n\t" \
            "v_dot2c_f32_bf16 v112, %[slo], " DS0 "\n\t" \
            "v_dot2c_f32_bf16 v113, %[slo], " DS1 "\n\t" \
            "v_dot2c_f32_bf16 v114, %[slo], " DS2 "\n\t" \
            "v_dot2c_f32_bf16 v115, %[slo], " DS3 "\n\t" \
            "v_exp_f32 v112, v112\n\t" \
            "v_exp_f32 v113, v113\n\t" \
            "v_exp_f32 v114, v114\n\t" \
            "v_exp_f32 v115, v115\n\t" \
            "v_pk_add_f32 v[112:113], v[112:113], 1.0 op_sel_hi:[1,0]\n\t" \
            "v_pk_add_f32 v[114:115], v[114:115], 1.0 op_sel_hi:[1,0]\n\t" \
            "v_rcp_f32 v112, v112\n\t" \
            "v_rcp_f32 v113, v113\n\t" \
            "v_rcp_f32 v114, v114\n\t" \
            "v_rcp_f32 v115, v115\n\t" \
            "s_sub_u32 %[cnt], %[cnt], 1\n\t" \
            "v_pk_add_f32 v[108:109], v[108:109], v[112:113]\n\t" \
            "v_pk_fma_f32 v[104:105], v[112:113], " DSL ", v[104:105]\n\t" \
            "v_pk_add_f32 v[110:111], v[110:111], v[114:115]\n\t" \
            "v_pk_fma_f32 v[106:107], v[114:115], " DSH ", v[106:107]\n\t" \
            "s_cmp_eq_u32 %[cnt], 0\n\t"
#define GIX(x) x
#define PRIO_ON 1
#define PRIO_INIT "s_setprio 3\n\t"
#define PRIO_STEP \
            "s_add_u32 %[tc], %[tc], 1\n\t" \
            "s_cmp_lg_u32 %[tc], 1\n\t s_cbranch_scc1 L_p1%=\n\t s_setprio 2\n\t L_p1%=:\n\t" \
            "s_cmp_lg_u32 %[tc], 2\n\t s_cbranch_scc1 L_p2%=\n\t s_setprio 1\n\t L_p2%=:\n\t" \
            "s_cmp_lg_u32 %[tc], 3\n\t s_cbranch_scc1 L_p3%=\n\t s_setprio 0\n\t L_p3%=:\n\t"
        asm volatile(
            PRIO_INIT
            "s_mov_b32 %[tc], 0\n\t"
            "s_lshl_b32 %[t], %[task], 5\n\t"
            "v_add_u32 v127, %[t], v91\n\t"
            "ds_read_b64 v[122:123], v127\n\t"
            "s_mov_b64 exec, 1\n\t"
            "ds_add_rtn_u32 v125, v93, v95\n\t"
            "s_mov_b64 exec, -1\n\t"
            "L_task%=:\n\t"
            "s_waitcnt lgkmcnt(0)\n\t"
            "v_readfirstlane_b32 %[nxt], v125\n\t"
            "v_readfirstlane_b32 %[cnt], v123\n\t"
            "v_mov_b32 v89, v122\n\t"
            "v_and_b32 v127, 0xffffff00, v123\n\t"
            "ds_read_b32 v80, v89\n\t"
            "ds_read_b32 v82, v89 offset:4\n\t"
            "ds_read_b32 v84, v89 offset:8\n\t"
            "ds_read_b32 v86, v89 offset:12\n\t"
            "ds_read_b32 v88, v89 offset:%c[le0]\n\t"
            "ds_read_b32 v90, v89 offset:%c[le1]\n\t"
            "ds_read_b32 v92, v89 offset:%c[le2]\n\t"
            "ds_read_b32 v94, v89 offset:%c[le3]\n\t"
            "v_add_u32 v112, v127, v81\n\t"
            "v_lshl_add_u32 v124, v127, 1, v126\n\t"
            "v_lshrrev_b32 v113, 1, v112\n\t"
            "ds_read_b128 v[116:119], v112 offset:%c[au]\n\t"
            "ds_read_b64 v[120:121], v113\n\t"
            "s_and_b32 %[cnt], %[cnt], 0xff\n\t"
            "s_lshl_b32 %[t], %[nxt], 5\n\t"
            "v_add_u32 v127, %[t], v91\n\t"
            "ds_read_b64 v[122:123], v127\n\t"
            "s_mov_b64 exec, 1\n\t"
            "ds_add_rtn_u32 v125, v93, v95\n\t"
            "s_mov_b64 exec, -1\n\t"
            "s_waitcnt lgkmcnt(9)\n\t"
            "buffer_load_dwordx4 v[64:67], v[80:81], %[bv], 0 idxen offen\n\t"
            "buffer_load_dwordx4 v[68:71], v[82:83], %[bv], 0 idxen offen\n\t"
            "buffer_load_dwordx4 v[72:75], v[84:85], %[bv], 0 idxen offen\n\t"
            "v_mov_b64 v[104:105], 0\n\t"
            "v_mov_b64 v[106:107], 0\n\t"
            "v_mov_b64 v[108:109], 0\n\t"
            "v_mov_b64 v[110:111], 0\n\t"
            "s_waitcnt lgkmcnt(2)\n\t"
            "v_lshlrev_b32 v96, 16, v116\n\t"
            "v_lshlrev_b32 v97, 16, v117\n\t"
            "v_lshlrev_b32 v98, 16, v118\n\t"
            "v_lshlrev_b32 v99, 16, v119\n\t"
            "s_cmp_eq_u32 %[cnt], 0\n\t"
            "s_cbranch_scc1 L_fin%=\n\t"
            "L_loop%=:\n\t"
            STAGE("v64", "v65", "v66", "v67", "v[64:65]", "v[66:67]", "v80", "v[88:89]", "v88", "v[76:79]", "v[86:87]", "16", "%c[le4]")
            "s_cbranch_scc1 L_fin%=\n\t"
            STAGE("v68", "v69", "v70", "v71", "v[68:69]", "v[70:71]", "v82", "v[90:91]", "v90", "v[64:67]", "v[80:81]", "20", "%c[le5]")
            "s_cbranch_scc1 L_fin%=\n\t"
            STAGE("v72", "v73", "v74", "v75", "v[72:73]", "v[74:75]", "v84", "v[92:93]", "v92", "v[68:71]", "v[82:83]", "24", "%c[le6]")
            "s_cbranch_scc1 L_fin%=\n\t"
            STAGE("v76", "v77", "v78", "v79", "v[76:77]", "v[78:79]", "v86", "v[94:95]", "v94", "v[72:75]", "v[84:85]", "28", "%c[le7]")
            "v_add_u32 v89, 16, v89\n\t"
            "s_cbranch_scc0 L_loop%=\n\t"
            "L_fin%=:\n\t"
            "v_pk_add_f32 v[108:109], v[108:109], %[eps]\n\t"
            "v_pk_add_f32 v[110:111], v[110:111], %[eps]\n\t"
            "v_rcp_f32 v108, v108\n\t"
            "v_rcp_f32 v109, v109\n\t"
            "v_rcp_f32 v110, v110\n\t"
            "v_rcp_f32 v111, v111\n\t"
            "v_pk_mul_f32 v[104:105], v[104:105], v[108:109]\n\t"
            "v_pk_mul_f32 v[106:107], v[106:107], v[110:111]\n\t"
            "v_dot2c_f32_bf16 v104, %[shi], v116\n\t"
            "v_dot2c_f32_bf16 v105, %[shi], v117\n\t"
            "v_dot2c_f32_bf16 v106, %[shi], v118\n\t"
            "v_dot2c_f32_bf16 v107, %[shi], v119\n\t"
            "v_maximum3_f32 v104, v104, 0, 0\n\t"
            "v_maximum3_f32 v105, v105, 0, 0\n\t"
            "v_maximum3_f32 v106, v106, 0, 0\n\t"
            "v_maximum3_f32 v107, v107, 0, 0\n\t"
            "v_dot2c_f32_bf16 v104, %[slo], v120\n\t"
            "v_dot2c_f32_bf16 v105, %[shi], v120\n\t"
            "v_dot2c_f32_bf16 v106, %[slo], v121\n\t"
            "v_dot2c_f32_bf16 v107, %[shi], v121\n\t"
            "s_nop 1\n\t"
            "v_cvt_pk_bf16_f32 v112, v104, v105\n\t"
            "v_cvt_pk_bf16_f32 v113, v106, v107\n\t"
            "buffer_store_dwordx2 v[112:113], v124, %[o], 0 offen\n\t"
            PRIO_STEP
            "s_cmp_lt_u32 %[nxt], %[ntask]\n\t"
            "s_cbranch_scc1 L_task%=\n\t"
            "s_waitcnt vmcnt(0) lgkmcnt(0)"
            : [cnt] "=&s"(s_cnt), [nxt] "=&s"(s_nxt), [t] "=&s"(s_t), [tc] "=&s"(s_tc)
            : "{v81}"(joff), "{v83}"(joff), "{v85}"(joff), "{v87}"(joff), "{v91}"(g8), "{v93}"(ctr), "{v95}"(one), "{v126}"(j8),
              "{v[100:101]}"(w01), "{v[102:103]}"(w23),
              [task] "s"(w), [ntask] "s"(ntask), [bv] "s"(srdBV), [o] "s"(srdO), [slo] "s"(0x00003F80u), [shi] "s"(0x3F800000u), [eps] "s"(eps2),
              [au] "i"(K2_AU), [le0] "i"(K2_LSZ), [le1] "i"(K2_LSZ + 4), [le2] "i"(K2_LSZ + 8), [le3] "i"(K2_LSZ + 12),
              [le4] "i"(K2_LSZ + 16), [le5] "i"(K2_LSZ + 20), [le6] "i"(K2_LSZ + 24), [le7] "i"(K2_LSZ + 28)
            : "v64", "v65", "v66", "v67", "v68", "v69", "v70", "v71", "v72", "v73", "v74", "v75", "v76", "v77", "v78", "v79",
              "v80", "v82", "v84", "v86", "v88", "v89", "v90", "v92", "v94", "v96", "v97", "v98", "v99",
              "v104", "v105", "v106", "v107", "v108", "v109", "v110", "v111", "v112", "v113", "v114", "v115",
              "v116", "v117", "v118", "v119", "v120", "v121", "v122", "v123", "v124", "v125", "v127", "scc", "memory");
#undef STAGE
    }
    STAMP(10);
    STAMPC(15);
    SPAN1(1);
}

__global__ void k_meas(const char* ws) {
    const unsigned long long t_start = __builtin_amdgcn_s_memrealtime();
    const int lane = threadIdx.x;
    const unsigned long long* sp = (const unsigned long long*)(ws + (192u << 20));
    unsigned long long mn1 = ~0ull, mx1 = 0, mn2 = ~0ull, mx2 = 0;
    for (int i = lane; i < 256 * 16; i += 64) { const int b2 = i >> 4, w2 = i & 15;
        if (w2 < 8) { const unsigned long long a = sp[(b2 * 16 + w2) * 2], e = sp[(b2 * 16 + w2) * 2 + 1]; mn1 = a < mn1 ? a : mn1; mx1 = e > mx1 ? e : mx1; }
        const unsigned long long a = sp[((256 + b2) * 16 + w2) * 2], e = sp[((256 + b2) * 16 + w2) * 2 + 1]; mn2 = a < mn2 ? a : mn2; mx2 = e > mx2 ? e : mx2; }
    for (int o = 32; o; o >>= 1) { unsigned long long t;
        t = __shfl_xor(mn1, o); mn1 = t < mn1 ? t : mn1; t = __shfl_xor(mx1, o); mx1 = t > mx1 ? t : mx1;
        t = __shfl_xor(mn2, o); mn2 = t < mn2 ? t : mn2; t = __shfl_xor(mx2, o); mx2 = t > mx2 ? t : mx2; }
    unsigned long long val = 0;
    if (MEASURE == 1) val = mx1 - mn1;
    if (MEASURE == 2) val = mn2 - mx1;
    if (MEASURE == 3) val = mx2 - mn2;
    if (MEASURE == 4) val = t_start - mx2;
    const unsigned long long t0 = __builtin_amdgcn_s_memrealtime();
    while (__builtin_amdgcn_s_memrealtime() - t0 < val * 10ull) __builtin_amdgcn_s_sleep(2);
}
extern "C" void kernel_launch(void* const* d_in, const int* in_sizes, int n_in,
                              void* d_out, int out_size, void* d_ws, size_t ws_size,
                              hipStream_t stream) {
    const u16* x = (const u16*)d_in[0];
    const u16* ea = (const u16*)d_in[1];
    const int* src = (const int*)d_in[2];
    const int* dst = (const int*)d_in[3];
    const u16* wa = (const u16*)d_in[4];
    const u16* wb = (const u16*)d_in[5];
    const u16* wc = (const u16*)d_in[6];
    const u16* wu = (const u16*)d_in[7];
    const u16* wv = (const u16*)d_in[8];
    (void)hipFuncSetAttribute((const void*)k1_gemm_bin, hipFuncAttributeMaxDynamicSharedMemorySize, K1_LDS);
    k1_gemm_bin<<<256, 512, K1_LDS, stream>>>(x, wa, wb, wu, wv, src, dst, ea, (char*)d_ws);
    (void)hipFuncSetAttribute((const void*)k2_edges, hipFuncAttributeMaxDynamicSharedMemorySize, K2_LDS);
    k2_edges<<<256, 1024, K2_LDS, stream>>>(x, wc, src, dst, ea, (const char*)d_ws, (u16*)d_out);
    k_meas<<<1, 64, 0, stream>>>((const char*)d_ws);
}
```

```cpp
#define MEASURE 0
#define SPAN 1
#include <hip/hip_runtime.h>
#include <hip/hip_bf16.h>
#include <stdint.h>

typedef unsigned short u16;
typedef unsigned int u32;
typedef u32 u32x2 __attribute__((ext_vector_type(2)));
typedef u32 u32x4 __attribute__((ext_vector_type(4)));
typedef float f32x2 __attribute__((ext_vector_type(2)));
typedef float f32x4 __attribute__((ext_vector_type(4)));
typedef float f32x16 __attribute__((ext_vector_type(16)));
typedef short s16x4 __attribute__((ext_vector_type(4)));
typedef short s16x8 __attribute__((ext_vector_type(8)));
typedef __bf16 bf16x8 __attribute__((ext_vector_type(8)));
typedef __bf16 bf16x4 __attribute__((ext_vector_type(4)));
typedef __bf16 bf16x2 __attribute__((ext_vector_type(2)));

constexpr int NN = 10000;
constexpr int NE = 160000;
constexpr int DD = 256;
constexpr int NP = 10048;
constexpr int RG = 320;
constexpr int NBK = 64;
constexpr int NB = 157;
constexpr int NCH = 64;
constexpr int CHE = 2500;
constexpr int CAP = 80;
constexpr int CAPN = 44;
constexpr int LST = 45;
constexpr float NLOG2E = -1.4426950408889634f;
constexpr u32 NEUTRAL_REC = (u32)NN;
constexpr u32 NEUTRAL_BV = 0x00007F00u;

constexpr size_t OFF_BV = 0;
constexpr size_t OFF_AU = OFF_BV + (size_t)4 * NP * 256;
constexpr size_t OFF_R4 = OFF_AU + (size_t)4 * NP * 256;
constexpr size_t OFF_R2 = OFF_R4 + (size_t)NBK * NCH * CAP * 4;
constexpr size_t OFF_CNT = OFF_R2 + (size_t)NBK * NCH * CAP * 2;

#define LDS_AS __attribute__((address_space(3)))
#define GLB_AS __attribute__((address_space(1)))

__device__ __forceinline__ void glds16(const void* g, void* l) {
    __builtin_amdgcn_global_load_lds((const GLB_AS u32*)g, (LDS_AS u32*)l, 16, 0, 0);
}
__device__ __forceinline__ void glds16a(const void* sbase, u32 voff, u32 lds) {
    u32 keep;
    asm volatile("s_mov_b32 %0, m0\n\ts_mov_b32 m0, %3\n\ts_nop 0\n\tglobal_load_lds_dwordx4 %1, %2\n\ts_mov_b32 m0, %0" : "=&s"(keep) : "v"(voff), "s"(sbase), "s"(lds) : "memory");
}
__device__ __forceinline__ u32 cvt_pk_bf16(float lo, float hi) {
    const f32x2 v = {lo, hi}; return __builtin_bit_cast(u32, __builtin_convertvector(v, bf16x2));
}
#define WAIT_V(n) asm volatile("s_waitcnt vmcnt(" #n ")" ::: "memory")
#define WAIT_L(n) asm volatile("s_waitcnt lgkmcnt(" #n ")" ::: "memory")
#define BAR() __builtin_amdgcn_s_barrier()
#define EXIT2_AT 99
#ifndef EXIT_AT
#define EXIT_AT 99
#endif
#define STORE_AUX 16
#define SPAN0() unsigned long long span_t0_ = __builtin_amdgcn_s_memrealtime()
#define SPAN1(K) do { asm volatile("s_waitcnt vmcnt(0) lgkmcnt(0)" ::: "memory"); unsigned long long t1_ = __builtin_amdgcn_s_memrealtime(); if ((threadIdx.x & 63) == 0) { unsigned long long* p_ = (unsigned long long*)((char*)ws + (192u << 20)) + (((K) * 256 + blockIdx.x) * 16 + (threadIdx.x >> 6)) * 2; p_[0] = span_t0_; p_[1] = t1_; } } while (0)
#define STAMP(i)
#define STAMPC(i)
#define TICK(v)

constexpr int K1_R0 = 0, K1_R1 = 65536, K1_SCR = 131072;
constexpr int K1_ST4 = K1_SCR, K1_ST2 = K1_ST4 + NBK * CAP * 4, K1_BC = K1_ST2 + NBK * CAP * 2;
constexpr int K1_LDS = K1_SCR + 32768;
static_assert(K1_BC + NBK * 4 <= K1_LDS, "lds");

__global__ __launch_bounds__(512, 2) void k1_gemm_bin(
    const u16* __restrict__ x, const u16* __restrict__ wa, const u16* __restrict__ wb, const u16* __restrict__ wu, const u16* __restrict__ wv,
    const int* __restrict__ src, const int* __restrict__ dst, const u16* __restrict__ ea, char* __restrict__ ws) {
    extern __shared__ __attribute__((aligned(16))) char smem[];
    const int tid = threadIdx.x, lane = tid & 63;
    const int w = __builtin_amdgcn_readfirstlane(tid >> 6);
    const int b = blockIdx.x;
    const int type = (b >> 5) & 1;
    const int ct2 = (b >> 3) & 3;
    const int rg = (b & 7) * 4 + (b >> 6);
    const int row0 = rg * RG;
    const int chunk = (b & 31) | ((b >> 6) << 5) ;
    const bool binner = type == 1 && b < 128;
    constexpr int KOFF = 0;
    SPAN0();
    if (blockIdx.x == 0 && threadIdx.x == 0) *(unsigned*)(ws + (211u << 20)) = 0;
#if EXIT_AT == 0
    if (ws) { SPAN1(0); return; }
#endif
    STAMP(0);
    int bd[5], bs[5]; u32 be[5];
    if (binner) {
#pragma unroll
        for (int i = 0; i < 5; ++i) {
            const int k = tid + 512 * i;
            const int e = chunk * CHE + (k < CHE ? k : 0);
            bd[i] = dst[e]; bs[i] = src[e]; be[i] = ea[e];
        }
        if (tid < NBK) *(u32*)(smem + K1_BC + tid * 4) = 0;
    }
    {
        const int m = w >> 2;
        const u16* W = type == 0 ? (m == 0 ? wb : wv) : (m == 0 ? wa : wu);
        const int kb0 = (w & 3) * 64, kr = lane >> 3;
        const u16* g = W + (size_t)(kb0 + kr) * DD + ct2 * 64 + (((lane & 7) ^ ((kr & 2) << 1)) << 3);
#pragma unroll
        for (int i = 0; i < 8; ++i) glds16(g + (size_t)i * 8 * DD, smem + K1_R0 + m * 32768 + (kb0 / 8 + i) * 1024);
    }
    const int xr = lane >> 5, xp = lane & 31;
    auto stage_x = [&](int tile, int q, int ldsbase) {
        const int r = q * 2 + xr;
        int grow = row0 + tile * 32 + r; grow = grow < NN ? grow : NN - 1;
        glds16((const char*)x + (u32)(grow * 512 + ((xp ^ (r & 15)) << 4)), smem + ldsbase + q * 1024);
    };
#pragma unroll
    for (int i = 0; i < 8; ++i) { const int q = w * 8 + i; stage_x(q >> 4, q & 15, K1_R1 + (q >> 4) * 16384); }
    STAMP(1);
    WAIT_V(8); WAIT_L(0);
    BAR();
    STAMP(2);
#if EXIT_AT == 1
    if (ws) { SPAN1(0); return; }
#endif
    const int ch = w >> 2;
    bf16x8 wf0[16], wf1[16];
    {
        const int h = lane >> 5, blk = (lane >> 4) & 1, q = (lane & 15) >> 2, p = lane & 3;
        const u32 base = K1_R0 + (8 * h + q) * 128 + (((ch * 4 + blk * 2 + (p >> 1)) ^ ((q & 2) << 1)) << 4) + (p & 1) * 8;
#define TRRD(S) { u32x2 a0, a1, b0, b1; \
            asm volatile("ds_read_b64_tr_b16 %0, %4 offset:%c5\n\tds_read_b64_tr_b16 %1, %4 offset:%c6\n\t" \
                         "ds_read_b64_tr_b16 %2, %4 offset:%c7\n\tds_read_b64_tr_b16 %3, %4 offset:%c8" \
                         : "=&v"(a0), "=&v"(a1), "=&v"(b0), "=&v"(b1) \
                         : "v"(base), "i"((S) * 2048), "i"((S) * 2048 + 512), "i"(32768 + (S) * 2048), "i"(32768 + (S) * 2048 + 512) : "memory"); \
            wf0[S] = __builtin_bit_cast(bf16x8, __builtin_shufflevector(a0, a1, 0, 1, 2, 3)); \
            wf1[S] = __builtin_bit_cast(bf16x8, __builtin_shufflevector(b0, b1, 0, 1, 2, 3)); }
        TRRD(0) TRRD(1) TRRD(2) TRRD(3) TRRD(4) TRRD(5) TRRD(6) TRRD(7) TRRD(8) TRRD(9) TRRD(10) TRRD(11) TRRD(12) TRRD(13) TRRD(14) TRRD(15)
#undef TRRD
    }
    if (binner) {
#pragma unroll
        for (int i = 0; i < 5; ++i) {
            const int k = tid + 512 * i;
            if (k < CHE) {
                const int bk = (int)(((u32)bd[i] * 53432u) >> 23);
                const int dl = bd[i] - bk * NB;
                const u32 pos = atomicAdd((u32*)(smem + K1_BC + bk * 4), 1u);
                if (pos < CAP) {
                    *(u32*)(smem + K1_ST4 + (bk * CAP + pos) * 4) = (u32)bs[i] | (be[i] << 16);
                    *(u16*)(smem + K1_ST2 + (bk * CAP + pos) * 2) = (u16)dl;
                }
            }
        }
    }
    STAMP(3);
    WAIT_V(0); WAIT_L(0);
    __builtin_amdgcn_sched_barrier(0);
    BAR();
    STAMP(4);
#if EXIT_AT == 2
    if (ws) { asm volatile("" :: "v"(wf0[0]), "v"(wf1[15])); SPAN1(0); return; }
#endif
#pragma unroll
    for (int i = 0; i < 8; ++i) { const int q = w * 8 + i; stage_x(4 + (q >> 4), q & 15, K1_R0 + (q >> 4) * 16384); }
    if (binner) {
        u32* R4 = (u32*)(ws + OFF_R4); u16* R2 = (u16*)(ws + OFF_R2); u32* CNT = (u32*)(ws + OFF_CNT);
        u32x4 t4[3]; u32x2 t2[3];
#pragma unroll
        for (int i = 0; i < 3; ++i) { const int q = tid + 512 * i; const int qq = q < NBK * (CAP / 4) ? q : 0;
            t4[i] = *(const u32x4*)(smem + K1_ST4 + qq * 16); t2[i] = *(const u32x2*)(smem + K1_ST2 + qq * 8); }
        const u32 cv = *(const u32*)(smem + K1_BC + (tid & 63) * 4);
#pragma unroll
        for (int i = 0; i < 3; ++i) { const int q = tid + 512 * i;
            if (q < NBK * (CAP / 4)) { const int bk = q / (CAP / 4), o = q - bk * (CAP / 4);
                *(u32x4*)(R4 + ((size_t)bk * NCH + chunk) * CAP + o * 4) = t4[i];
                *(u32x2*)(R2 + ((size_t)bk * NCH + chunk) * CAP + o * 4) = t2[i]; } }
        if (tid < NBK) CNT[tid * NCH + chunk] = cv;
        WAIT_L(0);
        BAR();
    }
    if (rg == 0 && type == 0 && w == 0) ((u32*)(ws + OFF_BV))[((size_t)ct2 * NP + NN) * 64 + lane] = NEUTRAL_BV;

    const int r = lane & 31, h = lane >> 5;
    const int abase = r * 512 + ((((r & 14) | ((h ^ r) & 1))) << 4);
    char* const scr = smem + K1_SCR + w * 4096;
    const __amdgpu_buffer_rsrc_t trs = __builtin_amdgcn_make_buffer_rsrc(ws + (type ? OFF_AU : OFF_BV) + (size_t)ct2 * NP * 256, 0, NN * 256, 0x00020000);
    const u32 svo = (lane >> 3) * 256 + ch * 128 + (lane & 7) * 16;

    const int s4 = w & 3;
    if (ch == 0) __builtin_amdgcn_s_setprio(1);
    STAMP(5);
#pragma unroll 1
    for (int st = 0; st < (EXIT_AT == 3 ? 1 : (EXIT_AT == 4 ? 2 : 3)); ++st) {
        int tile, ldsbase;
        if (st == 0) { tile = s4; ldsbase = K1_R1 + s4 * 16384; }
        else if (st == 1) { tile = 4 + s4; ldsbase = K1_R0 + s4 * 16384; }
        else { const int t = (w < 2) ? w : (w >= 6 ? w - 6 : 99); tile = 8 + t; ldsbase = K1_R1 + (t & 1) * 16384; }
        const int trow = row0 + tile * 32;
        const bool active = tile < 10 && trow < NN;
        unsigned long long tkA = 0, tkB = 0, tkC = 0, tkD = 0;
        if (active) {
            TICK(tkA);
            bf16x8 a[16];
#pragma unroll
            for (int s = 0; s < 8; ++s) a[s] = *(const bf16x8*)(smem + ldsbase + (abase ^ (s << 5)));
            __builtin_amdgcn_sched_barrier(0);
            f32x16 acc0, acc1;
#pragma unroll
            for (int i = 0; i < 16; ++i) { acc0[i] = 0.f; acc1[i] = 0.f; }
#pragma unroll
            for (int s = 0; s < 8; ++s) {
                a[s + 8] = *(const bf16x8*)(smem + ldsbase + (abase ^ (s << 5)) + 256);
                acc0 = __builtin_amdgcn_mfma_f32_32x32x16_bf16(a[s], wf0[s], acc0, 0, 0, 0);
                acc1 = __builtin_amdgcn_mfma_f32_32x32x16_bf16(a[s], wf1[s], acc1, 0, 0, 0);
                __builtin_amdgcn_sched_group_barrier(0x100, 1, 0);
                __builtin_amdgcn_sched_group_barrier(0x8, 2, 0);
            }
            __builtin_amdgcn_sched_barrier(0);
#pragma unroll
            for (int s = 8; s < 16; ++s) {
                acc0 = __builtin_amdgcn_mfma_f32_32x32x16_bf16(a[s], wf0[s], acc0, 0, 0, 0);
                acc1 = __builtin_amdgcn_mfma_f32_32x32x16_bf16(a[s], wf1[s], acc1, 0, 0, 0);
            }
            TICK(tkB);
            char* const sw = scr + h * 512 + r * 4;
            if (type == 0) {
#pragma unroll
                for (int i = 0; i < 16; i += 2) {
                    const u32 bb = cvt_pk_bf16(acc0[i] * NLOG2E, acc0[i + 1] * NLOG2E);
#pragma unroll
                    for (int j = 0; j < 2; ++j) {
                        const u32 vb = __float_as_uint(acc1[i + j]);
                        const u32 bl = j ? (bb >> 16) : (bb & 0xFFFFu);
                        const u32 u = __builtin_elementwise_sub_sat((vb & 0x7FFFFFFFu) + 0x8000u, bl);
                        *(u32*)(sw + (((i + j) & 3) + 8 * ((i + j) >> 2)) * 128) = (vb & 0x80000000u) | (u & 0x7FFF0000u) | bl;
                    }
                }
            } else {
#pragma unroll
                for (int i = 0; i < 16; ++i) *(u32*)(sw + ((i & 3) + 8 * (i >> 2)) * 128) = cvt_pk_bf16(acc0[i] * NLOG2E, acc1[i]);
            }
            TICK(tkC);
#pragma unroll
            for (int i = 0; i < 4; ++i) {
                const u32x4 v = *(const u32x4*)(scr + i * 1024 + lane * 16);
                __builtin_amdgcn_raw_buffer_store_b128(v, trs, svo + (u32)(trow + i * 8) * 256, 0, STORE_AUX);
            }
        }
        TICK(tkD);
        if (st == 0) STAMP(6); else if (st == 1) STAMP(8); else STAMP(10);
        if (st < 2) {
            if (active) WAIT_V(4); else WAIT_V(0);
            BAR();
            if (st == 0) {
#pragma unroll
                for (int i = 0; i < 4; ++i) { const int q = w * 4 + i; stage_x(8 + (q >> 4), q & 15, K1_R1 + (q >> 4) * 16384); }
            }
        }
    }
    SPAN1(0);
}

constexpr int LSTB = LST * 4;
constexpr int K2_X = 0;
constexpr int K2_AU = K2_X + 20480;
constexpr int K2_LIDX = K2_AU + 40960;
constexpr int K2_LSZ = (NB * LSTB + 32 + 256 + 15) / 16 * 16;
constexpr int K2_LE = K2_LIDX + K2_LSZ;
constexpr int K2_CNT = K2_LE + K2_LSZ;
constexpr int K2_HIST = K2_CNT + 160 * 4;
constexpr int K2_ORD = K2_HIST + 128 * 4;
constexpr int K2_MISC = K2_ORD + 80 * 32;
constexpr int K2_LDS = K2_MISC + 16;
constexpr int K2_TRASH = NB * LSTB + 32;
static_assert(K2_TRASH + 256 <= K2_LSZ, "trash");
static_assert(K2_LSZ < 65536 - 64, "ds offset");

__device__ __forceinline__ float dot2sel(u32 d, u32 sel, float c) {
    return __builtin_amdgcn_fdot2_f32_bf16(__builtin_bit_cast(bf16x2, d), __builtin_bit_cast(bf16x2, sel), c, false);
}

struct Acc { f32x2 n01, n23, d01, d23; };

__device__ __forceinline__ void edge_step(Acc& a, const u32x4 d, const u32 rec, const f32x2 a01, const f32x2 a23, const f32x2 w01, const f32x2 w23, const u32 sel) {
    const float e = __uint_as_float(rec & 0xFFFF0000u);
    const f32x2 ee = {e, e};
    const f32x2 t01 = __builtin_elementwise_fma(ee, w01, a01);
    const f32x2 t23 = __builtin_elementwise_fma(ee, w23, a23);
    f32x2 E01, E23;
    E01.x = __builtin_amdgcn_exp2f(dot2sel(d.x, sel, t01.x));
    E01.y = __builtin_amdgcn_exp2f(dot2sel(d.y, sel, t01.y));
    E23.x = __builtin_amdgcn_exp2f(dot2sel(d.z, sel, t23.x));
    E23.y = __builtin_amdgcn_exp2f(dot2sel(d.w, sel, t23.y));
    const f32x2 one = {1.f, 1.f};
    E01 += one; E23 += one;
    f32x2 s01, s23;
    s01.x = __builtin_amdgcn_rcpf(E01.x); s01.y = __builtin_amdgcn_rcpf(E01.y);
    s23.x = __builtin_amdgcn_rcpf(E23.x); s23.y = __builtin_amdgcn_rcpf(E23.y);
    a.d01 += s01; a.d23 += s23;
    const f32x2 v01 = {__uint_as_float(d.x), __uint_as_float(d.y)};
    const f32x2 v23 = {__uint_as_float(d.z), __uint_as_float(d.w)};
    a.n01 = __builtin_elementwise_fma(s01, v01, a.n01);
    a.n23 = __builtin_elementwise_fma(s23, v23, a.n23);
}

__device__ __forceinline__ void node_finish(const Acc& a, const u32x4 au, const u32x2 xv, u16* outp) {
    const float num[4] = {a.n01.x, a.n01.y, a.n23.x, a.n23.y};
    const float den[4] = {a.d01.x, a.d01.y, a.d23.x, a.d23.y};
    const u32 aus[4] = {au.x, au.y, au.z, au.w};
    const float xs[4] = {__uint_as_float(xv.x << 16), __uint_as_float(xv.x & 0xFFFF0000u), __uint_as_float(xv.y << 16), __uint_as_float(xv.y & 0xFFFF0000u)};
    float o[4];
#pragma unroll
    for (int c = 0; c < 4; ++c) {
        const float z = __uint_as_float(aus[c] & 0xFFFF0000u) + num[c] * __builtin_amdgcn_rcpf(den[c] + 1e-6f);
        const float rl = z > 0.f ? z : 0.f * z;
        o[c] = xs[c] + rl;
    }
    u32x2 ov; ov.x = cvt_pk_bf16(o[0], o[1]); ov.y = cvt_pk_bf16(o[2], o[3]);
    *(u32x2*)outp = ov;
}

template <int CTRL, int RM> __device__ __forceinline__ u32 dpp_add(u32 v) {
    return v + (u32)__builtin_amdgcn_update_dpp(0, (int)v, CTRL, RM, 0xf, false);
}

__global__ __launch_bounds__(1024, 4) void k2_edges(
    const u16* __restrict__ x, const u16* __restrict__ wc, const int* __restrict__ src, const int* __restrict__ dst, const u16* __restrict__ ea,
    const char* __restrict__ ws, u16* __restrict__ out) {
    extern __shared__ __attribute__((aligned(16))) char smem[];
    const int tid = threadIdx.x, lane = tid & 63;
    const int w = __builtin_amdgcn_readfirstlane(tid >> 6);
    const int b = blockIdx.x;
    const int slice = (b & 7) >> 1;
    const int bucket = (b >> 3) * 2 + (b & 1);
    const int node0 = bucket * NB;
    const int nvalid = (NN - node0) < NB ? (NN - node0) : NB;
    u32* const cnt = (u32*)(smem + K2_CNT);
    u32* const hist = (u32*)(smem + K2_HIST);
    u32* const misc = (u32*)(smem + K2_MISC);
    constexpr int KOFF = 256 * 16;
    SPAN0();
    STAMP(0);

    const u32* R4 = (const u32*)(ws + OFF_R4) + (size_t)bucket * NCH * CAP;
    const u16* R2 = (const u16*)(ws + OFF_R2) + (size_t)bucket * NCH * CAP;
    const u32* CNT = (const u32*)(ws + OFF_CNT) + bucket * NCH;
    const int ch0 = tid / (CAP / 4), ch1 = (tid + 1024) / (CAP / 4);
    const u32x4 r40 = *(const u32x4*)(R4 + tid * 4);
    const u32x2 r20 = *(const u32x2*)(R2 + tid * 4);
    const u32 c0 = CNT[ch0];
    u32x4 r41 = {0, 0, 0, 0}; u32x2 r21 = {0, 0}; u32 c1 = 0;
    if (w < 4) { r41 = *(const u32x4*)(R4 + (tid + 1024) * 4); r21 = *(const u32x2*)(R2 + (tid + 1024) * 4); c1 = CNT[ch1]; }
    const char* const bvb = ws + OFF_BV + (size_t)slice * NP * 256;
    const char* const aub = ws + OFF_AU + (size_t)slice * NP * 256;
    const int g = lane >> 4, j = lane & 15;
    const u32x2 wcv = *(const u32x2*)(wc + slice * 64 + j * 4);
    for (int i = tid; i < K2_LSZ / 16; i += 1024) {
        *(u32x4*)(smem + K2_LIDX + i * 16) = (u32x4){NEUTRAL_REC, NEUTRAL_REC, NEUTRAL_REC, NEUTRAL_REC};
        *(u32x4*)(smem + K2_LE + i * 16) = (u32x4){0, 0, 0, 0};
    }
    if (tid < 160 + 64) cnt[tid] = 0;
    if (tid == 0) { misc[0] = 16; misc[1] = 0; }
    f32x2 w01 = {__uint_as_float(wcv.x << 16) * NLOG2E, __uint_as_float(wcv.x & 0xFFFF0000u) * NLOG2E};
    f32x2 w23 = {__uint_as_float(wcv.y << 16) * NLOG2E, __uint_as_float(wcv.y & 0xFFFF0000u) * NLOG2E};
    asm volatile("" : "+v"(w01), "+v"(w23));
    STAMP(1);
    WAIT_V(0); WAIT_L(0); BAR();
    {
        const char* ga = aub + (size_t)node0 * 256;
#pragma unroll
        for (int i = 0; i < 3; ++i) { const int q = w + 16 * i; if (q < 40) glds16a(ga, q * 1024 + lane * 16, K2_AU + q * 1024); }
        const char* gx = (const char*)x + slice * 128;
#pragma unroll
        for (int i = 0; i < 2; ++i) {
            const int q = w + 16 * i;
            if (q < 20) { int row = node0 + q * 8 + (lane >> 3); row = row < NN ? row : NN - 1;
                glds16a(gx, row * 512 + (lane & 7) * 16, K2_X + q * 1024); }
        }
    }
    STAMP(2);
    {
        const int p0 = (tid - ch0 * (CAP / 4)) * 4;
        const u32 rec0[4] = {r40.x, r40.y, r40.z, r40.w};
        const u32 dl0[4] = {r20.x & 0xFFFF, r20.x >> 16, r20.y & 0xFFFF, r20.y >> 16};
        u32 slot[4];
        bool ovf = c0 > CAP;
#pragma unroll
        for (int k = 0; k < 4; ++k) slot[k] = atomicAdd(&cnt[p0 + k < c0 ? dl0[k] : 224 + lane], 1u);
        if (w < 4) {
            const int p1 = (tid + 1024 - ch1 * (CAP / 4)) * 4;
            const u32 rec1[4] = {r41.x, r41.y, r41.z, r41.w};
            const u32 dl1[4] = {r21.x & 0xFFFF, r21.x >> 16, r21.y & 0xFFFF, r21.y >> 16};
            u32 slot1[4];
            ovf |= c1 > CAP;
#pragma unroll
            for (int k = 0; k < 4; ++k) slot1[k] = atomicAdd(&cnt[p1 + k < c1 ? dl1[k] : 224 + lane], 1u);
#pragma unroll
            for (int k = 0; k < 4; ++k) {
                const bool v = p1 + k < c1, ok = v && slot1[k] < CAPN;
                ovf |= v && !ok;
                const u32 o = ok ? dl1[k] * LSTB + slot1[k] * 4 : K2_TRASH + lane * 4;
                *(u32*)(smem + K2_LIDX + o) = rec1[k] & 0xFFFFu;
                *(u32*)(smem + K2_LE + o) = rec1[k] & 0xFFFF0000u;
            }
        }
#pragma unroll
        for (int k = 0; k < 4; ++k) {
            const bool v = p0 + k < c0, ok = v && slot[k] < CAPN;
            ovf |= v && !ok;
            const u32 o = ok ? dl0[k] * LSTB + slot[k] * 4 : K2_TRASH + lane * 4;
            *(u32*)(smem + K2_LIDX + o) = rec0[k] & 0xFFFFu;
            *(u32*)(smem + K2_LE + o) = rec0[k] & 0xFFFF0000u;
        }
        if (ovf) misc[1] = 1;
    }
    STAMP(3);
    WAIT_L(0); BAR();
    STAMP(4);
    const bool slow = misc[1] != 0;

    const u32 joff = j * 16;

    if (slow) {
        u32 sel = 0x00003F80u;
        asm volatile("" : "+s"(sel));
        for (int task = w; task * 4 < nvalid; task += 16) {
            const int n = task * 4 + g;
            const bool nv = n < nvalid;
            const int node = node0 + (nv ? n : 0);
            const u32x4 au = *(const u32x4*)(aub + (size_t)node * 256 + joff);
            const u32x2 xv = *(const u32x2*)(x + (size_t)node * DD + slice * 64 + j * 4);
            const f32x2 a01 = {__uint_as_float(au.x << 16), __uint_as_float(au.y << 16)};
            const f32x2 a23 = {__uint_as_float(au.z << 16), __uint_as_float(au.w << 16)};
            Acc acc = {{0.f, 0.f}, {0.f, 0.f}, {0.f, 0.f}, {0.f, 0.f}};
            const int lo = node0 + task * 4;
            for (int e0 = 0; e0 < NE; e0 += 64) {
                const int d = dst[e0 + lane];
                unsigned long long m = __ballot((u32)(d - lo) < 4u);
                if (m) {
                    const u32 rc = (u32)src[e0 + lane] | ((u32)ea[e0 + lane] << 16);
                    while (m) {
                        const int l = __builtin_ctzll(m); m &= m - 1;
                        const int dd = __builtin_amdgcn_readlane(d, l);
                        const u32 rr = __builtin_amdgcn_readlane(rc, l);
                        const u32 rec = (nv && dd == node) ? rr : NEUTRAL_REC;
                        const u32x4 dv = *(const u32x4*)(bvb + (size_t)((rec & 0xFFFFu) * 256 + joff));
                        edge_step(acc, dv, rec, a01, a23, w01, w23, sel);
                    }
                }
            }
            if (nv) node_finish(acc, au, xv, out + (size_t)node * DD + slice * 64 + j * 4);
        }
        return;
    }

    u32 mydeg = 0, myrank = 0;
    if (tid < nvalid) { mydeg = cnt[tid]; myrank = atomicAdd(&hist[63 - mydeg], 1u); }
    WAIT_L(0); BAR();
    if (w < 3) {
        const u32 h0 = hist[lane];
        u32 v = h0;
        v = dpp_add<0x111, 0xf>(v); v = dpp_add<0x112, 0xf>(v); v = dpp_add<0x114, 0xf>(v); v = dpp_add<0x118, 0xf>(v);
        v = dpp_add<0x142, 0xa>(v); v = dpp_add<0x143, 0xc>(v);
        const u32 base = (u32)__builtin_amdgcn_ds_bpermute((int)((63 - mydeg) * 4), (int)(v - h0));
        if (tid < nvalid) {
            const u32 pos = base + myrank;
            const u32x2 ent = {(u32)(K2_LIDX + tid * LSTB), (u32)(tid * 256) + mydeg};
            u32x2* o = (u32x2*)(smem + K2_ORD) + pos;
            o[0] = ent;
            if (pos == (u32)nvalid - 1) { o[1] = ent; o[2] = ent; o[3] = ent; }
        }
    }
    WAIT_V(0); WAIT_L(0); BAR();
    STAMP(5);
    STAMPC(14);

    const int ntask = (nvalid + 3) >> 2;
    if (w < ntask) {
        u32x4 srdBV, srdO;
        { const size_t p = (size_t)bvb; srdBV = (u32x4){(u32)p, (u32)(p >> 32) | (256u << 16), 0xFFFFFFFFu, 0x00020000u}; }
        { const size_t ob = (size_t)node0 * 512 + slice * 128; const size_t p = (size_t)out + ob; srdO = (u32x4){(u32)p, (u32)(p >> 32), (u32)((size_t)NN * 512 - ob), 0x00020000u}; }
        u32 g8 = K2_ORD + g * 8, ctr = K2_MISC, one = 1, j8 = j * 8;
        unsigned long long eps2 = 0x358637BD358637BDull;
        u32 s_cnt, s_nxt, s_t, s_tc;
#define STAGE(DS0, DS1, DS2, DS3, DSL, DSH, IX, EP, E1, DN, IXN, O1, O2) \
            "s_waitcnt lgkmcnt(0)\n\t" \
            "buffer_load_dwordx4 " DN ", " GIX(IXN) ", %[bv], 0 idxen offen\n\t" \
            "v_pk_fma_f32 v[112:113], " EP ", v[100:101], v[96:97] op_sel_hi:[0,1,1]\n\t" \
            "v_pk_fma_f32 v[114:115], " EP ", v[102:103], v[98:99] op_sel_hi:[0,1,1]\n\t" \
            "ds_read_b32 " IX ", v89 offset:" O1 "\n\t" \
            "ds_read_b32 " E1 ", v89 offset:" O2 "\n\t" \
            "s_waitcnt vmcnt(3)\n\t" \
            "v_dot2c_f32_bf16 v112, %[slo], " DS0 "\n\t" \
            "v_dot2c_f32_bf16 v113, %[slo], " DS1 "\n\t" \
            "v_dot2c_f32_bf16 v114, %[slo], " DS2 "\n\t" \
            "v_dot2c_f32_bf16 v115, %[slo], " DS3 "\n\t" \
            "v_exp_f32 v112, v112\n\t" \
            "v_exp_f32 v113, v113\n\t" \
            "v_exp_f32 v114, v114\n\t" \
            "v_exp_f32 v115, v115\n\t" \
            "v_pk_add_f32 v[112:113], v[112:113], 1.0 op_sel_hi:[1,0]\n\t" \
            "v_pk_add_f32 v[114:115], v[114:115], 1.0 op_sel_hi:[1,0]\n\t" \
            "v_rcp_f32 v112, v112\n\t" \
            "v_rcp_f32 v113, v113\n\t" \
            "v_rcp_f32 v114, v114\n\t" \
            "v_rcp_f32 v115, v115\n\t" \
            "s_sub_u32 %[cnt], %[cnt], 1\n\t" \
            "v_pk_add_f32 v[108:109], v[108:109], v[112:113]\n\t" \
            "v_pk_fma_f32 v[104:105], v[112:113], " DSL ", v[104:105]\n\t" \
            "v_pk_add_f32 v[110:111], v[110:111], v[114:115]\n\t" \
            "v_pk_fma_f32 v[106:107], v[114:115], " DSH ", v[106:107]\n\t" \
            "s_cmp_eq_u32 %[cnt], 0\n\t"
#define GIX(x) x
#define PRIO_ON 1
#define PRIO_INIT "s_setprio 3\n\t"
#define PRIO_STEP \
            "s_add_u32 %[tc], %[tc], 1\n\t" \
            "s_cmp_lg_u32 %[tc], 1\n\t s_cbranch_scc1 L_p1%=\n\t s_setprio 2\n\t L_p1%=:\n\t" \
            "s_cmp_lg_u32 %[tc], 2\n\t s_cbranch_scc1 L_p2%=\n\t s_setprio 1\n\t L_p2%=:\n\t" \
            "s_cmp_lg_u32 %[tc], 3\n\t s_cbranch_scc1 L_p3%=\n\t s_setprio 0\n\t L_p3%=:\n\t"
        asm volatile(
            PRIO_INIT
            "s_mov_b32 %[tc], 0\n\t"
            "s_lshl_b32 %[t], %[task], 5\n\t"
            "v_add_u32 v127, %[t], v91\n\t"
            "ds_read_b64 v[122:123], v127\n\t"
            "s_mov_b64 exec, 1\n\t"
            "ds_add_rtn_u32 v125, v93, v95\n\t"
            "s_mov_b64 exec, -1\n\t"
            "L_task%=:\n\t"
            "s_waitcnt lgkmcnt(0)\n\t"
            "v_readfirstlane_b32 %[nxt], v125\n\t"
            "v_readfirstlane_b32 %[cnt], v123\n\t"
            "v_mov_b32 v89, v122\n\t"
            "v_and_b32 v127, 0xffffff00, v123\n\t"
            "ds_read_b32 v80, v89\n\t"
            "ds_read_b32 v82, v89 offset:4\n\t"
            "ds_read_b32 v84, v89 offset:8\n\t"
            "ds_read_b32 v86, v89 offset:12\n\t"
            "ds_read_b32 v88, v89 offset:%c[le0]\n\t"
            "ds_read_b32 v90, v89 offset:%c[le1]\n\t"
            "ds_read_b32 v92, v89 offset:%c[le2]\n\t"
            "ds_read_b32 v94, v89 offset:%c[le3]\n\t"
            "v_add_u32 v112, v127, v81\n\t"
            "v_lshl_add_u32 v124, v127, 1, v126\n\t"
            "v_lshrrev_b32 v113, 1, v112\n\t"
            "ds_read_b128 v[116:119], v112 offset:%c[au]\n\t"
            "ds_read_b64 v[120:121], v113\n\t"
            "s_and_b32 %[cnt], %[cnt], 0xff\n\t"
            "s_lshl_b32 %[t], %[nxt], 5\n\t"
            "v_add_u32 v127, %[t], v91\n\t"
            "ds_read_b64 v[122:123], v127\n\t"
            "s_mov_b64 exec, 1\n\t"
            "ds_add_rtn_u32 v125, v93, v95\n\t"
            "s_mov_b64 exec, -1\n\t"
            "s_waitcnt lgkmcnt(9)\n\t"
            "buffer_load_dwordx4 v[64:67], v[80:81], %[bv], 0 idxen offen\n\t"
            "buffer_load_dwordx4 v[68:71], v[82:83], %[bv], 0 idxen offen\n\t"
            "buffer_load_dwordx4 v[72:75], v[84:85], %[bv], 0 idxen offen\n\t"
            "v_mov_b64 v[104:105], 0\n\t"
            "v_mov_b64 v[106:107], 0\n\t"
            "v_mov_b64 v[108:109], 0\n\t"
            "v_mov_b64 v[110:111], 0\n\t"
            "s_waitcnt lgkmcnt(2)\n\t"
            "v_lshlrev_b32 v96, 16, v116\n\t"
            "v_lshlrev_b32 v97, 16, v117\n\t"
            "v_lshlrev_b32 v98, 16, v118\n\t"
            "v_lshlrev_b32 v99, 16, v119\n\t"
            "s_cmp_eq_u32 %[cnt], 0\n\t"
            "s_cbranch_scc1 L_fin%=\n\t"
            "L_loop%=:\n\t"
            STAGE("v64", "v65", "v66", "v67", "v[64:65]", "v[66:67]", "v80", "v[88:89]", "v88", "v[76:79]", "v[86:87]", "16", "%c[le4]")
            "s_cbranch_scc1 L_fin%=\n\t"
            STAGE("v68", "v69", "v70", "v71", "v[68:69]", "v[70:71]", "v82", "v[90:91]", "v90", "v[64:67]", "v[80:81]", "20", "%c[le5]")
            "s_cbranch_scc1 L_fin%=\n\t"
            STAGE("v72", "v73", "v74", "v75", "v[72:73]", "v[74:75]", "v84", "v[92:93]", "v92", "v[68:71]", "v[82:83]", "24", "%c[le6]")
            "s_cbranch_scc1 L_fin%=\n\t"
            STAGE("v76", "v77", "v78", "v79", "v[76:77]", "v[78:79]", "v86", "v[94:95]", "v94", "v[72:75]", "v[84:85]", "28", "%c[le7]")
            "v_add_u32 v89, 16, v89\n\t"
            "s_cbranch_scc0 L_loop%=\n\t"
            "L_fin%=:\n\t"
            "v_pk_add_f32 v[108:109], v[108:109], %[eps]\n\t"
            "v_pk_add_f32 v[110:111], v[110:111], %[eps]\n\t"
            "v_rcp_f32 v108, v108\n\t"
            "v_rcp_f32 v109, v109\n\t"
            "v_rcp_f32 v110, v110\n\t"
            "v_rcp_f32 v111, v111\n\t"
            "v_pk_mul_f32 v[104:105], v[104:105], v[108:109]\n\t"
            "v_pk_mul_f32 v[106:107], v[106:107], v[110:111]\n\t"
            "v_dot2c_f32_bf16 v104, %[shi], v116\n\t"
            "v_dot2c_f32_bf16 v105, %[shi], v117\n\t"
            "v_dot2c_f32_bf16 v106, %[shi], v118\n\t"
            "v_dot2c_f32_bf16 v107, %[shi], v119\n\t"
            "v_maximum3_f32 v104, v104, 0, 0\n\t"
            "v_maximum3_f32 v105, v105, 0, 0\n\t"
            "v_maximum3_f32 v106, v106, 0, 0\n\t"
            "v_maximum3_f32 v107, v107, 0, 0\n\t"
            "v_dot2c_f32_bf16 v104, %[slo], v120\n\t"
            "v_dot2c_f32_bf16 v105, %[shi], v120\n\t"
            "v_dot2c_f32_bf16 v106, %[slo], v121\n\t"
            "v_dot2c_f32_bf16 v107, %[shi], v121\n\t"
            "s_nop 1\n\t"
            "v_cvt_pk_bf16_f32 v112, v104, v105\n\t"
            "v_cvt_pk_bf16_f32 v113, v106, v107\n\t"
            "buffer_store_dwordx2 v[112:113], v124, %[o], 0 offen\n\t"
            PRIO_STEP
            "s_cmp_lt_u32 %[nxt], %[ntask]\n\t"
            "s_cbranch_scc1 L_task%=\n\t"
            "s_waitcnt vmcnt(0) lgkmcnt(0)"
            : [cnt] "=&s"(s_cnt), [nxt] "=&s"(s_nxt), [t] "=&s"(s_t), [tc] "=&s"(s_tc)
            : "{v81}"(joff), "{v83}"(joff), "{v85}"(joff), "{v87}"(joff), "{v91}"(g8), "{v93}"(ctr), "{v95}"(one), "{v126}"(j8),
              "{v[100:101]}"(w01), "{v[102:103]}"(w23),
              [task] "s"(w), [ntask] "s"(ntask), [bv] "s"(srdBV), [o] "s"(srdO), [slo] "s"(0x00003F80u), [shi] "s"(0x3F800000u), [eps] "s"(eps2),
              [au] "i"(K2_AU), [le0] "i"(K2_LSZ), [le1] "i"(K2_LSZ + 4), [le2] "i"(K2_LSZ + 8), [le3] "i"(K2_LSZ + 12),
              [le4] "i"(K2_LSZ + 16), [le5] "i"(K2_LSZ + 20), [le6] "i"(K2_LSZ + 24), [le7] "i"(K2_LSZ + 28)
            : "v64", "v65", "v66", "v67", "v68", "v69", "v70", "v71", "v72", "v73", "v74", "v75", "v76", "v77", "v78", "v79",
              "v80", "v82", "v84", "v86", "v88", "v89", "v90", "v92", "v94", "v96", "v97", "v98", "v99",
              "v104", "v105", "v106", "v107", "v108", "v109", "v110", "v111", "v112", "v113", "v114", "v115",
              "v116", "v117", "v118", "v119", "v120", "v121", "v122", "v123", "v124", "v125", "v127", "scc", "memory");
#undef STAGE
    }
    STAMP(10);
    STAMPC(15);
    SPAN1(1);
    asm volatile("s_waitcnt vmcnt(0)" ::: "memory");
    __threadfence();
    unsigned arrive = 0;
    if (lane == 0) arrive = atomicAdd((unsigned*)((char*)ws + (211u << 20)), 1u);
    arrive = __builtin_amdgcn_readfirstlane(arrive);
    if (arrive == 256 * 16 - 1) {
        __threadfence();
        const unsigned long long* sp = (const unsigned long long*)((const char*)ws + (192u << 20));
        unsigned long long mn1 = ~0ull, mx1 = 0, mn2 = ~0ull, mx2 = 0;
        for (int i = lane; i < 256 * 16; i += 64) { const int b2 = i >> 4, w2 = i & 15;
            if (w2 < 8) { const unsigned long long a = sp[(b2 * 16 + w2) * 2], e = sp[(b2 * 16 + w2) * 2 + 1]; mn1 = a < mn1 ? a : mn1; mx1 = e > mx1 ? e : mx1; }
            const unsigned long long a = __builtin_nontemporal_load(&sp[((256 + b2) * 16 + w2) * 2]), e = __builtin_nontemporal_load(&sp[((256 + b2) * 16 + w2) * 2 + 1]); mn2 = a < mn2 ? a : mn2; mx2 = e > mx2 ? e : mx2; }
        for (int o = 32; o; o >>= 1) { unsigned long long t;
            t = __shfl_xor(mn1, o); mn1 = t < mn1 ? t : mn1; t = __shfl_xor(mx1, o); mx1 = t > mx1 ? t : mx1;
            t = __shfl_xor(mn2, o); mn2 = t < mn2 ? t : mn2; t = __shfl_xor(mx2, o); mx2 = t > mx2 ? t : mx2; }
        unsigned long long val = 0;
        if (MEASURE == 1) val = mx1 - mn1;
        if (MEASURE == 2) val = mn2 - mx1;
        if (MEASURE == 3) val = mx2 - mn2;
        const unsigned long long t0 = __builtin_amdgcn_s_memrealtime();
        while (__builtin_amdgcn_s_memrealtime() - t0 < val * 10ull) __builtin_amdgcn_s_sleep(2);
    }
}

extern "C" void kernel_launch(void* const* d_in, const int* in_sizes, int n_in,
                              void* d_out, int out_size, void* d_ws, size_t ws_size,
                              hipStream_t stream) {
    const u16* x = (const u16*)d_in[0];
    const u16* ea = (const u16*)d_in[1];
    const int* src = (const int*)d_in[2];
    const int* dst = (const int*)d_in[3];
    const u16* wa = (const u16*)d_in[4];
    const u16* wb = (const u16*)d_in[5];
    const u16* wc = (const u16*)d_in[6];
    const u16* wu = (const u16*)d_in[7];
    const u16* wv = (const u16*)d_in[8];
    (void)hipFuncSetAttribute((const void*)k1_gemm_bin, hipFuncAttributeMaxDynamicSharedMemorySize, K1_LDS);
    k1_gemm_bin<<<256, 512, K1_LDS, stream>>>(x, wa, wb, wu, wv, src, dst, ea, (char*)d_ws);
    (void)hipFuncSetAttribute((const void*)k2_edges, hipFuncAttributeMaxDynamicSharedMemorySize, K2_LDS);
    k2_edges<<<256, 1024, K2_LDS, stream>>>(x, wc, src, dst, ea, (const char*)d_ws, (u16*)d_out);
}
```

```cpp
#define MEASURE 0
#define SPAN 1
#include <hip/hip_runtime.h>
#include <hip/hip_bf16.h>
#include <stdint.h>

typedef unsigned short u16;
typedef unsigned int u32;
typedef u32 u32x2 __attribute__((ext_vector_type(2)));
typedef u32 u32x4 __attribute__((ext_vector_type(4)));
typedef float f32x2 __attribute__((ext_vector_type(2)));
typedef float f32x4 __attribute__((ext_vector_type(4)));
typedef float f32x16 __attribute__((ext_vector_type(16)));
typedef short s16x4 __attribute__((ext_vector_type(4)));
typedef short s16x8 __attribute__((ext_vector_type(8)));
typedef __bf16 bf16x8 __attribute__((ext_vector_type(8)));
typedef __bf16 bf16x4 __attribute__((ext_vector_type(4)));
typedef __bf16 bf16x2 __attribute__((ext_vector_type(2)));

constexpr int NN = 10000;
constexpr int NE = 160000;
constexpr int DD = 256;
constexpr int NP = 10048;
constexpr int RG = 320;
constexpr int NBK = 64;
constexpr int NB = 157;
constexpr int NCH = 64;
constexpr int CHE = 2500;
constexpr int CAP = 80;
constexpr int CAPN = 44;
constexpr int LST = 45;
constexpr float NLOG2E = -1.4426950408889634f;
constexpr u32 NEUTRAL_REC = (u32)NN;
constexpr u32 NEUTRAL_BV = 0x00007F00u;

constexpr size_t OFF_BV = 0;
constexpr size_t OFF_AU = OFF_BV + (size_t)4 * NP * 256;
constexpr size_t OFF_R4 = OFF_AU + (size_t)4 * NP * 256;
constexpr size_t OFF_R2 = OFF_R4 + (size_t)NBK * NCH * CAP * 4;
constexpr size_t OFF_CNT = OFF_R2 + (size_t)NBK * NCH * CAP * 2;

#define LDS_AS __attribute__((address_space(3)))
#define GLB_AS __attribute__((address_space(1)))

__device__ __forceinline__ void glds16(const void* g, void* l) {
    __builtin_amdgcn_global_load_lds((const GLB_AS u32*)g, (LDS_AS u32*)l, 16, 0, 0);
}
__device__ __forceinline__ void glds16a(const void* sbase, u32 voff, u32 lds) {
    u32 keep;
    asm volatile("s_mov_b32 %0, m0\n\ts_mov_b32 m0, %3\n\ts_nop 0\n\tglobal_load_lds_dwordx4 %1, %2\n\ts_mov_b32 m0, %0" : "=&s"(keep) : "v"(voff), "s"(sbase), "s"(lds) : "memory");
}
__device__ __forceinline__ u32 cvt_pk_bf16(float lo, float hi) {
    const f32x2 v = {lo, hi}; return __builtin_bit_cast(u32, __builtin_convertvector(v, bf16x2));
}
#define WAIT_V(n) asm volatile("s_waitcnt vmcnt(" #n ")" ::: "memory")
#define WAIT_L(n) asm volatile("s_waitcnt lgkmcnt(" #n ")" ::: "memory")
#define BAR() __builtin_amdgcn_s_barrier()
#define EXIT2_AT 99
#ifndef EXIT_AT
#define EXIT_AT 99
#endif
#define STORE_AUX 16
#define SPAN0() unsigned long long span_t0_ = __builtin_amdgcn_s_memrealtime()
#define SPAN1(K) do { asm volatile("s_waitcnt vmcnt(0) lgkmcnt(0)" ::: "memory"); unsigned long long t1_ = __builtin_amdgcn_s_memrealtime(); if ((threadIdx.x & 63) == 0) { unsigned long long* p_ = (unsigned long long*)((char*)ws + (192u << 20)) + (((K) * 256 + blockIdx.x) * 16 + (threadIdx.x >> 6)) * 2; p_[0] = span_t0_; p_[1] = t1_; } } while (0)
#define STAMP(i)
#define STAMPC(i)
#define TICK(v)

constexpr int K1_R0 = 0, K1_R1 = 65536, K1_SCR = 131072;
constexpr int K1_ST4 = K1_SCR, K1_ST2 = K1_ST4 + NBK * CAP * 4, K1_BC = K1_ST2 + NBK * CAP * 2;
constexpr int K1_LDS = K1_SCR + 32768;
static_assert(K1_BC + NBK * 4 <= K1_LDS, "lds");

__global__ __launch_bounds__(512, 2) void k1_gemm_bin(
    const u16* __restrict__ x, const u16* __restrict__ wa, const u16* __restrict__ wb, const u16* __restrict__ wu, const u16* __restrict__ wv,
    const int* __restrict__ src, const int* __restrict__ dst, const u16* __restrict__ ea, char* __restrict__ ws) {
    extern __shared__ __attribute__((aligned(16))) char smem[];
    const int tid = threadIdx.x, lane = tid & 63;
    const int w = __builtin_amdgcn_readfirstlane(tid >> 6);
    const int b = blockIdx.x;
    const int type = (b >> 5) & 1;
    const int ct2 = (b >> 3) & 3;
    const int rg = (b & 7) * 4 + (b >> 6);
    const int row0 = rg * RG;
    const int chunk = (b & 31) | ((b >> 6) << 5) ;
    const bool binner = type == 1 && b < 128;
    constexpr int KOFF = 0;
    SPAN0();
#if EXIT_AT == 0
    if (ws) { SPAN1(0); return; }
#endif
    STAMP(0);
    int bd[5], bs[5]; u32 be[5];
    if (binner) {
#pragma unroll
        for (int i = 0; i < 5; ++i) {
            const int k = tid + 512 * i;
            const int e = chunk * CHE + (k < CHE ? k : 0);
            bd[i] = dst[e]; bs[i] = src[e]; be[i] = ea[e];
        }
        if (tid < NBK) *(u32*)(smem + K1_BC + tid * 4) = 0;
    }
    {
        const int m = w >> 2;
        const u16* W = type == 0 ? (m == 0 ? wb : wv) : (m == 0 ? wa : wu);
        const int kb0 = (w & 3) * 64, kr = lane >> 3;
        const u16* g = W + (size_t)(kb0 + kr) * DD + ct2 * 64 + (((lane & 7) ^ ((kr & 2) << 1)) << 3);
#pragma unroll
        for (int i = 0; i < 8; ++i) glds16(g + (size_t)i * 8 * DD, smem + K1_R0 + m * 32768 + (kb0 / 8 + i) * 1024);
    }
    const int xr = lane >> 5, xp = lane & 31;
    auto stage_x = [&](int tile, int q, int ldsbase) {
        const int r = q * 2 + xr;
        int grow = row0 + tile * 32 + r; grow = grow < NN ? grow : NN - 1;
        glds16((const char*)x + (u32)(grow * 512 + ((xp ^ (r & 15)) << 4)), smem + ldsbase + q * 1024);
    };
#pragma unroll
    for (int i = 0; i < 8; ++i) { const int q = w * 8 + i; stage_x(q >> 4, q & 15, K1_R1 + (q >> 4) * 16384); }
    STAMP(1);
    WAIT_V(8); WAIT_L(0);
    BAR();
    STAMP(2);
#if EXIT_AT == 1
    if (ws) { SPAN1(0); return; }
#endif
    const int ch = w >> 2;
    bf16x8 wf0[16], wf1[16];
    {
        const int h = lane >> 5, blk = (lane >> 4) & 1, q = (lane & 15) >> 2, p = lane & 3;
        const u32 base = K1_R0 + (8 * h + q) * 128 + (((ch * 4 + blk * 2 + (p >> 1)) ^ ((q & 2) << 1)) << 4) + (p & 1) * 8;
#define TRRD(S) { u32x2 a0, a1, b0, b1; \
            asm volatile("ds_read_b64_tr_b16 %0, %4 offset:%c5\n\tds_read_b64_tr_b16 %1, %4 offset:%c6\n\t" \
                         "ds_read_b64_tr_b16 %2, %4 offset:%c7\n\tds_read_b64_tr_b16 %3, %4 offset:%c8" \
                         : "=&v"(a0), "=&v"(a1), "=&v"(b0), "=&v"(b1) \
                         : "v"(base), "i"((S) * 2048), "i"((S) * 2048 + 512), "i"(32768 + (S) * 2048), "i"(32768 + (S) * 2048 + 512) : "memory"); \
            wf0[S] = __builtin_bit_cast(bf16x8, __builtin_shufflevector(a0, a1, 0, 1, 2, 3)); \
            wf1[S] = __builtin_bit_cast(bf16x8, __builtin_shufflevector(b0, b1, 0, 1, 2, 3)); }
        TRRD(0) TRRD(1) TRRD(2) TRRD(3) TRRD(4) TRRD(5) TRRD(6) TRRD(7) TRRD(8) TRRD(9) TRRD(10) TRRD(11) TRRD(12) TRRD(13) TRRD(14) TRRD(15)
#undef TRRD
    }
    if (binner) {
#pragma unroll
        for (int i = 0; i < 5; ++i) {
            const int k = tid + 512 * i;
            if (k < CHE) {
                const int bk = (int)(((u32)bd[i] * 53432u) >> 23);
                const int dl = bd[i] - bk * NB;
                const u32 pos = atomicAdd((u32*)(smem + K1_BC + bk * 4), 1u);
                if (pos < CAP) {
                    *(u32*)(smem + K1_ST4 + (bk * CAP + pos) * 4) = (u32)bs[i] | (be[i] << 16);
                    *(u16*)(smem + K1_ST2 + (bk * CAP + pos) * 2) = (u16)dl;
                }
            }
        }
    }
    STAMP(3);
    WAIT_V(0); WAIT_L(0);
    __builtin_amdgcn_sched_barrier(0);
    BAR();
    STAMP(4);
#if EXIT_AT == 2
    if (ws) { asm volatile("" :: "v"(wf0[0]), "v"(wf1[15])); SPAN1(0); return; }
#endif
#pragma unroll
    for (int i = 0; i < 8; ++i) { const int q = w * 8 + i; stage_x(4 + (q >> 4), q & 15, K1_R0 + (q >> 4) * 16384); }
    if (binner) {
        u32* R4 = (u32*)(ws + OFF_R4); u16* R2 = (u16*)(ws + OFF_R2); u32* CNT = (u32*)(ws + OFF_CNT);
        u32x4 t4[3]; u32x2 t2[3];
#pragma unroll
        for (int i = 0; i < 3; ++i) { const int q = tid + 512 * i; const int qq = q < NBK * (CAP / 4) ? q : 0;
            t4[i] = *(const u32x4*)(smem + K1_ST4 + qq * 16); t2[i] = *(const u32x2*)(smem + K1_ST2 + qq * 8); }
        const u32 cv = *(const u32*)(smem + K1_BC + (tid & 63) * 4);
#pragma unroll
        for (int i = 0; i < 3; ++i) { const int q = tid + 512 * i;
            if (q < NBK * (CAP / 4)) { const int bk = q / (CAP / 4), o = q - bk * (CAP / 4);
                *(u32x4*)(R4 + ((size_t)bk * NCH + chunk) * CAP + o * 4) = t4[i];
                *(u32x2*)(R2 + ((size_t)bk * NCH + chunk) * CAP + o * 4) = t2[i]; } }
        if (tid < NBK) CNT[tid * NCH + chunk] = cv;
        WAIT_L(0);
        BAR();
    }
    if (rg == 0 && type == 0 && w == 0) ((u32*)(ws + OFF_BV))[((size_t)ct2 * NP + NN) * 64 + lane] = NEUTRAL_BV;

    const int r = lane & 31, h = lane >> 5;
    const int abase = r * 512 + ((((r & 14) | ((h ^ r) & 1))) << 4);
    char* const scr = smem + K1_SCR + w * 4096;
    const __amdgpu_buffer_rsrc_t trs = __builtin_amdgcn_make_buffer_rsrc(ws + (type ? OFF_AU : OFF_BV) + (size_t)ct2 * NP * 256, 0, NN * 256, 0x00020000);
    const __amdgpu_buffer_rsrc_t trs2 = __builtin_amdgcn_make_buffer_rsrc(ws + (64u << 20) + (type ? OFF_AU : OFF_BV) + (size_t)ct2 * NP * 256, 0, NN * 256, 0x00020000);
    const u32 svo = (lane >> 3) * 256 + ch * 128 + (lane & 7) * 16;

    const int s4 = w & 3;
    if (ch == 0) __builtin_amdgcn_s_setprio(1);
    STAMP(5);
#pragma unroll 1
    for (int st = 0; st < (EXIT_AT == 3 ? 1 : (EXIT_AT == 4 ? 2 : 3)); ++st) {
        int tile, ldsbase;
        if (st == 0) { tile = s4; ldsbase = K1_R1 + s4 * 16384; }
        else if (st == 1) { tile = 4 + s4; ldsbase = K1_R0 + s4 * 16384; }
        else { const int t = (w < 2) ? w : (w >= 6 ? w - 6 : 99); tile = 8 + t; ldsbase = K1_R1 + (t & 1) * 16384; }
        const int trow = row0 + tile * 32;
        const bool active = tile < 10 && trow < NN;
        unsigned long long tkA = 0, tkB = 0, tkC = 0, tkD = 0;
        if (active) {
            TICK(tkA);
            bf16x8 a[16];
#pragma unroll
            for (int s = 0; s < 8; ++s) a[s] = *(const bf16x8*)(smem + ldsbase + (abase ^ (s << 5)));
            __builtin_amdgcn_sched_barrier(0);
            f32x16 acc0, acc1;
#pragma unroll
            for (int i = 0; i < 16; ++i) { acc0[i] = 0.f; acc1[i] = 0.f; }
#pragma unroll
            for (int s = 0; s < 8; ++s) {
                a[s + 8] = *(const bf16x8*)(smem + ldsbase + (abase ^ (s << 5)) + 256);
                acc0 = __builtin_amdgcn_mfma_f32_32x32x16_bf16(a[s], wf0[s], acc0, 0, 0, 0);
                acc1 = __builtin_amdgcn_mfma_f32_32x32x16_bf16(a[s], wf1[s], acc1, 0, 0, 0);
                __builtin_amdgcn_sched_group_barrier(0x100, 1, 0);
                __builtin_amdgcn_sched_group_barrier(0x8, 2, 0);
            }
            __builtin_amdgcn_sched_barrier(0);
#pragma unroll
            for (int s = 8; s < 16; ++s) {
                acc0 = __builtin_amdgcn_mfma_f32_32x32x16_bf16(a[s], wf0[s], acc0, 0, 0, 0);
                acc1 = __builtin_amdgcn_mfma_f32_32x32x16_bf16(a[s], wf1[s], acc1, 0, 0, 0);
            }
            TICK(tkB);
            char* const sw = scr + h * 512 + r * 4;
            if (type == 0) {
#pragma unroll
                for (int i = 0; i < 16; i += 2) {
                    const u32 bb = cvt_pk_bf16(acc0[i] * NLOG2E, acc0[i + 1] * NLOG2E);
#pragma unroll
                    for (int j = 0; j < 2; ++j) {
                        const u32 vb = __float_as_uint(acc1[i + j]);
                        const u32 bl = j ? (bb >> 16) : (bb & 0xFFFFu);
                        const u32 u = __builtin_elementwise_sub_sat((vb & 0x7FFFFFFFu) + 0x8000u, bl);
                        *(u32*)(sw + (((i + j) & 3) + 8 * ((i + j) >> 2)) * 128) = (vb & 0x80000000u) | (u & 0x7FFF0000u) | bl;
                    }
                }
            } else {
#pragma unroll
                for (int i = 0; i < 16; ++i) *(u32*)(sw + ((i & 3) + 8 * (i >> 2)) * 128) = cvt_pk_bf16(acc0[i] * NLOG2E, acc1[i]);
            }
            TICK(tkC);
#pragma unroll
            for (int i = 0; i < 4; ++i) {
                const u32x4 v = *(const u32x4*)(scr + i * 1024 + lane * 16);
                __builtin_amdgcn_raw_buffer_store_b128(v, trs, svo + (u32)(trow + i * 8) * 256, 0, STORE_AUX);
                __builtin_amdgcn_raw_buffer_store_b128(v, trs2, svo + (u32)(trow + i * 8) * 256, 0, STORE_AUX);
            }
        }
        TICK(tkD);
        if (st == 0) STAMP(6); else if (st == 1) STAMP(8); else STAMP(10);
        if (st < 2) {
            if (active) WAIT_V(8); else WAIT_V(0);
            BAR();
            if (st == 0) {
#pragma unroll
                for (int i = 0; i < 4; ++i) { const int q = w * 4 + i; stage_x(8 + (q >> 4), q & 15, K1_R1 + (q >> 4) * 16384); }
            }
        }
    }
    SPAN1(0);
}

constexpr int LSTB = LST * 4;
constexpr int K2_X = 0;
constexpr int K2_AU = K2_X + 20480;
constexpr int K2_LIDX = K2_AU + 40960;
constexpr int K2_LSZ = (NB * LSTB + 32 + 256 + 15) / 16 * 16;
constexpr int K2_LE = K2_LIDX + K2_LSZ;
constexpr int K2_CNT = K2_LE + K2_LSZ;
constexpr int K2_HIST = K2_CNT + 160 * 4;
constexpr int K2_ORD = K2_HIST + 128 * 4;
constexpr int K2_MISC = K2_ORD + 80 * 32;
constexpr int K2_LDS = K2_MISC + 16;
constexpr int K2_TRASH = NB * LSTB + 32;
static_assert(K2_TRASH + 256 <= K2_LSZ, "trash");
static_assert(K2_LSZ < 65536 - 64, "ds offset");

__device__ __forceinline__ float dot2sel(u32 d, u32 sel, float c) {
    return __builtin_amdgcn_fdot2_f32_bf16(__builtin_bit_cast(bf16x2, d), __builtin_bit_cast(bf16x2, sel), c, false);
}

struct Acc { f32x2 n01, n23, d01, d23; };

__device__ __forceinline__ void edge_step(Acc& a, const u32x4 d, const u32 rec, const f32x2 a01, const f32x2 a23, const f32x2 w01, const f32x2 w23, const u32 sel) {
    const float e = __uint_as_float(rec & 0xFFFF0000u);
    const f32x2 ee = {e, e};
    const f32x2 t01 = __builtin_elementwise_fma(ee, w01, a01);
    const f32x2 t23 = __builtin_elementwise_fma(ee, w23, a23);
    f32x2 E01, E23;
    E01.x = __builtin_amdgcn_exp2f(dot2sel(d.x, sel, t01.x));
    E01.y = __builtin_amdgcn_exp2f(dot2sel(d.y, sel, t01.y));
    E23.x = __builtin_amdgcn_exp2f(dot2sel(d.z, sel, t23.x));
    E23.y = __builtin_amdgcn_exp2f(dot2sel(d.w, sel, t23.y));
    const f32x2 one = {1.f, 1.f};
    E01 += one; E23 += one;
    f32x2 s01, s23;
    s01.x = __builtin_amdgcn_rcpf(E01.x); s01.y = __builtin_amdgcn_rcpf(E01.y);
    s23.x = __builtin_amdgcn_rcpf(E23.x); s23.y = __builtin_amdgcn_rcpf(E23.y);
    a.d01 += s01; a.d23 += s23;
    const f32x2 v01 = {__uint_as_float(d.x), __uint_as_float(d.y)};
    const f32x2 v23 = {__uint_as_float(d.z), __uint_as_float(d.w)};
    a.n01 = __builtin_elementwise_fma(s01, v01, a.n01);
    a.n23 = __builtin_elementwise_fma(s23, v23, a.n23);
}

__device__ __forceinline__ void node_finish(const Acc& a, const u32x4 au, const u32x2 xv, u16* outp) {
    const float num[4] = {a.n01.x, a.n01.y, a.n23.x, a.n23.y};
    const float den[4] = {a.d01.x, a.d01.y, a.d23.x, a.d23.y};
    const u32 aus[4] = {au.x, au.y, au.z, au.w};
    const float xs[4] = {__uint_as_float(xv.x << 16), __uint_as_float(xv.x & 0xFFFF0000u), __uint_as_float(xv.y << 16), __uint_as_float(xv.y & 0xFFFF0000u)};
    float o[4];
#pragma unroll
    for (int c = 0; c < 4; ++c) {
        const float z = __uint_as_float(aus[c] & 0xFFFF0000u) + num[c] * __builtin_amdgcn_rcpf(den[c] + 1e-6f);
        const float rl = z > 0.f ? z : 0.f * z;
        o[c] = xs[c] + rl;
    }
    u32x2 ov; ov.x = cvt_pk_bf16(o[0], o[1]); ov.y = cvt_pk_bf16(o[2], o[3]);
    *(u32x2*)outp = ov;
}

template <int CTRL, int RM> __device__ __forceinline__ u32 dpp_add(u32 v) {
    return v + (u32)__builtin_amdgcn_update_dpp(0, (int)v, CTRL, RM, 0xf, false);
}

__global__ __launch_bounds__(1024, 4) void k2_edges(
    const u16* __restrict__ x, const u16* __restrict__ wc, const int* __restrict__ src, const int* __restrict__ dst, const u16* __restrict__ ea,
    const char* __restrict__ ws, u16* __restrict__ out) {
    extern __shared__ __attribute__((aligned(16))) char smem[];
    const int tid = threadIdx.x, lane = tid & 63;
    const int w = __builtin_amdgcn_readfirstlane(tid >> 6);
    const int b = blockIdx.x;
    const int slice = (b & 7) >> 1;
    const int bucket = (b >> 3) * 2 + (b & 1);
    const int node0 = bucket * NB;
    const int nvalid = (NN - node0) < NB ? (NN - node0) : NB;
    u32* const cnt = (u32*)(smem + K2_CNT);
    u32* const hist = (u32*)(smem + K2_HIST);
    u32* const misc = (u32*)(smem + K2_MISC);
    constexpr int KOFF = 256 * 16;
    SPAN0();
    STAMP(0);

    const u32* R4 = (const u32*)(ws + OFF_R4) + (size_t)bucket * NCH * CAP;
    const u16* R2 = (const u16*)(ws + OFF_R2) + (size_t)bucket * NCH * CAP;
    const u32* CNT = (const u32*)(ws + OFF_CNT) + bucket * NCH;
    const int ch0 = tid / (CAP / 4), ch1 = (tid + 1024) / (CAP / 4);
    const u32x4 r40 = *(const u32x4*)(R4 + tid * 4);
    const u32x2 r20 = *(const u32x2*)(R2 + tid * 4);
    const u32 c0 = CNT[ch0];
    u32x4 r41 = {0, 0, 0, 0}; u32x2 r21 = {0, 0}; u32 c1 = 0;
    if (w < 4) { r41 = *(const u32x4*)(R4 + (tid + 1024) * 4); r21 = *(const u32x2*)(R2 + (tid + 1024) * 4); c1 = CNT[ch1]; }
    const char* const bvb = ws + OFF_BV + (size_t)slice * NP * 256;
    const char* const aub = ws + OFF_AU + (size_t)slice * NP * 256;
    const int g = lane >> 4, j = lane & 15;
    const u32x2 wcv = *(const u32x2*)(wc + slice * 64 + j * 4);
    for (int i = tid; i < K2_LSZ / 16; i += 1024) {
        *(u32x4*)(smem + K2_LIDX + i * 16) = (u32x4){NEUTRAL_REC, NEUTRAL_REC, NEUTRAL_REC, NEUTRAL_REC};
        *(u32x4*)(smem + K2_LE + i * 16) = (u32x4){0, 0, 0, 0};
    }
    if (tid < 160 + 64) cnt[tid] = 0;
    if (tid == 0) { misc[0] = 16; misc[1] = 0; }
    f32x2 w01 = {__uint_as_float(wcv.x << 16) * NLOG2E, __uint_as_float(wcv.x & 0xFFFF0000u) * NLOG2E};
    f32x2 w23 = {__uint_as_float(wcv.y << 16) * NLOG2E, __uint_as_float(wcv.y & 0xFFFF0000u) * NLOG2E};
    asm volatile("" : "+v"(w01), "+v"(w23));
    STAMP(1);
    WAIT_V(0); WAIT_L(0); BAR();
    {
        const char* ga = aub + (size_t)node0 * 256;
#pragma unroll
        for (int i = 0; i < 3; ++i) { const int q = w + 16 * i; if (q < 40) glds16a(ga, q * 1024 + lane * 16, K2_AU + q * 1024); }
        const char* gx = (const char*)x + slice * 128;
#pragma unroll
        for (int i = 0; i < 2; ++i) {
            const int q = w + 16 * i;
            if (q < 20) { int row = node0 + q * 8 + (lane >> 3); row = row < NN ? row : NN - 1;
                glds16a(gx, row * 512 + (lane & 7) * 16, K2_X + q * 1024); }
        }
    }
    STAMP(2);
    {
        const int p0 = (tid - ch0 * (CAP / 4)) * 4;
        const u32 rec0[4] = {r40.x, r40.y, r40.z, r40.w};
        const u32 dl0[4] = {r20.x & 0xFFFF, r20.x >> 16, r20.y & 0xFFFF, r20.y >> 16};
        u32 slot[4];
        bool ovf = c0 > CAP;
#pragma unroll
        for (int k = 0; k < 4; ++k) slot[k] = atomicAdd(&cnt[p0 + k < c0 ? dl0[k] : 224 + lane], 1u);
        if (w < 4) {
            const int p1 = (tid + 1024 - ch1 * (CAP / 4)) * 4;
            const u32 rec1[4] = {r41.x, r41.y, r41.z, r41.w};
            const u32 dl1[4] = {r21.x & 0xFFFF, r21.x >> 16, r21.y & 0xFFFF, r21.y >> 16};
            u32 slot1[4];
            ovf |= c1 > CAP;
#pragma unroll
            for (int k = 0; k < 4; ++k) slot1[k] = atomicAdd(&cnt[p1 + k < c1 ? dl1[k] : 224 + lane], 1u);
#pragma unroll
            for (int k = 0; k < 4; ++k) {
                const bool v = p1 + k < c1, ok = v && slot1[k] < CAPN;
                ovf |= v && !ok;
                const u32 o = ok ? dl1[k] * LSTB + slot1[k] * 4 : K2_TRASH + lane * 4;
                *(u32*)(smem + K2_LIDX + o) = rec1[k] & 0xFFFFu;
                *(u32*)(smem + K2_LE + o) = rec1[k] & 0xFFFF0000u;
            }
        }
#pragma unroll
        for (int k = 0; k < 4; ++k) {
            const bool v = p0 + k < c0, ok = v && slot[k] < CAPN;
            ovf |= v && !ok;
            const u32 o = ok ? dl0[k] * LSTB + slot[k] * 4 : K2_TRASH + lane * 4;
            *(u32*)(smem + K2_LIDX + o) = rec0[k] & 0xFFFFu;
            *(u32*)(smem + K2_LE + o) = rec0[k] & 0xFFFF0000u;
        }
        if (ovf) misc[1] = 1;
    }
    STAMP(3);
    WAIT_L(0); BAR();
    STAMP(4);
    const bool slow = misc[1] != 0;

    const u32 joff = j * 16;

    if (slow) {
        u32 sel = 0x00003F80u;
        asm volatile("" : "+s"(sel));
        for (int task = w; task * 4 < nvalid; task += 16) {
            const int n = task * 4 + g;
            const bool nv = n < nvalid;
            const int node = node0 + (nv ? n : 0);
            const u32x4 au = *(const u32x4*)(aub + (size_t)node * 256 + joff);
            const u32x2 xv = *(const u32x2*)(x + (size_t)node * DD + slice * 64 + j * 4);
            const f32x2 a01 = {__uint_as_float(au.x << 16), __uint_as_float(au.y << 16)};
            const f32x2 a23 = {__uint_as_float(au.z << 16), __uint_as_float(au.w << 16)};
            Acc acc = {{0.f, 0.f}, {0.f, 0.f}, {0.f, 0.f}, {0.f, 0.f}};
            const int lo = node0 + task * 4;
            for (int e0 = 0; e0 < NE; e0 += 64) {
                const int d = dst[e0 + lane];
                unsigned long long m = __ballot((u32)(d - lo) < 4u);
                if (m) {
                    const u32 rc = (u32)src[e0 + lane] | ((u32)ea[e0 + lane] << 16);
                    while (m) {
                        const int l = __builtin_ctzll(m); m &= m - 1;
                        const int dd = __builtin_amdgcn_readlane(d, l);
                        const u32 rr = __builtin_amdgcn_readlane(rc, l);
                        const u32 rec = (nv && dd == node) ? rr : NEUTRAL_REC;
                        const u32x4 dv = *(const u32x4*)(bvb + (size_t)((rec & 0xFFFFu) * 256 + joff));
                        edge_step(acc, dv, rec, a01, a23, w01, w23, sel);
                    }
                }
            }
            if (nv) node_finish(acc, au, xv, out + (size_t)node * DD + slice * 64 + j * 4);
        }
        return;
    }

    u32 mydeg = 0, myrank = 0;
    if (tid < nvalid) { mydeg = cnt[tid]; myrank = atomicAdd(&hist[63 - mydeg], 1u); }
    WAIT_L(0); BAR();
    if (w < 3) {
        const u32 h0 = hist[lane];
        u32 v = h0;
        v = dpp_add<0x111, 0xf>(v); v = dpp_add<0x112, 0xf>(v); v = dpp_add<0x114, 0xf>(v); v = dpp_add<0x118, 0xf>(v);
        v = dpp_add<0x142, 0xa>(v); v = dpp_add<0x143, 0xc>(v);
        const u32 base = (u32)__builtin_amdgcn_ds_bpermute((int)((63 - mydeg) * 4), (int)(v - h0));
        if (tid < nvalid) {
            const u32 pos = base + myrank;
            const u32x2 ent = {(u32)(K2_LIDX + tid * LSTB), (u32)(tid * 256) + mydeg};
            u32x2* o = (u32x2*)(smem + K2_ORD) + pos;
            o[0] = ent;
            if (pos == (u32)nvalid - 1) { o[1] = ent; o[2] = ent; o[3] = ent; }
        }
    }
    WAIT_V(0); WAIT_L(0); BAR();
    STAMP(5);
    STAMPC(14);

    const int ntask = (nvalid + 3) >> 2;
    if (w < ntask) {
        u32x4 srdBV, srdO;
        { const size_t p = (size_t)bvb; srdBV = (u32x4){(u32)p, (u32)(p >> 32) | (256u << 16), 0xFFFFFFFFu, 0x00020000u}; }
        { const size_t ob = (size_t)node0 * 512 + slice * 128; const size_t p = (size_t)out + ob; srdO = (u32x4){(u32)p, (u32)(p >> 32), (u32)((size_t)NN * 512 - ob), 0x00020000u}; }
        u32 g8 = K2_ORD + g * 8, ctr = K2_MISC, one = 1, j8 = j * 8;
        unsigned long long eps2 = 0x358637BD358637BDull;
        u32 s_cnt, s_nxt, s_t, s_tc;
#define STAGE(DS0, DS1, DS2, DS3, DSL, DSH, IX, EP, E1, DN, IXN, O1, O2) \
            "s_waitcnt lgkmcnt(0)\n\t" \
            "buffer_load_dwordx4 " DN ", " GIX(IXN) ", %[bv], 0 idxen offen\n\t" \
            "v_pk_fma_f32 v[112:113], " EP ", v[100:101], v[96:97] op_sel_hi:[0,1,1]\n\t" \
            "v_pk_fma_f32 v[114:115], " EP ", v[102:103], v[98:99] op_sel_hi:[0,1,1]\n\t" \
            "ds_read_b32 " IX ", v89 offset:" O1 "\n\t" \
            "ds_read_b32 " E1 ", v89 offset:" O2 "\n\t" \
            "s_waitcnt vmcnt(3)\n\t" \
            "v_dot2c_f32_bf16 v112, %[slo], " DS0 "\n\t" \
            "v_dot2c_f32_bf16 v113, %[slo], " DS1 "\n\t" \
            "v_dot2c_f32_bf16 v114, %[slo], " DS2 "\n\t" \
            "v_dot2c_f32_bf16 v115, %[slo], " DS3 "\n\t" \
            "v_exp_f32 v112, v112\n\t" \
            "v_exp_f32 v113, v113\n\t" \
            "v_exp_f32 v114, v114\n\t" \
            "v_exp_f32 v115, v115\n\t" \
            "v_pk_add_f32 v[112:113], v[112:113], 1.0 op_sel_hi:[1,0]\n\t" \
            "v_pk_add_f32 v[114:115], v[114:115], 1.0 op_sel_hi:[1,0]\n\t" \
            "v_rcp_f32 v112, v112\n\t" \
            "v_rcp_f32 v113, v113\n\t" \
            "v_rcp_f32 v114, v114\n\t" \
            "v_rcp_f32 v115, v115\n\t" \
            "s_sub_u32 %[cnt], %[cnt], 1\n\t" \
            "v_pk_add_f32 v[108:109], v[108:109], v[112:113]\n\t" \
            "v_pk_fma_f32 v[104:105], v[112:113], " DSL ", v[104:105]\n\t" \
            "v_pk_add_f32 v[110:111], v[110:111], v[114:115]\n\t" \
            "v_pk_fma_f32 v[106:107], v[114:115], " DSH ", v[106:107]\n\t" \
            "s_cmp_eq_u32 %[cnt], 0\n\t"
#define GIX(x) x
#define PRIO_ON 1
#define PRIO_INIT "s_setprio 3\n\t"
#define PRIO_STEP \
            "s_add_u32 %[tc], %[tc], 1\n\t" \
            "s_cmp_lg_u32 %[tc], 1\n\t s_cbranch_scc1 L_p1%=\n\t s_setprio 2\n\t L_p1%=:\n\t" \
            "s_cmp_lg_u32 %[tc], 2\n\t s_cbranch_scc1 L_p2%=\n\t s_setprio 1\n\t L_p2%=:\n\t" \
            "s_cmp_lg_u32 %[tc], 3\n\t s_cbranch_scc1 L_p3%=\n\t s_setprio 0\n\t L_p3%=:\n\t"
        asm volatile(
            PRIO_INIT
            "s_mov_b32 %[tc], 0\n\t"
            "s_lshl_b32 %[t], %[task], 5\n\t"
            "v_add_u32 v127, %[t], v91\n\t"
            "ds_read_b64 v[122:123], v127\n\t"
            "s_mov_b64 exec, 1\n\t"
            "ds_add_rtn_u32 v125, v93, v95\n\t"
            "s_mov_b64 exec, -1\n\t"
            "L_task%=:\n\t"
            "s_waitcnt lgkmcnt(0)\n\t"
            "v_readfirstlane_b32 %[nxt], v125\n\t"
            "v_readfirstlane_b32 %[cnt], v123\n\t"
            "v_mov_b32 v89, v122\n\t"
            "v_and_b32 v127, 0xffffff00, v123\n\t"
            "ds_read_b32 v80, v89\n\t"
            "ds_read_b32 v82, v89 offset:4\n\t"
            "ds_read_b32 v84, v89 offset:8\n\t"
            "ds_read_b32 v86, v89 offset:12\n\t"
            "ds_read_b32 v88, v89 offset:%c[le0]\n\t"
            "ds_read_b32 v90, v89 offset:%c[le1]\n\t"
            "ds_read_b32 v92, v89 offset:%c[le2]\n\t"
            "ds_read_b32 v94, v89 offset:%c[le3]\n\t"
            "v_add_u32 v112, v127, v81\n\t"
            "v_lshl_add_u32 v124, v127, 1, v126\n\t"
            "v_lshrrev_b32 v113, 1, v112\n\t"
            "ds_read_b128 v[116:119], v112 offset:%c[au]\n\t"
            "ds_read_b64 v[120:121], v113\n\t"
            "s_and_b32 %[cnt], %[cnt], 0xff\n\t"
            "s_lshl_b32 %[t], %[nxt], 5\n\t"
            "v_add_u32 v127, %[t], v91\n\t"
            "ds_read_b64 v[122:123], v127\n\t"
            "s_mov_b64 exec, 1\n\t"
            "ds_add_rtn_u32 v125, v93, v95\n\t"
            "s_mov_b64 exec, -1\n\t"
            "s_waitcnt lgkmcnt(9)\n\t"
            "buffer_load_dwordx4 v[64:67], v[80:81], %[bv], 0 idxen offen\n\t"
            "buffer_load_dwordx4 v[68:71], v[82:83], %[bv], 0 idxen offen\n\t"
            "buffer_load_dwordx4 v[72:75], v[84:85], %[bv], 0 idxen offen\n\t"
            "v_mov_b64 v[104:105], 0\n\t"
            "v_mov_b64 v[106:107], 0\n\t"
            "v_mov_b64 v[108:109], 0\n\t"
            "v_mov_b64 v[110:111], 0\n\t"
            "s_waitcnt lgkmcnt(2)\n\t"
            "v_lshlrev_b32 v96, 16, v116\n\t"
            "v_lshlrev_b32 v97, 16, v117\n\t"
            "v_lshlrev_b32 v98, 16, v118\n\t"
            "v_lshlrev_b32 v99, 16, v119\n\t"
            "s_cmp_eq_u32 %[cnt], 0\n\t"
            "s_cbranch_scc1 L_fin%=\n\t"
            "L_loop%=:\n\t"
            STAGE("v64", "v65", "v66", "v67", "v[64:65]", "v[66:67]", "v80", "v[88:89]", "v88", "v[76:79]", "v[86:87]", "16", "%c[le4]")
            "s_cbranch_scc1 L_fin%=\n\t"
            STAGE("v68", "v69", "v70", "v71", "v[68:69]", "v[70:71]", "v82", "v[90:91]", "v90", "v[64:67]", "v[80:81]", "20", "%c[le5]")
            "s_cbranch_scc1 L_fin%=\n\t"
            STAGE("v72", "v73", "v74", "v75", "v[72:73]", "v[74:75]", "v84", "v[92:93]", "v92", "v[68:71]", "v[82:83]", "24", "%c[le6]")
            "s_cbranch_scc1 L_fin%=\n\t"
            STAGE("v76", "v77", "v78", "v79", "v[76:77]", "v[78:79]", "v86", "v[94:95]", "v94", "v[72:75]", "v[84:85]", "28", "%c[le7]")
            "v_add_u32 v89, 16, v89\n\t"
            "s_cbranch_scc0 L_loop%=\n\t"
            "L_fin%=:\n\t"
            "v_pk_add_f32 v[108:109], v[108:109], %[eps]\n\t"
            "v_pk_add_f32 v[110:111], v[110:111], %[eps]\n\t"
            "v_rcp_f32 v108, v108\n\t"
            "v_rcp_f32 v109, v109\n\t"
            "v_rcp_f32 v110, v110\n\t"
            "v_rcp_f32 v111, v111\n\t"
            "v_pk_mul_f32 v[104:105], v[104:105], v[108:109]\n\t"
            "v_pk_mul_f32 v[106:107], v[106:107], v[110:111]\n\t"
            "v_dot2c_f32_bf16 v104, %[shi], v116\n\t"
            "v_dot2c_f32_bf16 v105, %[shi], v117\n\t"
            "v_dot2c_f32_bf16 v106, %[shi], v118\n\t"
            "v_dot2c_f32_bf16 v107, %[shi], v119\n\t"
            "v_maximum3_f32 v104, v104, 0, 0\n\t"
            "v_maximum3_f32 v105, v105, 0, 0\n\t"
            "v_maximum3_f32 v106, v106, 0, 0\n\t"
            "v_maximum3_f32 v107, v107, 0, 0\n\t"
            "v_dot2c_f32_bf16 v104, %[slo], v120\n\t"
            "v_dot2c_f32_bf16 v105, %[shi], v120\n\t"
            "v_dot2c_f32_bf16 v106, %[slo], v121\n\t"
            "v_dot2c_f32_bf16 v107, %[shi], v121\n\t"
            "s_nop 1\n\t"
            "v_cvt_pk_bf16_f32 v112, v104, v105\n\t"
            "v_cvt_pk_bf16_f32 v113, v106, v107\n\t"
            "buffer_store_dwordx2 v[112:113], v124, %[o], 0 offen\n\t"
            PRIO_STEP
            "s_cmp_lt_u32 %[nxt], %[ntask]\n\t"
            "s_cbranch_scc1 L_task%=\n\t"
            "s_waitcnt vmcnt(0) lgkmcnt(0)"
            : [cnt] "=&s"(s_cnt), [nxt] "=&s"(s_nxt), [t] "=&s"(s_t), [tc] "=&s"(s_tc)
            : "{v81}"(joff), "{v83}"(joff), "{v85}"(joff), "{v87}"(joff), "{v91}"(g8), "{v93}"(ctr), "{v95}"(one), "{v126}"(j8),
              "{v[100:101]}"(w01), "{v[102:103]}"(w23),
              [task] "s"(w), [ntask] "s"(ntask), [bv] "s"(srdBV), [o] "s"(srdO), [slo] "s"(0x00003F80u), [shi] "s"(0x3F800000u), [eps] "s"(eps2),
              [au] "i"(K2_AU), [le0] "i"(K2_LSZ), [le1] "i"(K2_LSZ + 4), [le2] "i"(K2_LSZ + 8), [le3] "i"(K2_LSZ + 12),
              [le4] "i"(K2_LSZ + 16), [le5] "i"(K2_LSZ + 20), [le6] "i"(K2_LSZ + 24), [le7] "i"(K2_LSZ + 28)
            : "v64", "v65", "v66", "v67", "v68", "v69", "v70", "v71", "v72", "v73", "v74", "v75", "v76", "v77", "v78", "v79",
              "v80", "v82", "v84", "v86", "v88", "v89", "v90", "v92", "v94", "v96", "v97", "v98", "v99",
              "v104", "v105", "v106", "v107", "v108", "v109", "v110", "v111", "v112", "v113", "v114", "v115",
              "v116", "v117", "v118", "v119", "v120", "v121", "v122", "v123", "v124", "v125", "v127", "scc", "memory");
#undef STAGE
    }
    STAMP(10);
    STAMPC(15);
    SPAN1(1);
}

__global__ void k_meas(const char* ws) {
    const unsigned long long t_start = __builtin_amdgcn_s_memrealtime();
    const int lane = threadIdx.x;
    const unsigned long long* sp = (const unsigned long long*)(ws + (192u << 20));
    unsigned long long mn1 = ~0ull, mx1 = 0, mn2 = ~0ull, mx2 = 0;
    for (int i = lane; i < 256 * 16; i += 64) { const int b2 = i >> 4, w2 = i & 15;
        if (w2 < 8) { const unsigned long long a = sp[(b2 * 16 + w2) * 2], e = sp[(b2 * 16 + w2) * 2 + 1]; mn1 = a < mn1 ? a : mn1; mx1 = e > mx1 ? e : mx1; }
        const unsigned long long a = sp[((256 + b2) * 16 + w2) * 2], e = sp[((256 + b2) * 16 + w2) * 2 + 1]; mn2 = a < mn2 ? a : mn2; mx2 = e > mx2 ? e : mx2; }
    for (int o = 32; o; o >>= 1) { unsigned long long t;
        t = __shfl_xor(mn1, o); mn1 = t < mn1 ? t : mn1; t = __shfl_xor(mx1, o); mx1 = t > mx1 ? t : mx1;
        t = __shfl_xor(mn2, o); mn2 = t < mn2 ? t : mn2; t = __shfl_xor(mx2, o); mx2 = t > mx2 ? t : mx2; }
    unsigned long long val = 0;
    if (MEASURE == 1) val = mx1 - mn1;
    if (MEASURE == 2) val = mn2 - mx1;
    if (MEASURE == 3) val = mx2 - mn2;
    if (MEASURE == 4) val = t_start - mx2;
    const unsigned long long t0 = __builtin_amdgcn_s_memrealtime();
    while (__builtin_amdgcn_s_memrealtime() - t0 < val * 10ull) __builtin_amdgcn_s_sleep(2);
}
extern "C" void kernel_launch(void* const* d_in, const int* in_sizes, int n_in,
                              void* d_out, int out_size, void* d_ws, size_t ws_size,
                              hipStream_t stream) {
    const u16* x = (const u16*)d_in[0];
    const u16* ea = (const u16*)d_in[1];
    const int* src = (const int*)d_in[2];
    const int* dst = (const int*)d_in[3];
    const u16* wa = (const u16*)d_in[4];
    const u16* wb = (const u16*)d_in[5];
    const u16* wc = (const u16*)d_in[6];
    const u16* wu = (const u16*)d_in[7];
    const u16* wv = (const u16*)d_in[8];
    (void)hipFuncSetAttribute((const void*)k1_gemm_bin, hipFuncAttributeMaxDynamicSharedMemorySize, K1_LDS);
    k1_gemm_bin<<<256, 512, K1_LDS, stream>>>(x, wa, wb, wu, wv, src, dst, ea, (char*)d_ws);
    (void)hipFuncSetAttribute((const void*)k2_edges, hipFuncAttributeMaxDynamicSharedMemorySize, K2_LDS);
    k2_edges<<<256, 1024, K2_LDS, stream>>>(x, wc, src, dst, ea, (const char*)d_ws, (u16*)d_out);
    k_meas<<<1, 64, 0, stream>>>((const char*)d_ws);
}
```

```cpp
#include <hip/hip_runtime.h>
#include <hip/hip_bf16.h>
#include <stdint.h>

typedef unsigned short u16;
typedef unsigned int u32;
typedef u32 u32x2 __attribute__((ext_vector_type(2)));
typedef u32 u32x4 __attribute__((ext_vector_type(4)));
typedef float f32x2 __attribute__((ext_vector_type(2)));
typedef float f32x4 __attribute__((ext_vector_type(4)));
typedef float f32x16 __attribute__((ext_vector_type(16)));
typedef short s16x4 __attribute__((ext_vector_type(4)));
typedef short s16x8 __attribute__((ext_vector_type(8)));
typedef __bf16 bf16x8 __attribute__((ext_vector_type(8)));
typedef __bf16 bf16x4 __attribute__((ext_vector_type(4)));
typedef __bf16 bf16x2 __attribute__((ext_vector_type(2)));

constexpr int NN = 10000;
constexpr int NE = 160000;
constexpr int DD = 256;
constexpr int NP = 10048;
constexpr int RG = 320;
constexpr int NBK = 64;
constexpr int NB = 157;
constexpr int NCH = 64;
constexpr int CHE = 2500;
constexpr int CAP = 80;
constexpr int CAPN = 44;
constexpr int LST = 45;
constexpr float NLOG2E = -1.4426950408889634f;
constexpr u32 NEUTRAL_REC = (u32)NN;
constexpr u32 NEUTRAL_BV = 0x00007F00u;

constexpr size_t OFF_BV = 0;
constexpr size_t OFF_AU = OFF_BV + (size_t)4 * NP * 256;
constexpr size_t OFF_R4 = OFF_AU + (size_t)4 * NP * 256;
constexpr size_t OFF_R2 = OFF_R4 + (size_t)NBK * NCH * CAP * 4;
constexpr size_t OFF_CNT = OFF_R2 + (size_t)NBK * NCH * CAP * 2;

#define LDS_AS __attribute__((address_space(3)))
#define GLB_AS __attribute__((address_space(1)))

__device__ __forceinline__ void glds16(const void* g, void* l) {
    __builtin_amdgcn_global_load_lds((const GLB_AS u32*)g, (LDS_AS u32*)l, 16, 0, 0);
}
__device__ __forceinline__ void glds16a(const void* sbase, u32 voff, u32 lds) {
    u32 keep;
    asm volatile("s_mov_b32 %0, m0\n\ts_mov_b32 m0, %3\n\ts_nop 0\n\tglobal_load_lds_dwordx4 %1, %2\n\ts_mov_b32 m0, %0" : "=&s"(keep) : "v"(voff), "s"(sbase), "s"(lds) : "memory");
}
__device__ __forceinline__ u32 cvt_pk_bf16(float lo, float hi) {
    const f32x2 v = {lo, hi}; return __builtin_bit_cast(u32, __builtin_convertvector(v, bf16x2));
}
#define WAIT_V(n) asm volatile("s_waitcnt vmcnt(" #n ")" ::: "memory")
#define WAIT_L(n) asm volatile("s_waitcnt lgkmcnt(" #n ")" ::: "memory")
#define BAR() __builtin_amdgcn_s_barrier()
#define EXIT2_AT 99
#ifndef EXIT_AT
#define EXIT_AT 99
#endif
#define STORE_AUX 16
#define SPAN0()
#define SPAN1(K)
#define STAMP(i)
#define STAMPC(i)
#define TICK(v)

constexpr int K1_R0 = 0, K1_R1 = 65536, K1_SCR = 131072;
constexpr int K1_ST4 = K1_SCR, K1_ST2 = K1_ST4 + NBK * CAP * 4, K1_BC = K1_ST2 + NBK * CAP * 2;
constexpr int K1_LDS = K1_SCR + 32768;
static_assert(K1_BC + NBK * 4 <= K1_LDS, "lds");

__global__ __launch_bounds__(512, 2) void k1_gemm_bin(
    const u16* __restrict__ x, const u16* __restrict__ wa, const u16* __restrict__ wb, const u16* __restrict__ wu, const u16* __restrict__ wv,
    const int* __restrict__ src, const int* __restrict__ dst, const u16* __restrict__ ea, char* __restrict__ ws) {
    extern __shared__ __attribute__((aligned(16))) char smem[];
    const int tid = threadIdx.x, lane = tid & 63;
    const int w = __builtin_amdgcn_readfirstlane(tid >> 6);
    const int b = blockIdx.x;
    const int type = (b >> 5) & 1;
    const int ct2 = (b >> 3) & 3;
    const int rg = (b & 7) * 4 + (b >> 6);
    const int row0 = rg * RG;
    const int chunk = (b & 31) | ((b >> 6) << 5) ;
    const bool binner = type == 1 && b < 128;
    constexpr int KOFF = 0;
    SPAN0();
#if EXIT_AT == 0
    if (ws) { SPAN1(0); return; }
#endif
    STAMP(0);
    int bd[5], bs[5]; u32 be[5];
    if (binner) {
#pragma unroll
        for (int i = 0; i < 5; ++i) {
            const int k = tid + 512 * i;
            const int e = chunk * CHE + (k < CHE ? k : 0);
            bd[i] = dst[e]; bs[i] = src[e]; be[i] = ea[e];
        }
        if (tid < NBK) *(u32*)(smem + K1_BC + tid * 4) = 0;
    }
    {
        const int m = w >> 2;
        const u16* W = type == 0 ? (m == 0 ? wb : wv) : (m == 0 ? wa : wu);
        const int kb0 = (w & 3) * 64, kr = lane >> 3;
        const u16* g = W + (size_t)(kb0 + kr) * DD + ct2 * 64 + (((lane & 7) ^ ((kr & 2) << 1)) << 3);
#pragma unroll
        for (int i = 0; i < 8; ++i) glds16(g + (size_t)i * 8 * DD, smem + K1_R0 + m * 32768 + (kb0 / 8 + i) * 1024);
    }
    const int xr = lane >> 5, xp = lane & 31;
    auto stage_x = [&](int tile, int q, int ldsbase) {
        const int r = q * 2 + xr;
        int grow = row0 + tile * 32 + r; grow = grow < NN ? grow : NN - 1;
        glds16((const char*)x + (u32)(grow * 512 + ((xp ^ (r & 15)) << 4)), smem + ldsbase + q * 1024);
    };
#pragma unroll
    for (int i = 0; i < 8; ++i) { const int q = w * 8 + i; stage_x(q >> 4, q & 15, K1_R1 + (q >> 4) * 16384); }
    STAMP(1);
    WAIT_V(8); WAIT_L(0);
    BAR();
    STAMP(2);
#if EXIT_AT == 1
    if (ws) { SPAN1(0); return; }
#endif
    const int ch = w >> 2;
    bf16x8 wf0[16], wf1[16];
    {
        const int h = lane >> 5, blk = (lane >> 4) & 1, q = (lane & 15) >> 2, p = lane & 3;
        const u32 base = K1_R0 + (8 * h + q) * 128 + (((ch * 4 + blk * 2 + (p >> 1)) ^ ((q & 2) << 1)) << 4) + (p & 1) * 8;
#define TRRD(S) { u32x2 a0, a1, b0, b1; \
            asm volatile("ds_read_b64_tr_b16 %0, %4 offset:%c5\n\tds_read_b64_tr_b16 %1, %4 offset:%c6\n\t" \
                         "ds_read_b64_tr_b16 %2, %4 offset:%c7\n\tds_read_b64_tr_b16 %3, %4 offset:%c8" \
                         : "=&v"(a0), "=&v"(a1), "=&v"(b0), "=&v"(b1) \
                         : "v"(base), "i"((S) * 2048), "i"((S) * 2048 + 512), "i"(32768 + (S) * 2048), "i"(32768 + (S) * 2048 + 512) : "memory"); \
            wf0[S] = __builtin_bit_cast(bf16x8, __builtin_shufflevector(a0, a1, 0, 1, 2, 3)); \
            wf1[S] = __builtin_bit_cast(bf16x8, __builtin_shufflevector(b0, b1, 0, 1, 2, 3)); }
        TRRD(0) TRRD(1) TRRD(2) TRRD(3) TRRD(4) TRRD(5) TRRD(6) TRRD(7) TRRD(8) TRRD(9) TRRD(10) TRRD(11) TRRD(12) TRRD(13) TRRD(14) TRRD(15)
#undef TRRD
    }
    if (binner) {
#pragma unroll
        for (int i = 0; i < 5; ++i) {
            const int k = tid + 512 * i;
            if (k < CHE) {
                const int bk = (int)(((u32)bd[i] * 53432u) >> 23);
                const int dl = bd[i] - bk * NB;
                const u32 pos = atomicAdd((u32*)(smem + K1_BC + bk * 4), 1u);
                if (pos < CAP) {
                    *(u32*)(smem + K1_ST4 + (bk * CAP + pos) * 4) = (u32)bs[i] | (be[i] << 16);
                    *(u16*)(smem + K1_ST2 + (bk * CAP + pos) * 2) = (u16)dl;
                }
            }
        }
    }
    STAMP(3);
    WAIT_V(0); WAIT_L(0);
    __builtin_amdgcn_sched_barrier(0);
    BAR();
    STAMP(4);
#if EXIT_AT == 2
    if (ws) { asm volatile("" :: "v"(wf0[0]), "v"(wf1[15])); SPAN1(0); return; }
#endif
#pragma unroll
    for (int i = 0; i < 8; ++i) { const int q = w * 8 + i; stage_x(4 + (q >> 4), q & 15, K1_R0 + (q >> 4) * 16384); }
    if (binner) {
        u32* R4 = (u32*)(ws + OFF_R4); u16* R2 = (u16*)(ws + OFF_R2); u32* CNT = (u32*)(ws + OFF_CNT);
        u32x4 t4[3]; u32x2 t2[3];
#pragma unroll
        for (int i = 0; i < 3; ++i) { const int q = tid + 512 * i; const int qq = q < NBK * (CAP / 4) ? q : 0;
            t4[i] = *(const u32x4*)(smem + K1_ST4 + qq * 16); t2[i] = *(const u32x2*)(smem + K1_ST2 + qq * 8); }
        const u32 cv = *(const u32*)(smem + K1_BC + (tid & 63) * 4);
#pragma unroll
        for (int i = 0; i < 3; ++i) { const int q = tid + 512 * i;
            if (q < NBK * (CAP / 4)) { const int bk = q / (CAP / 4), o = q - bk * (CAP / 4);
                *(u32x4*)(R4 + ((size_t)bk * NCH + chunk) * CAP + o * 4) = t4[i];
                *(u32x2*)(R2 + ((size_t)bk * NCH + chunk) * CAP + o * 4) = t2[i]; } }
        if (tid < NBK) CNT[tid * NCH + chunk] = cv;
        WAIT_L(0);
        BAR();
    }
    if (rg == 0 && type == 0 && w == 0) ((u32*)(ws + OFF_BV))[((size_t)ct2 * NP + NN) * 64 + lane] = NEUTRAL_BV;

    const int r = lane & 31, h = lane >> 5;
    const int abase = r * 512 + ((((r & 14) | ((h ^ r) & 1))) << 4);
    char* const scr = smem + K1_SCR + w * 4096;
    const __amdgpu_buffer_rsrc_t trs = __builtin_amdgcn_make_buffer_rsrc(ws + (type ? OFF_AU : OFF_BV) + (size_t)ct2 * NP * 256, 0, NN * 256, 0x00020000);
    const u32 svo = (lane >> 3) * 256 + ch * 128 + (lane & 7) * 16;

    const int s4 = w & 3;
    if (ch == 0) __builtin_amdgcn_s_setprio(1);
    STAMP(5);
#pragma unroll 1
    for (int st = 0; st < (EXIT_AT == 3 ? 1 : (EXIT_AT == 4 ? 2 : 3)); ++st) {
        int tile, ldsbase;
        if (st == 0) { tile = s4; ldsbase = K1_R1 + s4 * 16384; }
        else if (st == 1) { tile = 4 + s4; ldsbase = K1_R0 + s4 * 16384; }
        else { const int t = (w < 2) ? w : (w >= 6 ? w - 6 : 99); tile = 8 + t; ldsbase = K1_R1 + (t & 1) * 16384; }
        const int trow = row0 + tile * 32;
        const bool active = tile < 10 && trow < NN;
        unsigned long long tkA = 0, tkB = 0, tkC = 0, tkD = 0;
        if (active) {
            TICK(tkA);
            bf16x8 a[16];
#pragma unroll
            for (int s = 0; s < 8; ++s) a[s] = *(const bf16x8*)(smem + ldsbase + (abase ^ (s << 5)));
            __builtin_amdgcn_sched_barrier(0);
            f32x16 acc0, acc1;
#pragma unroll
            for (int i = 0; i < 16; ++i) { acc0[i] = 0.f; acc1[i] = 0.f; }
#pragma unroll
            for (int s = 0; s < 8; ++s) {
                a[s + 8] = *(const bf16x8*)(smem + ldsbase + (abase ^ (s << 5)) + 256);
                acc0 = __builtin_amdgcn_mfma_f32_32x32x16_bf16(a[s], wf0[s], acc0, 0, 0, 0);
                acc1 = __builtin_amdgcn_mfma_f32_32x32x16_bf16(a[s], wf1[s], acc1, 0, 0, 0);
                __builtin_amdgcn_sched_group_barrier(0x100, 1, 0);
                __builtin_amdgcn_sched_group_barrier(0x8, 2, 0);
            }
            __builtin_amdgcn_sched_barrier(0);
#pragma unroll
            for (int s = 8; s < 16; ++s) {
                acc0 = __builtin_amdgcn_mfma_f32_32x32x16_bf16(a[s], wf0[s], acc0, 0, 0, 0);
                acc1 = __builtin_amdgcn_mfma_f32_32x32x16_bf16(a[s], wf1[s], acc1, 0, 0, 0);
            }
            TICK(tkB);
            char* const sw = scr + h * 512 + r * 4;
            if (type == 0) {
#pragma unroll
                for (int i = 0; i < 16; i += 2) {
                    const u32 bb = cvt_pk_bf16(acc0[i] * NLOG2E, acc0[i + 1] * NLOG2E);
#pragma unroll
                    for (int j = 0; j < 2; ++j) {
                        const u32 vb = __float_as_uint(acc1[i + j]);
                        const u32 bl = j ? (bb >> 16) : (bb & 0xFFFFu);
                        const u32 u = __builtin_elementwise_sub_sat((vb & 0x7FFFFFFFu) + 0x8000u, bl);
                        *(u32*)(sw + (((i + j) & 3) + 8 * ((i + j) >> 2)) * 128) = (vb & 0x80000000u) | (u & 0x7FFF0000u) | bl;
                    }
                }
            } else {
#pragma unroll
                for (int i = 0; i < 16; ++i) *(u32*)(sw + ((i & 3) + 8 * (i >> 2)) * 128) = cvt_pk_bf16(acc0[i] * NLOG2E, acc1[i]);
            }
            TICK(tkC);
#pragma unroll
            for (int i = 0; i < 4; ++i) {
                const u32x4 v = *(const u32x4*)(scr + i * 1024 + lane * 16);
                __builtin_amdgcn_raw_buffer_store_b128(v, trs, svo + (u32)(trow + i * 8) * 256, 0, STORE_AUX);
            }
        }
        TICK(tkD);
        if (st == 0) STAMP(6); else if (st == 1) STAMP(8); else STAMP(10);
        if (st < 2) {
            if (active) WAIT_V(4); else WAIT_V(0);
            BAR();
            if (st == 0) {
#pragma unroll
                for (int i = 0; i < 4; ++i) { const int q = w * 4 + i; stage_x(8 + (q >> 4), q & 15, K1_R1 + (q >> 4) * 16384); }
            }
        }
    }
    SPAN1(0);
}

constexpr int LSTB = LST * 4;
constexpr int K2_X = 0;
constexpr int K2_AU = K2_X + 20480;
constexpr int K2_LIDX = K2_AU + 40960;
constexpr int K2_LSZ = (NB * LSTB + 32 + 256 + 15) / 16 * 16;
constexpr int K2_LE = K2_LIDX + K2_LSZ;
constexpr int K2_CNT = K2_LE + K2_LSZ;
constexpr int K2_HIST = K2_CNT + 160 * 4;
constexpr int K2_ORD = K2_HIST + 128 * 4;
constexpr int K2_MISC = K2_ORD + 80 * 32;
constexpr int K2_LDS = K2_MISC + 16;
constexpr int K2_TRASH = NB * LSTB + 32;
static_assert(K2_TRASH + 256 <= K2_LSZ, "trash");
static_assert(K2_LSZ < 65536 - 64, "ds offset");

__device__ __forceinline__ float dot2sel(u32 d, u32 sel, float c) {
    return __builtin_amdgcn_fdot2_f32_bf16(__builtin_bit_cast(bf16x2, d), __builtin_bit_cast(bf16x2, sel), c, false);
}

struct Acc { f32x2 n01, n23, d01, d23; };

__device__ __forceinline__ void edge_step(Acc& a, const u32x4 d, const u32 rec, const f32x2 a01, const f32x2 a23, const f32x2 w01, const f32x2 w23, const u32 sel) {
    const float e = __uint_as_float(rec & 0xFFFF0000u);
    const f32x2 ee = {e, e};
    const f32x2 t01 = __builtin_elementwise_fma(ee, w01, a01);
    const f32x2 t23 = __builtin_elementwise_fma(ee, w23, a23);
    f32x2 E01, E23;
    E01.x = __builtin_amdgcn_exp2f(dot2sel(d.x, sel, t01.x));
    E01.y = __builtin_amdgcn_exp2f(dot2sel(d.y, sel, t01.y));
    E23.x = __builtin_amdgcn_exp2f(dot2sel(d.z, sel, t23.x));
    E23.y = __builtin_amdgcn_exp2f(dot2sel(d.w, sel, t23.y));
    const f32x2 one = {1.f, 1.f};
    E01 += one; E23 += one;
    f32x2 s01, s23;
    s01.x = __builtin_amdgcn_rcpf(E01.x); s01.y = __builtin_amdgcn_rcpf(E01.y);
    s23.x = __builtin_amdgcn_rcpf(E23.x); s23.y = __builtin_amdgcn_rcpf(E23.y);
    a.d01 += s01; a.d23 += s23;
    const f32x2 v01 = {__uint_as_float(d.x), __uint_as_float(d.y)};
    const f32x2 v23 = {__uint_as_float(d.z), __uint_as_float(d.w)};
    a.n01 = __builtin_elementwise_fma(s01, v01, a.n01);
    a.n23 = __builtin_elementwise_fma(s23, v23, a.n23);
}

__device__ __forceinline__ void node_finish(const Acc& a, const u32x4 au, const u32x2 xv, u16* outp) {
    const float num[4] = {a.n01.x, a.n01.y, a.n23.x, a.n23.y};
    const float den[4] = {a.d01.x, a.d01.y, a.d23.x, a.d23.y};
    const u32 aus[4] = {au.x, au.y, au.z, au.w};
    const float xs[4] = {__uint_as_float(xv.x << 16), __uint_as_float(xv.x & 0xFFFF0000u), __uint_as_float(xv.y << 16), __uint_as_float(xv.y & 0xFFFF0000u)};
    float o[4];
#pragma unroll
    for (int c = 0; c < 4; ++c) {
        const float z = __uint_as_float(aus[c] & 0xFFFF0000u) + num[c] * __builtin_amdgcn_rcpf(den[c] + 1e-6f);
        const float rl = z > 0.f ? z : 0.f * z;
        o[c] = xs[c] + rl;
    }
    u32x2 ov; ov.x = cvt_pk_bf16(o[0], o[1]); ov.y = cvt_pk_bf16(o[2], o[3]);
    *(u32x2*)outp = ov;
}

template <int CTRL, int RM> __device__ __forceinline__ u32 dpp_add(u32 v) {
    return v + (u32)__builtin_amdgcn_update_dpp(0, (int)v, CTRL, RM, 0xf, false);
}

__global__ __launch_bounds__(1024, 4) void k2_edges(
    const u16* __restrict__ x, const u16* __restrict__ wc, const int* __restrict__ src, const int* __restrict__ dst, const u16* __restrict__ ea,
    const char* __restrict__ ws, u16* __restrict__ out) {
    extern __shared__ __attribute__((aligned(16))) char smem[];
    const int tid = threadIdx.x, lane = tid & 63;
    const int w = __builtin_amdgcn_readfirstlane(tid >> 6);
    const int b = blockIdx.x;
    const int slice = (b & 7) >> 1;
    const int bucket = (b >> 3) * 2 + (b & 1);
    const int node0 = bucket * NB;
    const int nvalid = (NN - node0) < NB ? (NN - node0) : NB;
    u32* const cnt = (u32*)(smem + K2_CNT);
    u32* const hist = (u32*)(smem + K2_HIST);
    u32* const misc = (u32*)(smem + K2_MISC);
    constexpr int KOFF = 256 * 16;
    SPAN0();
    STAMP(0);

    const u32* R4 = (const u32*)(ws + OFF_R4) + (size_t)bucket * NCH * CAP;
    const u16* R2 = (const u16*)(ws + OFF_R2) + (size_t)bucket * NCH * CAP;
    const u32* CNT = (const u32*)(ws + OFF_CNT) + bucket * NCH;
    const int ch0 = tid / (CAP / 4), ch1 = (tid + 1024) / (CAP / 4);
    const u32x4 r40 = *(const u32x4*)(R4 + tid * 4);
    const u32x2 r20 = *(const u32x2*)(R2 + tid * 4);
    const u32 c0 = CNT[ch0];
    u32x4 r41 = {0, 0, 0, 0}; u32x2 r21 = {0, 0}; u32 c1 = 0;
    if (w < 4) { r41 = *(const u32x4*)(R4 + (tid + 1024) * 4); r21 = *(const u32x2*)(R2 + (tid + 1024) * 4); c1 = CNT[ch1]; }
    const char* const bvb = ws + OFF_BV + (size_t)slice * NP * 256;
    const char* const aub = ws + OFF_AU + (size_t)slice * NP * 256;
    const int g = lane >> 4, j = lane & 15;
    const u32x2 wcv = *(const u32x2*)(wc + slice * 64 + j * 4);
    for (int i = tid; i < K2_LSZ / 16; i += 1024) {
        *(u32x4*)(smem + K2_LIDX + i * 16) = (u32x4){NEUTRAL_REC, NEUTRAL_REC, NEUTRAL_REC, NEUTRAL_REC};
        *(u32x4*)(smem + K2_LE + i * 16) = (u32x4){0, 0, 0, 0};
    }
    if (tid < 160 + 64) cnt[tid] = 0;
    if (tid == 0) { misc[0] = 16; misc[1] = 0; }
    f32x2 w01 = {__uint_as_float(wcv.x << 16) * NLOG2E, __uint_as_float(wcv.x & 0xFFFF0000u) * NLOG2E};
    f32x2 w23 = {__uint_as_float(wcv.y << 16) * NLOG2E, __uint_as_float(wcv.y & 0xFFFF0000u) * NLOG2E};
    asm volatile("" : "+v"(w01), "+v"(w23));
    STAMP(1);
    WAIT_V(0); WAIT_L(0); BAR();
    {
        const char* ga = aub + (size_t)node0 * 256;
#pragma unroll
        for (int i = 0; i < 3; ++i) { const int q = w + 16 * i; if (q < 40) glds16a(ga, q * 1024 + lane * 16, K2_AU + q * 1024); }
        const char* gx = (const char*)x + slice * 128;
#pragma unroll
        for (int i = 0; i < 2; ++i) {
            const int q = w + 16 * i;
            if (q < 20) { int row = node0 + q * 8 + (lane >> 3); row = row < NN ? row : NN - 1;
                glds16a(gx, row * 512 + (lane & 7) * 16, K2_X + q * 1024); }
        }
    }
    STAMP(2);
    {
        const int p0 = (tid - ch0 * (CAP / 4)) * 4;
        const u32 rec0[4] = {r40.x, r40.y, r40.z, r40.w};
        const u32 dl0[4] = {r20.x & 0xFFFF, r20.x >> 16, r20.y & 0xFFFF, r20.y >> 16};
        u32 slot[4];
        bool ovf = c0 > CAP;
#pragma unroll
        for (int k = 0; k < 4; ++k) slot[k] = atomicAdd(&cnt[p0 + k < c0 ? dl0[k] : 224 + lane], 1u);
        if (w < 4) {
            const int p1 = (tid + 1024 - ch1 * (CAP / 4)) * 4;
            const u32 rec1[4] = {r41.x, r41.y, r41.z, r41.w};
            const u32 dl1[4] = {r21.x & 0xFFFF, r21.x >> 16, r21.y & 0xFFFF, r21.y >> 16};
            u32 slot1[4];
            ovf |= c1 > CAP;
#pragma unroll
            for (int k = 0; k < 4; ++k) slot1[k] = atomicAdd(&cnt[p1 + k < c1 ? dl1[k] : 224 + lane], 1u);
#pragma unroll
            for (int k = 0; k < 4; ++k) {
                const bool v = p1 + k < c1, ok = v && slot1[k] < CAPN;
                ovf |= v && !ok;
                const u32 o = ok ? dl1[k] * LSTB + slot1[k] * 4 : K2_TRASH + lane * 4;
                *(u32*)(smem + K2_LIDX + o) = rec1[k] & 0xFFFFu;
                *(u32*)(smem + K2_LE + o) = rec1[k] & 0xFFFF0000u;
            }
        }
#pragma unroll
        for (int k = 0; k < 4; ++k) {
            const bool v = p0 + k < c0, ok = v && slot[k] < CAPN;
            ovf |= v && !ok;
            const u32 o = ok ? dl0[k] * LSTB + slot[k] * 4 : K2_TRASH + lane * 4;
            *(u32*)(smem + K2_LIDX + o) = rec0[k] & 0xFFFFu;
            *(u32*)(smem + K2_LE + o) = rec0[k] & 0xFFFF0000u;
        }
        if (ovf) misc[1] = 1;
    }
    STAMP(3);
    WAIT_L(0); BAR();
    STAMP(4);
    const bool slow = misc[1] != 0;

    const u32 joff = j * 16;

    if (slow) {
        u32 sel = 0x00003F80u;
        asm volatile("" : "+s"(sel));
        for (int task = w; task * 4 < nvalid; task += 16) {
            const int n = task * 4 + g;
            const bool nv = n < nvalid;
            const int node = node0 + (nv ? n : 0);
            const u32x4 au = *(const u32x4*)(aub + (size_t)node * 256 + joff);
            const u32x2 xv = *(const u32x2*)(x + (size_t)node * DD + slice * 64 + j * 4);
            const f32x2 a01 = {__uint_as_float(au.x << 16), __uint_as_float(au.y << 16)};
            const f32x2 a23 = {__uint_as_float(au.z << 16), __uint_as_float(au.w << 16)};
            Acc acc = {{0.f, 0.f}, {0.f, 0.f}, {0.f, 0.f}, {0.f, 0.f}};
            const int lo = node0 + task * 4;
            for (int e0 = 0; e0 < NE; e0 += 64) {
                const int d = dst[e0 + lane];
                unsigned long long m = __ballot((u32)(d - lo) < 4u);
                if (m) {
                    const u32 rc = (u32)src[e0 + lane] | ((u32)ea[e0 + lane] << 16);
                    while (m) {
                        const int l = __builtin_ctzll(m); m &= m - 1;
                        const int dd = __builtin_amdgcn_readlane(d, l);
                        const u32 rr = __builtin_amdgcn_readlane(rc, l);
                        const u32 rec = (nv && dd == node) ? rr : NEUTRAL_REC;
                        const u32x4 dv = *(const u32x4*)(bvb + (size_t)((rec & 0xFFFFu) * 256 + joff));
                        edge_step(acc, dv, rec, a01, a23, w01, w23, sel);
                    }
                }
            }
            if (nv) node_finish(acc, au, xv, out + (size_t)node * DD + slice * 64 + j * 4);
        }
        return;
    }

    u32 mydeg = 0, myrank = 0;
    if (tid < nvalid) { mydeg = cnt[tid]; myrank = atomicAdd(&hist[63 - mydeg], 1u); }
    WAIT_L(0); BAR();
    if (w < 3) {
        const u32 h0 = hist[lane];
        u32 v = h0;
        v = dpp_add<0x111, 0xf>(v); v = dpp_add<0x112, 0xf>(v); v = dpp_add<0x114, 0xf>(v); v = dpp_add<0x118, 0xf>(v);
        v = dpp_add<0x142, 0xa>(v); v = dpp_add<0x143, 0xc>(v);
        const u32 base = (u32)__builtin_amdgcn_ds_bpermute((int)((63 - mydeg) * 4), (int)(v - h0));
        if (tid < nvalid) {
            const u32 pos = base + myrank;
            const u32x2 ent = {(u32)(K2_LIDX + tid * LSTB), (u32)(tid * 256) + mydeg};
            u32x2* o = (u32x2*)(smem + K2_ORD) + pos;
            o[0] = ent;
            if (pos == (u32)nvalid - 1) { o[1] = ent; o[2] = ent; o[3] = ent; }
        }
    }
    WAIT_V(0); WAIT_L(0); BAR();
    STAMP(5);
    STAMPC(14);

    const int ntask = (nvalid + 3) >> 2;
    if (w < ntask) {
        u32x4 srdBV, srdO;
        { const size_t p = (size_t)bvb; srdBV = (u32x4){(u32)p, (u32)(p >> 32) | (256u << 16), 0xFFFFFFFFu, 0x00020000u}; }
        { const size_t ob = (size_t)node0 * 512 + slice * 128; const size_t p = (size_t)out + ob; srdO = (u32x4){(u32)p, (u32)(p >> 32), (u32)((size_t)NN * 512 - ob), 0x00020000u}; }
        u32 g8 = K2_ORD + g * 8, ctr = K2_MISC, one = 1, j8 = j * 8;
        unsigned long long eps2 = 0x358637BD358637BDull;
        u32 s_cnt, s_nxt, s_t, s_tc;
#define STAGE(DS0, DS1, DS2, DS3, DSL, DSH, IX, EP, E1, DN, IXN, O1, O2) \
            "s_waitcnt lgkmcnt(0)\n\t" \
            "buffer_load_dwordx4 " DN ", " GIX(IXN) ", %[bv], 0 idxen offen\n\t" \
            "v_pk_fma_f32 v[112:113], " EP ", v[100:101], v[96:97] op_sel_hi:[0,1,1]\n\t" \
            "v_pk_fma_f32 v[114:115], " EP ", v[102:103], v[98:99] op_sel_hi:[0,1,1]\n\t" \
            "ds_read_b32 " IX ", v89 offset:" O1 "\n\t" \
            "ds_read_b32 " E1 ", v89 offset:" O2 "\n\t" \
            "s_waitcnt vmcnt(3)\n\t" \
            "v_dot2c_f32_bf16 v112, %[slo], " DS0 "\n\t" \
            "v_dot2c_f32_bf16 v113, %[slo], " DS1 "\n\t" \
            "v_dot2c_f32_bf16 v114, %[slo], " DS2 "\n\t" \
            "v_dot2c_f32_bf16 v115, %[slo], " DS3 "\n\t" \
            "v_exp_f32 v112, v112\n\t" \
            "v_exp_f32 v113, v113\n\t" \
            "v_exp_f32 v114, v114\n\t" \
            "v_exp_f32 v115, v115\n\t" \
            "v_pk_add_f32 v[112:113], v[112:113], 1.0 op_sel_hi:[1,0]\n\t" \
            "v_pk_add_f32 v[114:115], v[114:115], 1.0 op_sel_hi:[1,0]\n\t" \
            "v_rcp_f32 v112, v112\n\t" \
            "v_rcp_f32 v113, v113\n\t" \
            "v_rcp_f32 v114, v114\n\t" \
            "v_rcp_f32 v115, v115\n\t" \
            "s_sub_u32 %[cnt], %[cnt], 1\n\t" \
            "v_pk_add_f32 v[108:109], v[108:109], v[112:113]\n\t" \
            "v_pk_fma_f32 v[104:105], v[112:113], " DSL ", v[104:105]\n\t" \
            "v_pk_add_f32 v[110:111], v[110:111], v[114:115]\n\t" \
            "v_pk_fma_f32 v[106:107], v[114:115], " DSH ", v[106:107]\n\t" \
            "s_cmp_eq_u32 %[cnt], 0\n\t"
#define GIX(x) x
#define PRIO_ON 1
#define PRIO_INIT "s_setprio 3\n\t"
#define PRIO_STEP \
            "s_add_u32 %[tc], %[tc], 1\n\t" \
            "s_cmp_lg_u32 %[tc], 1\n\t s_cbranch_scc1 L_p1%=\n\t s_setprio 2\n\t L_p1%=:\n\t" \
            "s_cmp_lg_u32 %[tc], 2\n\t s_cbranch_scc1 L_p2%=\n\t s_setprio 1\n\t L_p2%=:\n\t" \
            "s_cmp_lg_u32 %[tc], 3\n\t s_cbranch_scc1 L_p3%=\n\t s_setprio 0\n\t L_p3%=:\n\t"
        asm volatile(
            PRIO_INIT
            "s_mov_b32 %[tc], 0\n\t"
            "s_lshl_b32 %[t], %[task], 5\n\t"
            "v_add_u32 v127, %[t], v91\n\t"
            "ds_read_b64 v[122:123], v127\n\t"
            "s_mov_b64 exec, 1\n\t"
            "ds_add_rtn_u32 v125, v93, v95\n\t"
            "s_mov_b64 exec, -1\n\t"
            "L_task%=:\n\t"
            "s_waitcnt lgkmcnt(0)\n\t"
            "v_readfirstlane_b32 %[nxt], v125\n\t"
            "v_readfirstlane_b32 %[cnt], v123\n\t"
            "v_mov_b32 v89, v122\n\t"
            "v_and_b32 v127, 0xffffff00, v123\n\t"
            "ds_read_b32 v80, v89\n\t"
            "ds_read_b32 v82, v89 offset:4\n\t"
            "ds_read_b32 v84, v89 offset:8\n\t"
            "ds_read_b32 v86, v89 offset:12\n\t"
            "ds_read_b32 v88, v89 offset:%c[le0]\n\t"
            "ds_read_b32 v90, v89 offset:%c[le1]\n\t"
            "ds_read_b32 v92, v89 offset:%c[le2]\n\t"
            "ds_read_b32 v94, v89 offset:%c[le3]\n\t"
            "v_add_u32 v112, v127, v81\n\t"
            "v_lshl_add_u32 v124, v127, 1, v126\n\t"
            "v_lshrrev_b32 v113, 1, v112\n\t"
            "ds_read_b128 v[116:119], v112 offset:%c[au]\n\t"
            "ds_read_b64 v[120:121], v113\n\t"
            "s_and_b32 %[cnt], %[cnt], 0xff\n\t"
            "s_lshl_b32 %[t], %[nxt], 5\n\t"
            "v_add_u32 v127, %[t], v91\n\t"
            "ds_read_b64 v[122:123], v127\n\t"
            "s_mov_b64 exec, 1\n\t"
            "ds_add_rtn_u32 v125, v93, v95\n\t"
            "s_mov_b64 exec, -1\n\t"
            "s_waitcnt lgkmcnt(9)\n\t"
            "buffer_load_dwordx4 v[64:67], v[80:81], %[bv], 0 idxen offen\n\t"
            "buffer_load_dwordx4 v[68:71], v[82:83], %[bv], 0 idxen offen\n\t"
            "buffer_load_dwordx4 v[72:75], v[84:85], %[bv], 0 idxen offen\n\t"
            "v_mov_b64 v[104:105], 0\n\t"
            "v_mov_b64 v[106:107], 0\n\t"
            "v_mov_b64 v[108:109], 0\n\t"
            "v_mov_b64 v[110:111], 0\n\t"
            "s_waitcnt lgkmcnt(2)\n\t"
            "v_lshlrev_b32 v96, 16, v116\n\t"
            "v_lshlrev_b32 v97, 16, v117\n\t"
            "v_lshlrev_b32 v98, 16, v118\n\t"
            "v_lshlrev_b32 v99, 16, v119\n\t"
            "s_cmp_eq_u32 %[cnt], 0\n\t"
            "s_cbranch_scc1 L_fin%=\n\t"
            "L_loop%=:\n\t"
            STAGE("v64", "v65", "v66", "v67", "v[64:65]", "v[66:67]", "v80", "v[88:89]", "v88", "v[76:79]", "v[86:87]", "16", "%c[le4]")
            "s_cbranch_scc1 L_fin%=\n\t"
            STAGE("v68", "v69", "v70", "v71", "v[68:69]", "v[70:71]", "v82", "v[90:91]", "v90", "v[64:67]", "v[80:81]", "20", "%c[le5]")
            "s_cbranch_scc1 L_fin%=\n\t"
            STAGE("v72", "v73", "v74", "v75", "v[72:73]", "v[74:75]", "v84", "v[92:93]", "v92", "v[68:71]", "v[82:83]", "24", "%c[le6]")
            "s_cbranch_scc1 L_fin%=\n\t"
            STAGE("v76", "v77", "v78", "v79", "v[76:77]", "v[78:79]", "v86", "v[94:95]", "v94", "v[72:75]", "v[84:85]", "28", "%c[le7]")
            "v_add_u32 v89, 16, v89\n\t"
            "s_cbranch_scc0 L_loop%=\n\t"
            "L_fin%=:\n\t"
            "v_pk_add_f32 v[108:109], v[108:109], %[eps]\n\t"
            "v_pk_add_f32 v[110:111], v[110:111], %[eps]\n\t"
            "v_rcp_f32 v108, v108\n\t"
            "v_rcp_f32 v109, v109\n\t"
            "v_rcp_f32 v110, v110\n\t"
            "v_rcp_f32 v111, v111\n\t"
            "v_pk_mul_f32 v[104:105], v[104:105], v[108:109]\n\t"
            "v_pk_mul_f32 v[106:107], v[106:107], v[110:111]\n\t"
            "v_dot2c_f32_bf16 v104, %[shi], v116\n\t"
            "v_dot2c_f32_bf16 v105, %[shi], v117\n\t"
            "v_dot2c_f32_bf16 v106, %[shi], v118\n\t"
            "v_dot2c_f32_bf16 v107, %[shi], v119\n\t"
            "v_maximum3_f32 v104, v104, 0, 0\n\t"
            "v_maximum3_f32 v105, v105, 0, 0\n\t"
            "v_maximum3_f32 v106, v106, 0, 0\n\t"
            "v_maximum3_f32 v107, v107, 0, 0\n\t"
            "v_dot2c_f32_bf16 v104, %[slo], v120\n\t"
            "v_dot2c_f32_bf16 v105, %[shi], v120\n\t"
            "v_dot2c_f32_bf16 v106, %[slo], v121\n\t"
            "v_dot2c_f32_bf16 v107, %[shi], v121\n\t"
            "s_nop 1\n\t"
            "v_cvt_pk_bf16_f32 v112, v104, v105\n\t"
            "v_cvt_pk_bf16_f32 v113, v106, v107\n\t"
            "buffer_store_dwordx2 v[112:113], v124, %[o], 0 offen\n\t"
            PRIO_STEP
            "s_cmp_lt_u32 %[nxt], %[ntask]\n\t"
            "s_cbranch_scc1 L_task%=\n\t"
            "s_waitcnt vmcnt(0) lgkmcnt(0)"
            : [cnt] "=&s"(s_cnt), [nxt] "=&s"(s_nxt), [t] "=&s"(s_t), [tc] "=&s"(s_tc)
            : "{v81}"(joff), "{v83}"(joff), "{v85}"(joff), "{v87}"(joff), "{v91}"(g8), "{v93}"(ctr), "{v95}"(one), "{v126}"(j8),
              "{v[100:101]}"(w01), "{v[102:103]}"(w23),
              [task] "s"(w), [ntask] "s"(ntask), [bv] "s"(srdBV), [o] "s"(srdO), [slo] "s"(0x00003F80u), [shi] "s"(0x3F800000u), [eps] "s"(eps2),
              [au] "i"(K2_AU), [le0] "i"(K2_LSZ), [le1] "i"(K2_LSZ + 4), [le2] "i"(K2_LSZ + 8), [le3] "i"(K2_LSZ + 12),
              [le4] "i"(K2_LSZ + 16), [le5] "i"(K2_LSZ + 20), [le6] "i"(K2_LSZ + 24), [le7] "i"(K2_LSZ + 28)
            : "v64", "v65", "v66", "v67", "v68", "v69", "v70", "v71", "v72", "v73", "v74", "v75", "v76", "v77", "v78", "v79",
              "v80", "v82", "v84", "v86", "v88", "v89", "v90", "v92", "v94", "v96", "v97", "v98", "v99",
              "v104", "v105", "v106", "v107", "v108", "v109", "v110", "v111", "v112", "v113", "v114", "v115",
              "v116", "v117", "v118", "v119", "v120", "v121", "v122", "v123", "v124", "v125", "v127", "scc", "memory");
#undef STAGE
    }
    STAMP(10);
    STAMPC(15);
    SPAN1(1);
}

__global__ void k_e1(int* p) { if (p == (int*)1) *p = 0; }
__global__ void k_e2(int* p) { if (p == (int*)2) *p = 1; }
__global__ __launch_bounds__(512, 2) void k_x(int* p) { extern __shared__ char sm[]; asm volatile("v_mov_b32 v200, 0" ::: "v200"); if (p == (int*)1) *p = sm[0]; }
__global__ __launch_bounds__(512, 2) void k_y(int* p) { extern __shared__ char sm[]; asm volatile("v_mov_b32 v201, 0" ::: "v201"); if (p == (int*)2) *p = sm[1]; }
extern "C" void kernel_launch(void* const* d_in, const int* in_sizes, int n_in,
                              void* d_out, int out_size, void* d_ws, size_t ws_size,
                              hipStream_t stream) {
    const u16* x = (const u16*)d_in[0];
    const u16* ea = (const u16*)d_in[1];
    const int* src = (const int*)d_in[2];
    const int* dst = (const int*)d_in[3];
    const u16* wa = (const u16*)d_in[4];
    const u16* wb = (const u16*)d_in[5];
    const u16* wc = (const u16*)d_in[6];
    const u16* wu = (const u16*)d_in[7];
    const u16* wv = (const u16*)d_in[8];
    (void)hipFuncSetAttribute((const void*)k1_gemm_bin, hipFuncAttributeMaxDynamicSharedMemorySize, K1_LDS);
    k1_gemm_bin<<<256, 512, K1_LDS, stream>>>(x, wa, wb, wu, wv, src, dst, ea, (char*)d_ws);
    (void)hipFuncSetAttribute((const void*)k2_edges, hipFuncAttributeMaxDynamicSharedMemorySize, K2_LDS);
    k2_edges<<<256, 1024, K2_LDS, stream>>>(x, wc, src, dst, ea, (const char*)d_ws, (u16*)d_out);
    (void)hipFuncSetAttribute((const void*)k_x, hipFuncAttributeMaxDynamicSharedMemorySize, 163840);
    (void)hipFuncSetAttribute((const void*)k_y, hipFuncAttributeMaxDynamicSharedMemorySize, 163840);
    k_e1<<<256, 256, 0, stream>>>((int*)d_ws);
}
```

```cpp
#include <hip/hip_runtime.h>
#include <hip/hip_bf16.h>
#include <stdint.h>

typedef unsigned short u16;
typedef unsigned int u32;
typedef unsigned long long u64;
typedef u32 u32x2 __attribute__((ext_vector_type(2)));
typedef u32 u32x4 __attribute__((ext_vector_type(4)));
typedef float f32x2 __attribute__((ext_vector_type(2)));
typedef float f32x4 __attribute__((ext_vector_type(4)));
typedef __bf16 bf16x8 __attribute__((ext_vector_type(8)));
typedef __bf16 bf16x2 __attribute__((ext_vector_type(2)));

constexpr int NN = 10000;
constexpr int NE = 160000;
constexpr int DD = 256;
constexpr int NP = 10048;
constexpr int NBK = 64;
constexpr int NB = 157;
constexpr int NCH = 256;
constexpr int CHE = 625;
constexpr int CAP = 32;
constexpr int CAPN = 44;
constexpr int LST = 45;
constexpr float NLOG2E = -1.4426950408889634f;
constexpr u32 NEUTRAL_REC = (u32)NN;
constexpr u32 NEUTRAL_BV = 0x00007F00u;

constexpr size_t OFF_BV = 0;
constexpr size_t OFF_R4 = OFF_BV + (size_t)4 * NP * 256;
constexpr size_t OFF_R1 = OFF_R4 + (size_t)NBK * NCH * CAP * 4;
constexpr size_t OFF_F1 = OFF_R1 + (size_t)NBK * NCH * CAP;
constexpr size_t OFF_F2 = OFF_F1 + 2048;
constexpr size_t OFF_G = OFF_F2 + 2048;

#define LDS_AS __attribute__((address_space(3)))
#define GLB_AS __attribute__((address_space(1)))

__device__ __forceinline__ void glds16a(const void* sbase, u32 voff, u32 lds) {
    u32 keep;
    asm volatile("s_mov_b32 %0, m0\n\ts_mov_b32 m0, %3\n\ts_nop 0\n\tglobal_load_lds_dwordx4 %1, %2\n\ts_mov_b32 m0, %0" : "=&s"(keep) : "v"(voff), "s"(sbase), "s"(lds) : "memory");
}
__device__ __forceinline__ u32 cvt_pk_bf16(float lo, float hi) {
    const f32x2 v = {lo, hi}; return __builtin_bit_cast(u32, __builtin_convertvector(v, bf16x2));
}
#define WAIT_V(n) asm volatile("s_waitcnt vmcnt(" #n ")" ::: "memory")
#define WAIT_L(n) asm volatile("s_waitcnt lgkmcnt(" #n ")" ::: "memory")
#define BAR() __builtin_amdgcn_s_barrier()
#define AUX_SC1 16

#define MARK(I)

constexpr int L_WR = 0;
constexpr int L_AU = 0;
constexpr int L_XS = 40960;
constexpr int L_XT = 65536;
constexpr int L_FR = L_XT + 81920;
constexpr int L_ST4 = L_FR, L_ST1 = L_FR + 8192, L_BC = L_FR + 10240;
constexpr int L_END = 163840;
constexpr int LSTB = LST * 4;
constexpr int L_LIDX = L_XT;
constexpr int LSZ = (NB * LSTB + 32 + 256 + 15) / 16 * 16;
constexpr int L_LE = L_LIDX + LSZ;
constexpr int L_CNT = L_LE + LSZ;
constexpr int L_HIST = L_CNT + 160 * 4;
constexpr int L_ORD = L_HIST + 128 * 4;
constexpr int L_MISC = L_ORD + 80 * 32;
constexpr int TRASH = NB * LSTB + 32;
static_assert(L_MISC + 16 <= L_FR, "lds");
static_assert(TRASH + 256 <= LSZ && LSZ < 65536 - 64, "lists");

__device__ __forceinline__ float dot2sel(u32 d, u32 sel, float c) {
    return __builtin_amdgcn_fdot2_f32_bf16(__builtin_bit_cast(bf16x2, d), __builtin_bit_cast(bf16x2, sel), c, false);
}
struct Acc { f32x2 n01, n23, d01, d23; };
__device__ __forceinline__ void edge_step(Acc& a, const u32x4 d, const u32 rec, const f32x2 a01, const f32x2 a23, const f32x2 w01, const f32x2 w23, const u32 sel) {
    const float e = __uint_as_float(rec & 0xFFFF0000u);
    const f32x2 ee = {e, e};
    const f32x2 t01 = __builtin_elementwise_fma(ee, w01, a01);
    const f32x2 t23 = __builtin_elementwise_fma(ee, w23, a23);
    f32x2 E01, E23;
    E01.x = __builtin_amdgcn_exp2f(dot2sel(d.x, sel, t01.x));
    E01.y = __builtin_amdgcn_exp2f(dot2sel(d.y, sel, t01.y));
    E23.x = __builtin_amdgcn_exp2f(dot2sel(d.z, sel, t23.x));
    E23.y = __builtin_amdgcn_exp2f(dot2sel(d.w, sel, t23.y));
    const f32x2 one = {1.f, 1.f};
    E01 += one; E23 += one;
    f32x2 s01, s23;
    s01.x = __builtin_amdgcn_rcpf(E01.x); s01.y = __builtin_amdgcn_rcpf(E01.y);
    s23.x = __builtin_amdgcn_rcpf(E23.x); s23.y = __builtin_amdgcn_rcpf(E23.y);
    a.d01 += s01; a.d23 += s23;
    const f32x2 v01 = {__uint_as_float(d.x), __uint_as_float(d.y)};
    const f32x2 v23 = {__uint_as_float(d.z), __uint_as_float(d.w)};
    a.n01 = __builtin_elementwise_fma(s01, v01, a.n01);
    a.n23 = __builtin_elementwise_fma(s23, v23, a.n23);
}
__device__ __forceinline__ void node_finish(const Acc& a, const u32x4 au, const u32x2 xv, u16* outp) {
    const float num[4] = {a.n01.x, a.n01.y, a.n23.x, a.n23.y};
    const float den[4] = {a.d01.x, a.d01.y, a.d23.x, a.d23.y};
    const u32 aus[4] = {au.x, au.y, au.z, au.w};
    const float xs[4] = {__uint_as_float(xv.x << 16), __uint_as_float(xv.x & 0xFFFF0000u), __uint_as_float(xv.y << 16), __uint_as_float(xv.y & 0xFFFF0000u)};
    float o[4];
#pragma unroll
    for (int c = 0; c < 4; ++c) {
        const float z = __uint_as_float(aus[c] & 0xFFFF0000u) + num[c] * __builtin_amdgcn_rcpf(den[c] + 1e-6f);
        const float rl = z > 0.f ? z : 0.f * z;
        o[c] = xs[c] + rl;
    }
    u32x2 ov; ov.x = cvt_pk_bf16(o[0], o[1]); ov.y = cvt_pk_bf16(o[2], o[3]);
    *(u32x2*)outp = ov;
}
template <int CTRL, int RM> __device__ __forceinline__ u32 dpp_add(u32 v) {
    return v + (u32)__builtin_amdgcn_update_dpp(0, (int)v, CTRL, RM, 0xf, false);
}

__device__ __forceinline__ void wait_flags(const char* flags, u64 tag, int lane) {
    const u32 lo = (u32)tag, hi = (u32)(tag >> 32);
    for (int spins = 0; spins < (1 << 21); ++spins) {
        u32x4 a, b;
        asm volatile("global_load_dwordx4 %0, %2, %3 sc1\n\tglobal_load_dwordx4 %1, %2, %3 offset:16 sc1\n\ts_waitcnt vmcnt(0)"
                     : "=&v"(a), "=&v"(b) : "v"(lane * 32), "s"(flags) : "memory");
        const bool ok = a.x == lo && a.y == hi && a.z == lo && a.w == hi && b.x == lo && b.y == hi && b.z == lo && b.w == hi;
        if (__all(ok)) break;
        __builtin_amdgcn_s_sleep(1);
    }
}

#define TRRD(S) { u32x2 a0, a1, b0, b1; \
        asm volatile("ds_read_b64_tr_b16 %0, %4 offset:%c5\n\tds_read_b64_tr_b16 %1, %4 offset:%c6\n\t" \
                     "ds_read_b64_tr_b16 %2, %4 offset:%c7\n\tds_read_b64_tr_b16 %3, %4 offset:%c8" \
                     : "=&v"(a0), "=&v"(a1), "=&v"(b0), "=&v"(b1) \
                     : "v"(trbase), "i"((S) * 4096), "i"((S) * 4096 + 512), "i"(32768 + (S) * 4096), "i"(32768 + (S) * 4096 + 512) : "memory"); \
        wf0[S] = __builtin_bit_cast(bf16x8, __builtin_shufflevector(a0, a1, 0, 1, 2, 3)); \
        wf1[S] = __builtin_bit_cast(bf16x8, __builtin_shufflevector(b0, b1, 0, 1, 2, 3)); }
#define TRRD_ALL TRRD(0) TRRD(1) TRRD(2) TRRD(3) TRRD(4) TRRD(5) TRRD(6) TRRD(7)

__global__ __launch_bounds__(1024, 4) void gatedgcn_fused(
    const u16* __restrict__ x, const u16* __restrict__ ea, const int* __restrict__ src, const int* __restrict__ dst,
    const u16* __restrict__ wa, const u16* __restrict__ wb, const u16* __restrict__ wc, const u16* __restrict__ wu, const u16* __restrict__ wv,
    char* __restrict__ ws, u16* __restrict__ out) {
    extern __shared__ __attribute__((aligned(16))) char smem[];
    const int tid = threadIdx.x, lane = tid & 63;
    const int w = __builtin_amdgcn_readfirstlane(tid >> 6);
    const int b = blockIdx.x;
    const int slice = (b & 7) >> 1;
    const int bucket = (b >> 3) * 2 + (b & 1);
    const int node0 = bucket * NB;
    const int nvalid = (NN - node0) < NB ? (NN - node0) : NB;
    MARK(0);
    asm volatile("" :: "s"(x), "s"(ea), "s"(src), "s"(dst), "s"(wa), "s"(wb), "s"(wc), "s"(wu), "s"(wv), "s"(ws), "s"(out));

    const u64 tag = __hip_atomic_load((const GLB_AS u64*)(ws + OFF_G), __ATOMIC_RELAXED, __HIP_MEMORY_SCOPE_AGENT) + 0x9E3779B97F4A7C15ull;
    int bd = 0, bs = 0; u32 be = 0;
    if (w < 10) { const int e = b * CHE + (tid < CHE ? tid : 0); bd = dst[e]; bs = src[e]; be = ea[e]; }
    const int g = lane >> 4, j = lane & 15;
    const u32x2 wcv = *(const u32x2*)(wc + slice * 64 + j * 4);
    const int wkr = lane >> 3;
    auto stage_w = [&](const u16* W0, const u16* W1) {
#pragma unroll
        for (int i = 0; i < 4; ++i) {
            const int q = (w & 7) * 4 + i;
            const int hh = ((wkr >> 1) & 1) | ((q & 1) << 1);
            glds16a(w < 8 ? W0 : W1, (u32)((q * 8 + wkr) * 512 + slice * 128 + (((lane & 7) ^ (hh << 1)) << 4)), L_WR + (w >> 3) * 32768 + q * 1024);
        }
    };
    stage_w(wb, wv);
#pragma unroll
    for (int i = 0; i < 5; ++i) {
        const int q = w * 5 + i, r = q * 2 + (lane >> 5);
        int grow = node0 + r; grow = grow < NN ? grow : NN - 1;
        glds16a(x, (u32)(grow * 512 + (((lane & 31) ^ (r & 15)) << 4)), L_XT + q * 1024);
    }
    if (tid < 128) *(u32x4*)(smem + L_ST1 + tid * 16) = (u32x4){~0u, ~0u, ~0u, ~0u};
    else if (tid < 128 + NBK) *(u32*)(smem + L_BC + (tid - 128) * 4) = 0;
    f32x2 w01 = {__uint_as_float(wcv.x << 16) * NLOG2E, __uint_as_float(wcv.x & 0xFFFF0000u) * NLOG2E};
    f32x2 w23 = {__uint_as_float(wcv.y << 16) * NLOG2E, __uint_as_float(wcv.y & 0xFFFF0000u) * NLOG2E};
    u32 brec = (u32)bs | (be << 16);
    u64 tagv = tag;
    asm volatile("" : "+v"(w01), "+v"(w23), "+v"(brec), "+v"(bd), "+v"(tagv));
    WAIT_V(0); WAIT_L(0);
    BAR();
    MARK(1);

    const int strip = w & 3, rgp = w >> 2;
    const u32 trbase = L_WR + (8 * g + (j >> 2)) * 128
                     + (((2 * strip + ((lane & 3) >> 1)) ^ (((((j >> 2) >> 1) & 1) | ((g & 1) << 1)) << 1)) << 4) + (lane & 1) * 8;
    bf16x8 wf0[8], wf1[8];
    TRRD_ALL
    if (w < 10 && tid < CHE) {
        const int bk = (int)(((u32)bd * 53432u) >> 23);
        const u32 pos = atomicAdd((u32*)(smem + L_BC + bk * 4), 1u);
        if (pos < CAP) {
            *(u32*)(smem + L_ST4 + (bk * CAP + pos) * 4) = brec;
            *(unsigned char*)(smem + L_ST1 + bk * CAP + pos) = (unsigned char)(bd - bk * NB);
        }
    }
    WAIT_L(0);
    __builtin_amdgcn_sched_barrier(0);
    BAR();
    {
        const __amdgpu_buffer_rsrc_t r4s = __builtin_amdgcn_make_buffer_rsrc(ws + OFF_R4, 0, NBK * NCH * CAP * 4, 0x00020000);
        const __amdgpu_buffer_rsrc_t r1s = __builtin_amdgcn_make_buffer_rsrc(ws + OFF_R1, 0, NBK * NCH * CAP, 0x00020000);
        if (w < 8) {
            const u32x4 v = *(const u32x4*)(smem + L_ST4 + tid * 16);
            __builtin_amdgcn_raw_buffer_store_b128(v, r4s, ((tid >> 3) * NCH + b) * 128 + (tid & 7) * 16, 0, AUX_SC1);
        } else if (w < 10) {
            const int t = tid - 512, bk = t >> 1;
            u32x4 v = *(const u32x4*)(smem + L_ST1 + t * 16);
            if (*(const u32*)(smem + L_BC + bk * 4) > CAP) v = (u32x4){0xFEFEFEFEu, 0xFEFEFEFEu, 0xFEFEFEFEu, 0xFEFEFEFEu};
            __builtin_amdgcn_raw_buffer_store_b128(v, r1s, (bk * NCH + b) * 32 + (t & 1) * 16, 0, AUX_SC1);
        } else if (w == 10 && bucket == 0) {
            __hip_atomic_store((GLB_AS u32*)(ws + OFF_BV + ((size_t)slice * NP + NN) * 256) + lane, NEUTRAL_BV, __ATOMIC_RELAXED, __HIP_MEMORY_SCOPE_AGENT);
        }
        WAIT_L(0);
    }
    stage_w(wa, wu);
    BAR();

    const int xr = lane & 15;
    const u32 xbase = L_XT + xr * 512 + (((xr & 12) | ((g ^ xr) & 3)) << 4);
    const int ntile = rgp < 2 ? 3 : 2;
    {
        const __amdgpu_buffer_rsrc_t bvs = __builtin_amdgcn_make_buffer_rsrc(ws + OFF_BV + ((size_t)slice * NP + node0) * 256, 0, nvalid * 256, 0x00020000);
        char* const scr = smem + L_FR + w * 1024;
        const u32 swr = xr * 64 + ((g ^ ((xr >> 1) & 3)) << 4);
        const u32 srd = (lane >> 2) * 64 + (((lane & 3) ^ ((lane >> 3) & 3)) << 4);
        const u32 svo = (lane >> 2) * 256 + strip * 64 + (lane & 3) * 16;
#pragma unroll 1
        for (int it = 0; it < ntile; ++it) {
            const int tile = rgp + 4 * it;
            const u32 xa = xbase + tile * 8192;
            f32x4 acc0 = {0.f, 0.f, 0.f, 0.f}, acc1 = {0.f, 0.f, 0.f, 0.f};
#pragma unroll
            for (int s = 0; s < 8; ++s) {
                const bf16x8 xb = *(const bf16x8*)(smem + ((xa ^ ((s & 3) << 6)) + (s >> 2) * 256));
                acc0 = __builtin_amdgcn_mfma_f32_16x16x32_bf16(wf0[s], xb, acc0, 0, 0, 0);
                acc1 = __builtin_amdgcn_mfma_f32_16x16x32_bf16(wf1[s], xb, acc1, 0, 0, 0);
            }
            u32x4 d;
#pragma unroll
            for (int i = 0; i < 4; i += 2) {
                const u32 bb = cvt_pk_bf16(acc0[i] * NLOG2E, acc0[i + 1] * NLOG2E);
#pragma unroll
                for (int k = 0; k < 2; ++k) {
                    const u32 vb = __float_as_uint(acc1[i + k]);
                    const u32 bl = k ? (bb >> 16) : (bb & 0xFFFFu);
                    const u32 m = __float_as_uint(__builtin_fmaxf(__builtin_fabsf(acc1[i + k]), 1.17549435e-38f));
                    const u32 u = m + 0x8000u - bl;
                    d[i + k] = (vb & 0x80000000u) | (u & 0x7FFF0000u) | bl;
                }
            }
            *(u32x4*)(scr + swr) = d;
            const u32x4 v = *(const u32x4*)(scr + srd);
            __builtin_amdgcn_raw_buffer_store_b128(v, bvs, svo + (u32)tile * 4096, 0, AUX_SC1);
        }
        if (rgp < 2) WAIT_V(3); else WAIT_V(2);
    }
    BAR();
    if (tid == 0) __hip_atomic_store((GLB_AS u64*)(ws + OFF_F1) + b, tagv, __ATOMIC_RELAXED, __HIP_MEMORY_SCOPE_AGENT);
    MARK(2);
    TRRD_ALL
    WAIT_L(0);
    __builtin_amdgcn_sched_barrier(0);
    BAR();
    {
#pragma unroll 1
        for (int it = 0; it < ntile; ++it) {
            const int tile = rgp + 4 * it;
            const u32 xa = xbase + tile * 8192;
            f32x4 acc0 = {0.f, 0.f, 0.f, 0.f}, acc1 = {0.f, 0.f, 0.f, 0.f};
#pragma unroll
            for (int s = 0; s < 8; ++s) {
                const bf16x8 xb = *(const bf16x8*)(smem + ((xa ^ ((s & 3) << 6)) + (s >> 2) * 256));
                acc0 = __builtin_amdgcn_mfma_f32_16x16x32_bf16(wf0[s], xb, acc0, 0, 0, 0);
                acc1 = __builtin_amdgcn_mfma_f32_16x16x32_bf16(wf1[s], xb, acc1, 0, 0, 0);
            }
            u32x4 d;
#pragma unroll
            for (int i = 0; i < 4; ++i) d[i] = cvt_pk_bf16(acc0[i] * NLOG2E, acc1[i]);
            *(u32x4*)(smem + L_AU + (tile * 16 + xr) * 256 + (((strip * 4 + g) ^ xr) << 4)) = d;
        }
#pragma unroll
        for (int i = 0; i < 2; ++i) {
            const int q = tid + 1024 * i;
            if (q < 1280) {
                const int r = q >> 3, c8 = q & 7;
                const u32x4 v = *(const u32x4*)(smem + L_XT + r * 512 + (((slice * 8 + c8) ^ (r & 15)) << 4));
                *(u32x2*)(smem + L_XS + r * 128 + (((2 * c8) ^ (r & 15)) << 3)) = (u32x2){v.x, v.y};
                *(u32x2*)(smem + L_XS + r * 128 + (((2 * c8 + 1) ^ (r & 15)) << 3)) = (u32x2){v.z, v.w};
            }
        }
    }
    if (w == 15) wait_flags(ws + OFF_F1, tag, lane);
    WAIT_V(0); WAIT_L(0);
    BAR();
    if (tid == 0) __hip_atomic_store((GLB_AS u64*)(ws + OFF_F2) + b, tagv, __ATOMIC_RELAXED, __HIP_MEMORY_SCOPE_AGENT);
    MARK(3);

    u32x4 r4a, r4b; u32x2 r1;
    asm volatile("global_load_dwordx4 %0, %3, %4 sc1\n\tglobal_load_dwordx4 %1, %3, %4 offset:16 sc1\n\tglobal_load_dwordx2 %2, %5, %6 sc1"
                 : "=&v"(r4a), "=&v"(r4b), "=&v"(r1)
                 : "v"(tid * 32), "s"(ws + OFF_R4 + (size_t)bucket * NCH * CAP * 4), "v"(tid * 8), "s"(ws + OFF_R1 + (size_t)bucket * NCH * CAP) : "memory");
    u32* const cnt = (u32*)(smem + L_CNT);
    u32* const hist = (u32*)(smem + L_HIST);
    u32* const misc = (u32*)(smem + L_MISC);
    for (int i = tid; i < LSZ / 16; i += 1024) {
        *(u32x4*)(smem + L_LIDX + i * 16) = (u32x4){NEUTRAL_REC, NEUTRAL_REC, NEUTRAL_REC, NEUTRAL_REC};
        *(u32x4*)(smem + L_LE + i * 16) = (u32x4){0, 0, 0, 0};
    }
    if (tid < 160 + 64) cnt[tid] = 0;
    if (tid == 0) { misc[0] = 16; misc[1] = 0; }
    asm volatile("s_waitcnt vmcnt(0) lgkmcnt(0)" : "+v"(r4a), "+v"(r4b), "+v"(r1) :: "memory");
    __builtin_amdgcn_sched_barrier(0);
    BAR();
    MARK(4);
    {
        const u32 rec[8] = {r4a.x, r4a.y, r4a.z, r4a.w, r4b.x, r4b.y, r4b.z, r4b.w};
        u32 dl[8], slot[8];
        bool ovf = false;
#pragma unroll
        for (int k = 0; k < 8; ++k) { dl[k] = ((k < 4 ? r1.x : r1.y) >> (8 * (k & 3))) & 0xFF; ovf |= dl[k] == 0xFE; }
#pragma unroll
        for (int k = 0; k < 8; ++k) slot[k] = atomicAdd(&cnt[dl[k] < (u32)NB ? dl[k] : 224 + lane], 1u);
#pragma unroll
        for (int k = 0; k < 8; ++k) {
            const bool v = dl[k] < (u32)NB, ok = v && slot[k] < CAPN;
            ovf |= v && !ok;
            const u32 o = ok ? dl[k] * LSTB + slot[k] * 4 : TRASH + lane * 4;
            *(u32*)(smem + L_LIDX + o) = rec[k] & 0xFFFFu;
            *(u32*)(smem + L_LE + o) = rec[k] & 0xFFFF0000u;
        }
        if (ovf) misc[1] = 1;
    }
    WAIT_L(0); BAR();
    const bool slow = misc[1] != 0;
    u32 mydeg = 0, myrank = 0;
    if (tid < nvalid) { mydeg = cnt[tid]; mydeg = mydeg < 63 ? mydeg : 63; myrank = atomicAdd(&hist[63 - mydeg], 1u); }
    WAIT_L(0); BAR();
    if (w < 3) {
        const u32 h0 = hist[lane];
        u32 v = h0;
        v = dpp_add<0x111, 0xf>(v); v = dpp_add<0x112, 0xf>(v); v = dpp_add<0x114, 0xf>(v); v = dpp_add<0x118, 0xf>(v);
        v = dpp_add<0x142, 0xa>(v); v = dpp_add<0x143, 0xc>(v);
        const u32 base = (u32)__builtin_amdgcn_ds_bpermute((int)((63 - mydeg) * 4), (int)(v - h0));
        if (tid < nvalid) {
            const u32 pos = base + myrank;
            const u32x2 ent = {(u32)(L_LIDX + tid * LSTB) | (mydeg << 24), (u32)(tid * 256) | (u32)((tid & 15) << 4)};
            u32x2* o = (u32x2*)(smem + L_ORD) + pos;
            o[0] = ent;
            if (pos == (u32)nvalid - 1) { o[1] = ent; o[2] = ent; o[3] = ent; }
        }
    }
    if (w == 15) wait_flags(ws + OFF_F2, tag, lane);
    WAIT_L(0); BAR();
    if (b == 0 && tid == 0) __hip_atomic_store((GLB_AS u64*)(ws + OFF_G), tagv, __ATOMIC_RELAXED, __HIP_MEMORY_SCOPE_AGENT);
    MARK(5);

    const char* const bvb = ws + OFF_BV + (size_t)slice * NP * 256;
    const u32 joff = j * 16;
    if (slow) {
        __builtin_amdgcn_fence(__ATOMIC_ACQUIRE, "agent");
        u32 sel = 0x00003F80u;
        asm volatile("" : "+s"(sel));
        for (int task = w; task * 4 < nvalid; task += 16) {
            const int n = task * 4 + g;
            const bool nv = n < nvalid;
            const int nl = nv ? n : 0;
            const int node = node0 + nl;
            const u32x4 au = *(const u32x4*)(smem + L_AU + nl * 256 + ((j ^ (nl & 15)) << 4));
            const u32x2 xv = *(const u32x2*)(smem + L_XS + nl * 128 + ((j ^ (nl & 15)) << 3));
            const f32x2 a01 = {__uint_as_float(au.x << 16), __uint_as_float(au.y << 16)};
            const f32x2 a23 = {__uint_as_float(au.z << 16), __uint_as_float(au.w << 16)};
            Acc acc = {{0.f, 0.f}, {0.f, 0.f}, {0.f, 0.f}, {0.f, 0.f}};
            const int lo = node0 + task * 4;
            for (int e0 = 0; e0 < NE; e0 += 64) {
                const int d = dst[e0 + lane];
                unsigned long long m = __ballot((u32)(d - lo) < 4u);
                if (m) {
                    const u32 rc = (u32)src[e0 + lane] | ((u32)ea[e0 + lane] << 16);
                    while (m) {
                        const int l = __builtin_ctzll(m); m &= m - 1;
                        const int dd = __builtin_amdgcn_readlane(d, l);
                        const u32 rr = __builtin_amdgcn_readlane(rc, l);
                        const u32 rec = (nv && dd == node) ? rr : NEUTRAL_REC;
                        const u32x4 dv = *(const u32x4*)(bvb + (size_t)((rec & 0xFFFFu) * 256 + joff));
                        edge_step(acc, dv, rec, a01, a23, w01, w23, sel);
                    }
                }
            }
            if (nv) node_finish(acc, au, xv, out + (size_t)node * DD + slice * 64 + j * 4);
        }
        return;
    }

    const int ntask = (nvalid + 3) >> 2;
    if (w < ntask) {
        u32x4 srdBV, srdO;
        { const size_t p = (size_t)bvb; srdBV = (u32x4){(u32)p, (u32)(p >> 32) | (256u << 16), 0xFFFFFFFFu, 0x00020000u}; }
        { const size_t ob = (size_t)node0 * 512 + slice * 128; const size_t p = (size_t)out + ob; srdO = (u32x4){(u32)p, (u32)(p >> 32), (u32)((size_t)NN * 512 - ob), 0x00020000u}; }
        u32 g8 = L_ORD + g * 8, ctr = L_MISC, one = 1, j8 = j * 8;
        unsigned long long eps2 = 0x358637BD358637BDull;
        u32 s_cnt, s_nxt, s_t, s_tc;
#define STAGE(DS0, DS1, DS2, DS3, DSL, DSH, IX, EP, E1, DN, IXN, O1, O2) \
            "s_waitcnt lgkmcnt(0)\n\t" \
            "buffer_load_dwordx4 " DN ", " IXN ", %[bv], 0 idxen offen sc1\n\t" \
            "v_pk_fma_f32 v[112:113], " EP ", v[100:101], v[96:97] op_sel_hi:[0,1,1]\n\t" \
            "v_pk_fma_f32 v[114:115], " EP ", v[102:103], v[98:99] op_sel_hi:[0,1,1]\n\t" \
            "ds_read_b32 " IX ", v89 offset:" O1 "\n\t" \
            "ds_read_b32 " E1 ", v89 offset:" O2 "\n\t" \
            "s_waitcnt vmcnt(3)\n\t" \
            "v_dot2c_f32_bf16 v112, %[slo], " DS0 "\n\t" \
            "v_dot2c_f32_bf16 v113, %[slo], " DS1 "\n\t" \
            "v_dot2c_f32_bf16 v114, %[slo], " DS2 "\n\t" \
            "v_dot2c_f32_bf16 v115, %[slo], " DS3 "\n\t" \
            "v_exp_f32 v112, v112\n\t" \
            "v_exp_f32 v113, v113\n\t" \
            "v_exp_f32 v114, v114\n\t" \
            "v_exp_f32 v115, v115\n\t" \
            "v_pk_add_f32 v[112:113], v[112:113], 1.0 op_sel_hi:[1,0]\n\t" \
            "v_pk_add_f32 v[114:115], v[114:115], 1.0 op_sel_hi:[1,0]\n\t" \
            "v_rcp_f32 v112, v112\n\t" \
            "v_rcp_f32 v113, v113\n\t" \
            "v_rcp_f32 v114, v114\n\t" \
            "v_rcp_f32 v115, v115\n\t" \
            "s_sub_u32 %[cnt], %[cnt], 1\n\t" \
            "v_pk_add_f32 v[108:109], v[108:109], v[112:113]\n\t" \
            "v_pk_fma_f32 v[104:105], v[112:113], " DSL ", v[104:105]\n\t" \
            "v_pk_add_f32 v[110:111], v[110:111], v[114:115]\n\t" \
            "v_pk_fma_f32 v[106:107], v[114:115], " DSH ", v[106:107]\n\t" \
            "s_cmp_eq_u32 %[cnt], 0\n\t"
#define PRIO_INIT "s_setprio 3\n\t"
#define PRIO_STEP \
            "s_add_u32 %[tc], %[tc], 1\n\t" \
            "s_cmp_lg_u32 %[tc], 1\n\t s_cbranch_scc1 L_p1%=\n\t s_setprio 2\n\t L_p1%=:\n\t" \
            "s_cmp_lg_u32 %[tc], 2\n\t s_cbranch_scc1 L_p2%=\n\t s_setprio 1\n\t L_p2%=:\n\t" \
            "s_cmp_lg_u32 %[tc], 3\n\t s_cbranch_scc1 L_p3%=\n\t s_setprio 0\n\t L_p3%=:\n\t"
        asm volatile(
            PRIO_INIT
            "s_mov_b32 %[tc], 0\n\t"
            "s_lshl_b32 %[t], %[task], 5\n\t"
            "v_add_u32 v127, %[t], v91\n\t"
            "ds_read_b64 v[122:123], v127\n\t"
            "s_mov_b64 exec, 1\n\t"
            "ds_add_rtn_u32 v125, v93, v95\n\t"
            "s_mov_b64 exec, -1\n\t"
            "L_task%=:\n\t"
            "s_waitcnt lgkmcnt(0)\n\t"
            "v_readfirstlane_b32 %[nxt], v125\n\t"
            "v_readfirstlane_b32 %[cnt], v122\n\t"
            "v_and_b32 v89, 0xffffff, v122\n\t"
            "v_and_b32 v127, 0xffffff00, v123\n\t"
            "v_xor_b32 v112, v123, v81\n\t"
            "ds_read_b32 v80, v89\n\t"
            "ds_read_b32 v82, v89 offset:4\n\t"
            "ds_read_b32 v84, v89 offset:8\n\t"
            "ds_read_b32 v86, v89 offset:12\n\t"
            "ds_read_b32 v88, v89 offset:%c[le0]\n\t"
            "ds_read_b32 v90, v89 offset:%c[le1]\n\t"
            "ds_read_b32 v92, v89 offset:%c[le2]\n\t"
            "ds_read_b32 v94, v89 offset:%c[le3]\n\t"
            "v_lshl_add_u32 v124, v127, 1, v126\n\t"
            "v_lshrrev_b32 v113, 1, v112\n\t"
            "ds_read_b128 v[116:119], v112 offset:%c[au]\n\t"
            "ds_read_b64 v[120:121], v113 offset:%c[xs]\n\t"
            "s_lshr_b32 %[cnt], %[cnt], 24\n\t"
            "s_lshl_b32 %[t], %[nxt], 5\n\t"
            "v_add_u32 v127, %[t], v91\n\t"
            "ds_read_b64 v[122:123], v127\n\t"
            "s_mov_b64 exec, 1\n\t"
            "ds_add_rtn_u32 v125, v93, v95\n\t"
            "s_mov_b64 exec, -1\n\t"
            "s_waitcnt lgkmcnt(9)\n\t"
            "buffer_load_dwordx4 v[64:67], v[80:81], %[bv], 0 idxen offen sc1\n\t"
            "buffer_load_dwordx4 v[68:71], v[82:83], %[bv], 0 idxen offen sc1\n\t"
            "buffer_load_dwordx4 v[72:75], v[84:85], %[bv], 0 idxen offen sc1\n\t"
            "v_mov_b64 v[104:105], 0\n\t"
            "v_mov_b64 v[106:107], 0\n\t"
            "v_mov_b64 v[108:109], 0\n\t"
            "v_mov_b64 v[110:111], 0\n\t"
            "s_waitcnt lgkmcnt(2)\n\t"
            "v_lshlrev_b32 v96, 16, v116\n\t"
            "v_lshlrev_b32 v97, 16, v117\n\t"
            "v_lshlrev_b32 v98, 16, v118\n\t"
            "v_lshlrev_b32 v99, 16, v119\n\t"
            "s_cmp_eq_u32 %[cnt], 0\n\t"
            "s_cbranch_scc1 L_fin%=\n\t"
            "L_loop%=:\n\t"
            STAGE("v64", "v65", "v66", "v67", "v[64:65]", "v[66:67]", "v80", "v[88:89]", "v88", "v[76:79]", "v[86:87]", "16", "%c[le4]")
            "s_cbranch_scc1 L_fin%=\n\t"
            STAGE("v68", "v69", "v70", "v71", "v[68:69]", "v[70:71]", "v82", "v[90:91]", "v90", "v[64:67]", "v[80:81]", "20", "%c[le5]")
            "s_cbranch_scc1 L_fin%=\n\t"
            STAGE("v72", "v73", "v74", "v75", "v[72:73]", "v[74:75]", "v84", "v[92:93]", "v92", "v[68:71]", "v[82:83]", "24", "%c[le6]")
            "s_cbranch_scc1 L_fin%=\n\t"
            STAGE("v76", "v77", "v78", "v79", "v[76:77]", "v[78:79]", "v86", "v[94:95]", "v94", "v[72:75]", "v[84:85]", "28", "%c[le7]")
            "v_add_u32 v89, 16, v89\n\t"
            "s_cbranch_scc0 L_loop%=\n\t"
            "L_fin%=:\n\t"
            "v_pk_add_f32 v[108:109], v[108:109], %[eps]\n\t"
            "v_pk_add_f32 v[110:111], v[110:111], %[eps]\n\t"
            "v_rcp_f32 v108, v108\n\t"
            "v_rcp_f32 v109, v109\n\t"
            "v_rcp_f32 v110, v110\n\t"
            "v_rcp_f32 v111, v111\n\t"
            "v_pk_mul_f32 v[104:105], v[104:105], v[108:109]\n\t"
            "v_pk_mul_f32 v[106:107], v[106:107], v[110:111]\n\t"
            "v_dot2c_f32_bf16 v104, %[shi], v116\n\t"
            "v_dot2c_f32_bf16 v105, %[shi], v117\n\t"
            "v_dot2c_f32_bf16 v106, %[shi], v118\n\t"
            "v_dot2c_f32_bf16 v107, %[shi], v119\n\t"
            "v_maximum3_f32 v104, v104, 0, 0\n\t"
            "v_maximum3_f32 v105, v105, 0, 0\n\t"
            "v_maximum3_f32 v106, v106, 0, 0\n\t"
            "v_maximum3_f32 v107, v107, 0, 0\n\t"
            "v_dot2c_f32_bf16 v104, %[slo], v120\n\t"
            "v_dot2c_f32_bf16 v105, %[shi], v120\n\t"
            "v_dot2c_f32_bf16 v106, %[slo], v121\n\t"
            "v_dot2c_f32_bf16 v107, %[shi], v121\n\t"
            "s_nop 1\n\t"
            "v_cvt_pk_bf16_f32 v112, v104, v105\n\t"
            "v_cvt_pk_bf16_f32 v113, v106, v107\n\t"
            "buffer_store_dwordx2 v[112:113], v124, %[o], 0 offen\n\t"
            PRIO_STEP
            "s_cmp_lt_u32 %[nxt], %[ntask]\n\t"
            "s_cbranch_scc1 L_task%=\n\t"
            "s_waitcnt vmcnt(0) lgkmcnt(0)"
            : [cnt] "=&s"(s_cnt), [nxt] "=&s"(s_nxt), [t] "=&s"(s_t), [tc] "=&s"(s_tc)
            : "{v81}"(joff), "{v83}"(joff), "{v85}"(joff), "{v87}"(joff), "{v91}"(g8), "{v93}"(ctr), "{v95}"(one), "{v126}"(j8),
              "{v[100:101]}"(w01), "{v[102:103]}"(w23),
              [task] "s"(w), [ntask] "s"(ntask), [bv] "s"(srdBV), [o] "s"(srdO), [slo] "s"(0x00003F80u), [shi] "s"(0x3F800000u), [eps] "s"(eps2),
              [au] "i"(L_AU), [xs] "i"(L_XS), [le0] "i"(LSZ), [le1] "i"(LSZ + 4), [le2] "i"(LSZ + 8), [le3] "i"(LSZ + 12),
              [le4] "i"(LSZ + 16), [le5] "i"(LSZ + 20), [le6] "i"(LSZ + 24), [le7] "i"(LSZ + 28)
            : "v64", "v65", "v66", "v67", "v68", "v69", "v70", "v71", "v72", "v73", "v74", "v75", "v76", "v77", "v78", "v79",
              "v80", "v82", "v84", "v86", "v88", "v89", "v90", "v92", "v94", "v96", "v97", "v98", "v99",
              "v104", "v105", "v106", "v107", "v108", "v109", "v110", "v111", "v112", "v113", "v114", "v115",
              "v116", "v117", "v118", "v119", "v120", "v121", "v122", "v123", "v124", "v125", "v127", "scc", "memory");
#undef STAGE
    }
    MARK(6);
}

extern "C" void kernel_launch(void* const* d_in, const int* in_sizes, int n_in,
                              void* d_out, int out_size, void* d_ws, size_t ws_size,
                              hipStream_t stream) {
    const u16* x = (const u16*)d_in[0];
    const u16* ea = (const u16*)d_in[1];
    const int* src = (const int*)d_in[2];
    const int* dst = (const int*)d_in[3];
    const u16* wa = (const u16*)d_in[4];
    const u16* wb = (const u16*)d_in[5];
    const u16* wc = (const u16*)d_in[6];
    const u16* wu = (const u16*)d_in[7];
    const u16* wv = (const u16*)d_in[8];
    (void)hipFuncSetAttribute((const void*)gatedgcn_fused, hipFuncAttributeMaxDynamicSharedMemorySize, L_END);
    gatedgcn_fused<<<256, 1024, L_END, stream>>>(x, ea, src, dst, wa, wb, wc, wu, wv, (char*)d_ws, (u16*)d_out);
}
```

```cpp
#define FMARKS 7
#define MEASURE 0
#include <hip/hip_runtime.h>
#include <hip/hip_bf16.h>
#include <stdint.h>

typedef unsigned short u16;
typedef unsigned int u32;
typedef unsigned long long u64;
typedef u32 u32x2 __attribute__((ext_vector_type(2)));
typedef u32 u32x4 __attribute__((ext_vector_type(4)));
typedef float f32x2 __attribute__((ext_vector_type(2)));
typedef float f32x4 __attribute__((ext_vector_type(4)));
typedef __bf16 bf16x8 __attribute__((ext_vector_type(8)));
typedef __bf16 bf16x2 __attribute__((ext_vector_type(2)));

constexpr int NN = 10000;
constexpr int NE = 160000;
constexpr int DD = 256;
constexpr int NP = 10048;
constexpr int NBK = 64;
constexpr int NB = 157;
constexpr int NCH = 256;
constexpr int CHE = 625;
constexpr int CAP = 32;
constexpr int CAPN = 44;
constexpr int LST = 45;
constexpr float NLOG2E = -1.4426950408889634f;
constexpr u32 NEUTRAL_REC = (u32)NN;
constexpr u32 NEUTRAL_BV = 0x00007F00u;

constexpr size_t OFF_BV = 0;
constexpr size_t OFF_R4 = OFF_BV + (size_t)4 * NP * 256;
constexpr size_t OFF_R1 = OFF_R4 + (size_t)NBK * NCH * CAP * 4;
constexpr size_t OFF_F1 = OFF_R1 + (size_t)NBK * NCH * CAP;
constexpr size_t OFF_F2 = OFF_F1 + 2048;
constexpr size_t OFF_G = OFF_F2 + 2048;

#define LDS_AS __attribute__((address_space(3)))
#define GLB_AS __attribute__((address_space(1)))

__device__ __forceinline__ void glds16a(const void* sbase, u32 voff, u32 lds) {
    u32 keep;
    asm volatile("s_mov_b32 %0, m0\n\ts_mov_b32 m0, %3\n\ts_nop 0\n\tglobal_load_lds_dwordx4 %1, %2\n\ts_mov_b32 m0, %0" : "=&s"(keep) : "v"(voff), "s"(sbase), "s"(lds) : "memory");
}
__device__ __forceinline__ u32 cvt_pk_bf16(float lo, float hi) {
    const f32x2 v = {lo, hi}; return __builtin_bit_cast(u32, __builtin_convertvector(v, bf16x2));
}
#define WAIT_V(n) asm volatile("s_waitcnt vmcnt(" #n ")" ::: "memory")
#define WAIT_L(n) asm volatile("s_waitcnt lgkmcnt(" #n ")" ::: "memory")
#define BAR() __builtin_amdgcn_s_barrier()
#define AUX_SC1 16

#define MARK(I) do { u64 tm_ = __builtin_amdgcn_s_memrealtime(); if ((threadIdx.x & 63) == 0) __hip_atomic_store((GLB_AS u64*)((char*)ws + (200u << 20)) + ((I) * 256 + blockIdx.x) * 16 + (threadIdx.x >> 6), tm_, __ATOMIC_RELAXED, __HIP_MEMORY_SCOPE_AGENT); } while (0)

constexpr int L_WR = 0;
constexpr int L_AU = 0;
constexpr int L_XS = 40960;
constexpr int L_XT = 65536;
constexpr int L_FR = L_XT + 81920;
constexpr int L_ST4 = L_FR, L_ST1 = L_FR + 8192, L_BC = L_FR + 10240;
constexpr int L_END = 163840;
constexpr int LSTB = LST * 4;
constexpr int L_LIDX = L_XT;
constexpr int LSZ = (NB * LSTB + 32 + 256 + 15) / 16 * 16;
constexpr int L_LE = L_LIDX + LSZ;
constexpr int L_CNT = L_LE + LSZ;
constexpr int L_HIST = L_CNT + 160 * 4;
constexpr int L_ORD = L_HIST + 128 * 4;
constexpr int L_MISC = L_ORD + 80 * 32;
constexpr int TRASH = NB * LSTB + 32;
static_assert(L_MISC + 16 <= L_FR, "lds");
static_assert(TRASH + 256 <= LSZ && LSZ < 65536 - 64, "lists");

__device__ __forceinline__ float dot2sel(u32 d, u32 sel, float c) {
    return __builtin_amdgcn_fdot2_f32_bf16(__builtin_bit_cast(bf16x2, d), __builtin_bit_cast(bf16x2, sel), c, false);
}
struct Acc { f32x2 n01, n23, d01, d23; };
__device__ __forceinline__ void edge_step(Acc& a, const u32x4 d, const u32 rec, const f32x2 a01, const f32x2 a23, const f32x2 w01, const f32x2 w23, const u32 sel) {
    const float e = __uint_as_float(rec & 0xFFFF0000u);
    const f32x2 ee = {e, e};
    const f32x2 t01 = __builtin_elementwise_fma(ee, w01, a01);
    const f32x2 t23 = __builtin_elementwise_fma(ee, w23, a23);
    f32x2 E01, E23;
    E01.x = __builtin_amdgcn_exp2f(dot2sel(d.x, sel, t01.x));
    E01.y = __builtin_amdgcn_exp2f(dot2sel(d.y, sel, t01.y));
    E23.x = __builtin_amdgcn_exp2f(dot2sel(d.z, sel, t23.x));
    E23.y = __builtin_amdgcn_exp2f(dot2sel(d.w, sel, t23.y));
    const f32x2 one = {1.f, 1.f};
    E01 += one; E23 += one;
    f32x2 s01, s23;
    s01.x = __builtin_amdgcn_rcpf(E01.x); s01.y = __builtin_amdgcn_rcpf(E01.y);
    s23.x = __builtin_amdgcn_rcpf(E23.x); s23.y = __builtin_amdgcn_rcpf(E23.y);
    a.d01 += s01; a.d23 += s23;
    const f32x2 v01 = {__uint_as_float(d.x), __uint_as_float(d.y)};
    const f32x2 v23 = {__uint_as_float(d.z), __uint_as_float(d.w)};
    a.n01 = __builtin_elementwise_fma(s01, v01, a.n01);
    a.n23 = __builtin_elementwise_fma(s23, v23, a.n23);
}
__device__ __forceinline__ void node_finish(const Acc& a, const u32x4 au, const u32x2 xv, u16* outp) {
    const float num[4] = {a.n01.x, a.n01.y, a.n23.x, a.n23.y};
    const float den[4] = {a.d01.x, a.d01.y, a.d23.x, a.d23.y};
    const u32 aus[4] = {au.x, au.y, au.z, au.w};
    const float xs[4] = {__uint_as_float(xv.x << 16), __uint_as_float(xv.x & 0xFFFF0000u), __uint_as_float(xv.y << 16), __uint_as_float(xv.y & 0xFFFF0000u)};
    float o[4];
#pragma unroll
    for (int c = 0; c < 4; ++c) {
        const float z = __uint_as_float(aus[c] & 0xFFFF0000u) + num[c] * __builtin_amdgcn_rcpf(den[c] + 1e-6f);
        const float rl = z > 0.f ? z : 0.f * z;
        o[c] = xs[c] + rl;
    }
    u32x2 ov; ov.x = cvt_pk_bf16(o[0], o[1]); ov.y = cvt_pk_bf16(o[2], o[3]);
    *(u32x2*)outp = ov;
}
template <int CTRL, int RM> __device__ __forceinline__ u32 dpp_add(u32 v) {
    return v + (u32)__builtin_amdgcn_update_dpp(0, (int)v, CTRL, RM, 0xf, false);
}

__device__ __forceinline__ void wait_flags(const char* flags, u64 tag, int lane) {
    const u32 lo = (u32)tag, hi = (u32)(tag >> 32);
    for (int spins = 0; spins < (1 << 21); ++spins) {
        u32x4 a, b;
        asm volatile("global_load_dwordx4 %0, %2, %3 sc1\n\tglobal_load_dwordx4 %1, %2, %3 offset:16 sc1\n\ts_waitcnt vmcnt(0)"
                     : "=&v"(a), "=&v"(b) : "v"(lane * 32), "s"(flags) : "memory");
        const bool ok = a.x == lo && a.y == hi && a.z == lo && a.w == hi && b.x == lo && b.y == hi && b.z == lo && b.w == hi;
        if (__all(ok)) break;
        __builtin_amdgcn_s_sleep(1);
    }
}

#define TRRD(S) { u32x2 a0, a1, b0, b1; \
        asm volatile("ds_read_b64_tr_b16 %0, %4 offset:%c5\n\tds_read_b64_tr_b16 %1, %4 offset:%c6\n\t" \
                     "ds_read_b64_tr_b16 %2, %4 offset:%c7\n\tds_read_b64_tr_b16 %3, %4 offset:%c8" \
                     : "=&v"(a0), "=&v"(a1), "=&v"(b0), "=&v"(b1) \
                     : "v"(trbase), "i"((S) * 4096), "i"((S) * 4096 + 512), "i"(32768 + (S) * 4096), "i"(32768 + (S) * 4096 + 512) : "memory"); \
        wf0[S] = __builtin_bit_cast(bf16x8, __builtin_shufflevector(a0, a1, 0, 1, 2, 3)); \
        wf1[S] = __builtin_bit_cast(bf16x8, __builtin_shufflevector(b0, b1, 0, 1, 2, 3)); }
#define TRRD_ALL TRRD(0) TRRD(1) TRRD(2) TRRD(3) TRRD(4) TRRD(5) TRRD(6) TRRD(7)

__global__ __launch_bounds__(1024, 4) void gatedgcn_fused(
    const u16* __restrict__ x, const u16* __restrict__ ea, const int* __restrict__ src, const int* __restrict__ dst,
    const u16* __restrict__ wa, const u16* __restrict__ wb, const u16* __restrict__ wc, const u16* __restrict__ wu, const u16* __restrict__ wv,
    char* __restrict__ ws, u16* __restrict__ out) {
    extern __shared__ __attribute__((aligned(16))) char smem[];
    const int tid = threadIdx.x, lane = tid & 63;
    const int w = __builtin_amdgcn_readfirstlane(tid >> 6);
    const int b = blockIdx.x;
    const int slice = (b & 7) >> 1;
    const int bucket = (b >> 3) * 2 + (b & 1);
    const int node0 = bucket * NB;
    const int nvalid = (NN - node0) < NB ? (NN - node0) : NB;
    MARK(0);
    asm volatile("" :: "s"(x), "s"(ea), "s"(src), "s"(dst), "s"(wa), "s"(wb), "s"(wc), "s"(wu), "s"(wv), "s"(ws), "s"(out));

    const u64 tag = __hip_atomic_load((const GLB_AS u64*)(ws + OFF_G), __ATOMIC_RELAXED, __HIP_MEMORY_SCOPE_AGENT) + 0x9E3779B97F4A7C15ull;
    int bd = 0, bs = 0; u32 be = 0;
    if (w < 10) { const int e = b * CHE + (tid < CHE ? tid : 0); bd = dst[e]; bs = src[e]; be = ea[e]; }
    const int g = lane >> 4, j = lane & 15;
    const u32x2 wcv = *(const u32x2*)(wc + slice * 64 + j * 4);
    const int wkr = lane >> 3;
    auto stage_w = [&](const u16* W0, const u16* W1) {
#pragma unroll
        for (int i = 0; i < 4; ++i) {
            const int q = (w & 7) * 4 + i;
            const int hh = ((wkr >> 1) & 1) | ((q & 1) << 1);
            glds16a(w < 8 ? W0 : W1, (u32)((q * 8 + wkr) * 512 + slice * 128 + (((lane & 7) ^ (hh << 1)) << 4)), L_WR + (w >> 3) * 32768 + q * 1024);
        }
    };
    stage_w(wb, wv);
#pragma unroll
    for (int i = 0; i < 5; ++i) {
        const int q = w * 5 + i, r = q * 2 + (lane >> 5);
        int grow = node0 + r; grow = grow < NN ? grow : NN - 1;
        glds16a(x, (u32)(grow * 512 + (((lane & 31) ^ (r & 15)) << 4)), L_XT + q * 1024);
    }
    if (tid < 128) *(u32x4*)(smem + L_ST1 + tid * 16) = (u32x4){~0u, ~0u, ~0u, ~0u};
    else if (tid < 128 + NBK) *(u32*)(smem + L_BC + (tid - 128) * 4) = 0;
    f32x2 w01 = {__uint_as_float(wcv.x << 16) * NLOG2E, __uint_as_float(wcv.x & 0xFFFF0000u) * NLOG2E};
    f32x2 w23 = {__uint_as_float(wcv.y << 16) * NLOG2E, __uint_as_float(wcv.y & 0xFFFF0000u) * NLOG2E};
    u32 brec = (u32)bs | (be << 16);
    u64 tagv = tag;
    asm volatile("" : "+v"(w01), "+v"(w23), "+v"(brec), "+v"(bd), "+v"(tagv));
    WAIT_V(0); WAIT_L(0);
    BAR();
    MARK(1);

    const int strip = w & 3, rgp = w >> 2;
    const u32 trbase = L_WR + (8 * g + (j >> 2)) * 128
                     + (((2 * strip + ((lane & 3) >> 1)) ^ (((((j >> 2) >> 1) & 1) | ((g & 1) << 1)) << 1)) << 4) + (lane & 1) * 8;
    bf16x8 wf0[8], wf1[8];
    TRRD_ALL
    if (w < 10 && tid < CHE) {
        const int bk = (int)(((u32)bd * 53432u) >> 23);
        const u32 pos = atomicAdd((u32*)(smem + L_BC + bk * 4), 1u);
        if (pos < CAP) {
            *(u32*)(smem + L_ST4 + (bk * CAP + pos) * 4) = brec;
            *(unsigned char*)(smem + L_ST1 + bk * CAP + pos) = (unsigned char)(bd - bk * NB);
        }
    }
    WAIT_L(0);
    __builtin_amdgcn_sched_barrier(0);
    BAR();
    {
        const __amdgpu_buffer_rsrc_t r4s = __builtin_amdgcn_make_buffer_rsrc(ws + OFF_R4, 0, NBK * NCH * CAP * 4, 0x00020000);
        const __amdgpu_buffer_rsrc_t r1s = __builtin_amdgcn_make_buffer_rsrc(ws + OFF_R1, 0, NBK * NCH * CAP, 0x00020000);
        if (w < 8) {
            const u32x4 v = *(const u32x4*)(smem + L_ST4 + tid * 16);
            __builtin_amdgcn_raw_buffer_store_b128(v, r4s, ((tid >> 3) * NCH + b) * 128 + (tid & 7) * 16, 0, AUX_SC1);
        } else if (w < 10) {
            const int t = tid - 512, bk = t >> 1;
            u32x4 v = *(const u32x4*)(smem + L_ST1 + t * 16);
            if (*(const u32*)(smem + L_BC + bk * 4) > CAP) v = (u32x4){0xFEFEFEFEu, 0xFEFEFEFEu, 0xFEFEFEFEu, 0xFEFEFEFEu};
            __builtin_amdgcn_raw_buffer_store_b128(v, r1s, (bk * NCH + b) * 32 + (t & 1) * 16, 0, AUX_SC1);
        } else if (w == 10 && bucket == 0) {
            __hip_atomic_store((GLB_AS u32*)(ws + OFF_BV + ((size_t)slice * NP + NN) * 256) + lane, NEUTRAL_BV, __ATOMIC_RELAXED, __HIP_MEMORY_SCOPE_AGENT);
        }
        WAIT_L(0);
    }
    stage_w(wa, wu);
    BAR();

    const int xr = lane & 15;
    const u32 xbase = L_XT + xr * 512 + (((xr & 12) | ((g ^ xr) & 3)) << 4);
    const int ntile = rgp < 2 ? 3 : 2;
    {
        const __amdgpu_buffer_rsrc_t bvs = __builtin_amdgcn_make_buffer_rsrc(ws + OFF_BV + ((size_t)slice * NP + node0) * 256, 0, nvalid * 256, 0x00020000);
        char* const scr = smem + L_FR + w * 1024;
        const u32 swr = xr * 64 + ((g ^ ((xr >> 1) & 3)) << 4);
        const u32 srd = (lane >> 2) * 64 + (((lane & 3) ^ ((lane >> 3) & 3)) << 4);
        const u32 svo = (lane >> 2) * 256 + strip * 64 + (lane & 3) * 16;
#pragma unroll 1
        for (int it = 0; it < ntile; ++it) {
            const int tile = rgp + 4 * it;
            const u32 xa = xbase + tile * 8192;
            f32x4 acc0 = {0.f, 0.f, 0.f, 0.f}, acc1 = {0.f, 0.f, 0.f, 0.f};
#pragma unroll
            for (int s = 0; s < 8; ++s) {
                const bf16x8 xb = *(const bf16x8*)(smem + ((xa ^ ((s & 3) << 6)) + (s >> 2) * 256));
                acc0 = __builtin_amdgcn_mfma_f32_16x16x32_bf16(wf0[s], xb, acc0, 0, 0, 0);
                acc1 = __builtin_amdgcn_mfma_f32_16x16x32_bf16(wf1[s], xb, acc1, 0, 0, 0);
            }
            u32x4 d;
#pragma unroll
            for (int i = 0; i < 4; i += 2) {
                const u32 bb = cvt_pk_bf16(acc0[i] * NLOG2E, acc0[i + 1] * NLOG2E);
#pragma unroll
                for (int k = 0; k < 2; ++k) {
                    const u32 vb = __float_as_uint(acc1[i + k]);
                    const u32 bl = k ? (bb >> 16) : (bb & 0xFFFFu);
                    const u32 m = __float_as_uint(__builtin_fmaxf(__builtin_fabsf(acc1[i + k]), 1.17549435e-38f));
                    const u32 u = m + 0x8000u - bl;
                    d[i + k] = (vb & 0x80000000u) | (u & 0x7FFF0000u) | bl;
                }
            }
            *(u32x4*)(scr + swr) = d;
            const u32x4 v = *(const u32x4*)(scr + srd);
            __builtin_amdgcn_raw_buffer_store_b128(v, bvs, svo + (u32)tile * 4096, 0, AUX_SC1);
        }
        if (rgp < 2) WAIT_V(3); else WAIT_V(2);
    }
    BAR();
    if (tid == 0) __hip_atomic_store((GLB_AS u64*)(ws + OFF_F1) + b, tagv, __ATOMIC_RELAXED, __HIP_MEMORY_SCOPE_AGENT);
    MARK(2);
    TRRD_ALL
    WAIT_L(0);
    __builtin_amdgcn_sched_barrier(0);
    BAR();
    {
#pragma unroll 1
        for (int it = 0; it < ntile; ++it) {
            const int tile = rgp + 4 * it;
            const u32 xa = xbase + tile * 8192;
            f32x4 acc0 = {0.f, 0.f, 0.f, 0.f}, acc1 = {0.f, 0.f, 0.f, 0.f};
#pragma unroll
            for (int s = 0; s < 8; ++s) {
                const bf16x8 xb = *(const bf16x8*)(smem + ((xa ^ ((s & 3) << 6)) + (s >> 2) * 256));
                acc0 = __builtin_amdgcn_mfma_f32_16x16x32_bf16(wf0[s], xb, acc0, 0, 0, 0);
                acc1 = __builtin_amdgcn_mfma_f32_16x16x32_bf16(wf1[s], xb, acc1, 0, 0, 0);
            }
            u32x4 d;
#pragma unroll
            for (int i = 0; i < 4; ++i) d[i] = cvt_pk_bf16(acc0[i] * NLOG2E, acc1[i]);
            *(u32x4*)(smem + L_AU + (tile * 16 + xr) * 256 + (((strip * 4 + g) ^ xr) << 4)) = d;
        }
#pragma unroll
        for (int i = 0; i < 2; ++i) {
            const int q = tid + 1024 * i;
            if (q < 1280) {
                const int r = q >> 3, c8 = q & 7;
                const u32x4 v = *(const u32x4*)(smem + L_XT + r * 512 + (((slice * 8 + c8) ^ (r & 15)) << 4));
                *(u32x2*)(smem + L_XS + r * 128 + (((2 * c8) ^ (r & 15)) << 3)) = (u32x2){v.x, v.y};
                *(u32x2*)(smem + L_XS + r * 128 + (((2 * c8 + 1) ^ (r & 15)) << 3)) = (u32x2){v.z, v.w};
            }
        }
    }
    if (w == 15) wait_flags(ws + OFF_F1, tag, lane);
    WAIT_V(0); WAIT_L(0);
    BAR();
    if (tid == 0) __hip_atomic_store((GLB_AS u64*)(ws + OFF_F2) + b, tagv, __ATOMIC_RELAXED, __HIP_MEMORY_SCOPE_AGENT);
    MARK(3);

    u32x4 r4a, r4b; u32x2 r1;
    asm volatile("global_load_dwordx4 %0, %3, %4 sc1\n\tglobal_load_dwordx4 %1, %3, %4 offset:16 sc1\n\tglobal_load_dwordx2 %2, %5, %6 sc1"
                 : "=&v"(r4a), "=&v"(r4b), "=&v"(r1)
                 : "v"(tid * 32), "s"(ws + OFF_R4 + (size_t)bucket * NCH * CAP * 4), "v"(tid * 8), "s"(ws + OFF_R1 + (size_t)bucket * NCH * CAP) : "memory");
    u32* const cnt = (u32*)(smem + L_CNT);
    u32* const hist = (u32*)(smem + L_HIST);
    u32* const misc = (u32*)(smem + L_MISC);
    for (int i = tid; i < LSZ / 16; i += 1024) {
        *(u32x4*)(smem + L_LIDX + i * 16) = (u32x4){NEUTRAL_REC, NEUTRAL_REC, NEUTRAL_REC, NEUTRAL_REC};
        *(u32x4*)(smem + L_LE + i * 16) = (u32x4){0, 0, 0, 0};
    }
    if (tid < 160 + 64) cnt[tid] = 0;
    if (tid == 0) { misc[0] = 16; misc[1] = 0; }
    asm volatile("s_waitcnt vmcnt(0) lgkmcnt(0)" : "+v"(r4a), "+v"(r4b), "+v"(r1) :: "memory");
    __builtin_amdgcn_sched_barrier(0);
    BAR();
    MARK(4);
    {
        const u32 rec[8] = {r4a.x, r4a.y, r4a.z, r4a.w, r4b.x, r4b.y, r4b.z, r4b.w};
        u32 dl[8], slot[8];
        bool ovf = false;
#pragma unroll
        for (int k = 0; k < 8; ++k) { dl[k] = ((k < 4 ? r1.x : r1.y) >> (8 * (k & 3))) & 0xFF; ovf |= dl[k] == 0xFE; }
#pragma unroll
        for (int k = 0; k < 8; ++k) slot[k] = atomicAdd(&cnt[dl[k] < (u32)NB ? dl[k] : 224 + lane], 1u);
#pragma unroll
        for (int k = 0; k < 8; ++k) {
            const bool v = dl[k] < (u32)NB, ok = v && slot[k] < CAPN;
            ovf |= v && !ok;
            const u32 o = ok ? dl[k] * LSTB + slot[k] * 4 : TRASH + lane * 4;
            *(u32*)(smem + L_LIDX + o) = rec[k] & 0xFFFFu;
            *(u32*)(smem + L_LE + o) = rec[k] & 0xFFFF0000u;
        }
        if (ovf) misc[1] = 1;
    }
    WAIT_L(0); BAR();
    const bool slow = misc[1] != 0;
    u32 mydeg = 0, myrank = 0;
    if (tid < nvalid) { mydeg = cnt[tid]; mydeg = mydeg < 63 ? mydeg : 63; myrank = atomicAdd(&hist[63 - mydeg], 1u); }
    WAIT_L(0); BAR();
    if (w < 3) {
        const u32 h0 = hist[lane];
        u32 v = h0;
        v = dpp_add<0x111, 0xf>(v); v = dpp_add<0x112, 0xf>(v); v = dpp_add<0x114, 0xf>(v); v = dpp_add<0x118, 0xf>(v);
        v = dpp_add<0x142, 0xa>(v); v = dpp_add<0x143, 0xc>(v);
        const u32 base = (u32)__builtin_amdgcn_ds_bpermute((int)((63 - mydeg) * 4), (int)(v - h0));
        if (tid < nvalid) {
            const u32 pos = base + myrank;
            const u32x2 ent = {(u32)(L_LIDX + tid * LSTB) | (mydeg << 24), (u32)(tid * 256) | (u32)((tid & 15) << 4)};
            u32x2* o = (u32x2*)(smem + L_ORD) + pos;
            o[0] = ent;
            if (pos == (u32)nvalid - 1) { o[1] = ent; o[2] = ent; o[3] = ent; }
        }
    }
    if (w == 15) wait_flags(ws + OFF_F2, tag, lane);
    WAIT_L(0); BAR();
    if (b == 0 && tid == 0) __hip_atomic_store((GLB_AS u64*)(ws + OFF_G), tagv, __ATOMIC_RELAXED, __HIP_MEMORY_SCOPE_AGENT);
    MARK(5);

    const char* const bvb = ws + OFF_BV + (size_t)slice * NP * 256;
    const u32 joff = j * 16;
    if (slow) {
        __builtin_amdgcn_fence(__ATOMIC_ACQUIRE, "agent");
        u32 sel = 0x00003F80u;
        asm volatile("" : "+s"(sel));
        for (int task = w; task * 4 < nvalid; task += 16) {
            const int n = task * 4 + g;
            const bool nv = n < nvalid;
            const int nl = nv ? n : 0;
            const int node = node0 + nl;
            const u32x4 au = *(const u32x4*)(smem + L_AU + nl * 256 + ((j ^ (nl & 15)) << 4));
            const u32x2 xv = *(const u32x2*)(smem + L_XS + nl * 128 + ((j ^ (nl & 15)) << 3));
            const f32x2 a01 = {__uint_as_float(au.x << 16), __uint_as_float(au.y << 16)};
            const f32x2 a23 = {__uint_as_float(au.z << 16), __uint_as_float(au.w << 16)};
            Acc acc = {{0.f, 0.f}, {0.f, 0.f}, {0.f, 0.f}, {0.f, 0.f}};
            const int lo = node0 + task * 4;
            for (int e0 = 0; e0 < NE; e0 += 64) {
                const int d = dst[e0 + lane];
                unsigned long long m = __ballot((u32)(d - lo) < 4u);
                if (m) {
                    const u32 rc = (u32)src[e0 + lane] | ((u32)ea[e0 + lane] << 16);
                    while (m) {
                        const int l = __builtin_ctzll(m); m &= m - 1;
                        const int dd = __builtin_amdgcn_readlane(d, l);
                        const u32 rr = __builtin_amdgcn_readlane(rc, l);
                        const u32 rec = (nv && dd == node) ? rr : NEUTRAL_REC;
                        const u32x4 dv = *(const u32x4*)(bvb + (size_t)((rec & 0xFFFFu) * 256 + joff));
                        edge_step(acc, dv, rec, a01, a23, w01, w23, sel);
                    }
                }
            }
            if (nv) node_finish(acc, au, xv, out + (size_t)node * DD + slice * 64 + j * 4);
        }
        return;
    }

    const int ntask = (nvalid + 3) >> 2;
    if (w < ntask) {
        u32x4 srdBV, srdO;
        { const size_t p = (size_t)bvb; srdBV = (u32x4){(u32)p, (u32)(p >> 32) | (256u << 16), 0xFFFFFFFFu, 0x00020000u}; }
        { const size_t ob = (size_t)node0 * 512 + slice * 128; const size_t p = (size_t)out + ob; srdO = (u32x4){(u32)p, (u32)(p >> 32), (u32)((size_t)NN * 512 - ob), 0x00020000u}; }
        u32 g8 = L_ORD + g * 8, ctr = L_MISC, one = 1, j8 = j * 8;
        unsigned long long eps2 = 0x358637BD358637BDull;
        u32 s_cnt, s_nxt, s_t, s_tc;
#define STAGE(DS0, DS1, DS2, DS3, DSL, DSH, IX, EP, E1, DN, IXN, O1, O2) \
            "s_waitcnt lgkmcnt(0)\n\t" \
            "buffer_load_dwordx4 " DN ", " IXN ", %[bv], 0 idxen offen sc1\n\t" \
            "v_pk_fma_f32 v[112:113], " EP ", v[100:101], v[96:97] op_sel_hi:[0,1,1]\n\t" \
            "v_pk_fma_f32 v[114:115], " EP ", v[102:103], v[98:99] op_sel_hi:[0,1,1]\n\t" \
            "ds_read_b32 " IX ", v89 offset:" O1 "\n\t" \
            "ds_read_b32 " E1 ", v89 offset:" O2 "\n\t" \
            "s_waitcnt vmcnt(3)\n\t" \
            "v_dot2c_f32_bf16 v112, %[slo], " DS0 "\n\t" \
            "v_dot2c_f32_bf16 v113, %[slo], " DS1 "\n\t" \
            "v_dot2c_f32_bf16 v114, %[slo], " DS2 "\n\t" \
            "v_dot2c_f32_bf16 v115, %[slo], " DS3 "\n\t" \
            "v_exp_f32 v112, v112\n\t" \
            "v_exp_f32 v113, v113\n\t" \
            "v_exp_f32 v114, v114\n\t" \
            "v_exp_f32 v115, v115\n\t" \
            "v_pk_add_f32 v[112:113], v[112:113], 1.0 op_sel_hi:[1,0]\n\t" \
            "v_pk_add_f32 v[114:115], v[114:115], 1.0 op_sel_hi:[1,0]\n\t" \
            "v_rcp_f32 v112, v112\n\t" \
            "v_rcp_f32 v113, v113\n\t" \
            "v_rcp_f32 v114, v114\n\t" \
            "v_rcp_f32 v115, v115\n\t" \
            "s_sub_u32 %[cnt], %[cnt], 1\n\t" \
            "v_pk_add_f32 v[108:109], v[108:109], v[112:113]\n\t" \
            "v_pk_fma_f32 v[104:105], v[112:113], " DSL ", v[104:105]\n\t" \
            "v_pk_add_f32 v[110:111], v[110:111], v[114:115]\n\t" \
            "v_pk_fma_f32 v[106:107], v[114:115], " DSH ", v[106:107]\n\t" \
            "s_cmp_eq_u32 %[cnt], 0\n\t"
#define PRIO_INIT "s_setprio 3\n\t"
#define PRIO_STEP \
            "s_add_u32 %[tc], %[tc], 1\n\t" \
            "s_cmp_lg_u32 %[tc], 1\n\t s_cbranch_scc1 L_p1%=\n\t s_setprio 2\n\t L_p1%=:\n\t" \
            "s_cmp_lg_u32 %[tc], 2\n\t s_cbranch_scc1 L_p2%=\n\t s_setprio 1\n\t L_p2%=:\n\t" \
            "s_cmp_lg_u32 %[tc], 3\n\t s_cbranch_scc1 L_p3%=\n\t s_setprio 0\n\t L_p3%=:\n\t"
        asm volatile(
            PRIO_INIT
            "s_mov_b32 %[tc], 0\n\t"
            "s_lshl_b32 %[t], %[task], 5\n\t"
            "v_add_u32 v127, %[t], v91\n\t"
            "ds_read_b64 v[122:123], v127\n\t"
            "s_mov_b64 exec, 1\n\t"
            "ds_add_rtn_u32 v125, v93, v95\n\t"
            "s_mov_b64 exec, -1\n\t"
            "L_task%=:\n\t"
            "s_waitcnt lgkmcnt(0)\n\t"
            "v_readfirstlane_b32 %[nxt], v125\n\t"
            "v_readfirstlane_b32 %[cnt], v122\n\t"
            "v_and_b32 v89, 0xffffff, v122\n\t"
            "v_and_b32 v127, 0xffffff00, v123\n\t"
            "v_xor_b32 v112, v123, v81\n\t"
            "ds_read_b32 v80, v89\n\t"
            "ds_read_b32 v82, v89 offset:4\n\t"
            "ds_read_b32 v84, v89 offset:8\n\t"
            "ds_read_b32 v86, v89 offset:12\n\t"
            "ds_read_b32 v88, v89 offset:%c[le0]\n\t"
            "ds_read_b32 v90, v89 offset:%c[le1]\n\t"
            "ds_read_b32 v92, v89 offset:%c[le2]\n\t"
            "ds_read_b32 v94, v89 offset:%c[le3]\n\t"
            "v_lshl_add_u32 v124, v127, 1, v126\n\t"
            "v_lshrrev_b32 v113, 1, v112\n\t"
            "ds_read_b128 v[116:119], v112 offset:%c[au]\n\t"
            "ds_read_b64 v[120:121], v113 offset:%c[xs]\n\t"
            "s_lshr_b32 %[cnt], %[cnt], 24\n\t"
            "s_lshl_b32 %[t], %[nxt], 5\n\t"
            "v_add_u32 v127, %[t], v91\n\t"
            "ds_read_b64 v[122:123], v127\n\t"
            "s_mov_b64 exec, 1\n\t"
            "ds_add_rtn_u32 v125, v93, v95\n\t"
            "s_mov_b64 exec, -1\n\t"
            "s_waitcnt lgkmcnt(9)\n\t"
            "buffer_load_dwordx4 v[64:67], v[80:81], %[bv], 0 idxen offen sc1\n\t"
            "buffer_load_dwordx4 v[68:71], v[82:83], %[bv], 0 idxen offen sc1\n\t"
            "buffer_load_dwordx4 v[72:75], v[84:85], %[bv], 0 idxen offen sc1\n\t"
            "v_mov_b64 v[104:105], 0\n\t"
            "v_mov_b64 v[106:107], 0\n\t"
            "v_mov_b64 v[108:109], 0\n\t"
            "v_mov_b64 v[110:111], 0\n\t"
            "s_waitcnt lgkmcnt(2)\n\t"
            "v_lshlrev_b32 v96, 16, v116\n\t"
            "v_lshlrev_b32 v97, 16, v117\n\t"
            "v_lshlrev_b32 v98, 16, v118\n\t"
            "v_lshlrev_b32 v99, 16, v119\n\t"
            "s_cmp_eq_u32 %[cnt], 0\n\t"
            "s_cbranch_scc1 L_fin%=\n\t"
            "L_loop%=:\n\t"
            STAGE("v64", "v65", "v66", "v67", "v[64:65]", "v[66:67]", "v80", "v[88:89]", "v88", "v[76:79]", "v[86:87]", "16", "%c[le4]")
            "s_cbranch_scc1 L_fin%=\n\t"
            STAGE("v68", "v69", "v70", "v71", "v[68:69]", "v[70:71]", "v82", "v[90:91]", "v90", "v[64:67]", "v[80:81]", "20", "%c[le5]")
            "s_cbranch_scc1 L_fin%=\n\t"
            STAGE("v72", "v73", "v74", "v75", "v[72:73]", "v[74:75]", "v84", "v[92:93]", "v92", "v[68:71]", "v[82:83]", "24", "%c[le6]")
            "s_cbranch_scc1 L_fin%=\n\t"
            STAGE("v76", "v77", "v78", "v79", "v[76:77]", "v[78:79]", "v86", "v[94:95]", "v94", "v[72:75]", "v[84:85]", "28", "%c[le7]")
            "v_add_u32 v89, 16, v89\n\t"
            "s_cbranch_scc0 L_loop%=\n\t"
            "L_fin%=:\n\t"
            "v_pk_add_f32 v[108:109], v[108:109], %[eps]\n\t"
            "v_pk_add_f32 v[110:111], v[110:111], %[eps]\n\t"
            "v_rcp_f32 v108, v108\n\t"
            "v_rcp_f32 v109, v109\n\t"
            "v_rcp_f32 v110, v110\n\t"
            "v_rcp_f32 v111, v111\n\t"
            "v_pk_mul_f32 v[104:105], v[104:105], v[108:109]\n\t"
            "v_pk_mul_f32 v[106:107], v[106:107], v[110:111]\n\t"
            "v_dot2c_f32_bf16 v104, %[shi], v116\n\t"
            "v_dot2c_f32_bf16 v105, %[shi], v117\n\t"
            "v_dot2c_f32_bf16 v106, %[shi], v118\n\t"
            "v_dot2c_f32_bf16 v107, %[shi], v119\n\t"
            "v_maximum3_f32 v104, v104, 0, 0\n\t"
            "v_maximum3_f32 v105, v105, 0, 0\n\t"
            "v_maximum3_f32 v106, v106, 0, 0\n\t"
            "v_maximum3_f32 v107, v107, 0, 0\n\t"
            "v_dot2c_f32_bf16 v104, %[slo], v120\n\t"
            "v_dot2c_f32_bf16 v105, %[shi], v120\n\t"
            "v_dot2c_f32_bf16 v106, %[slo], v121\n\t"
            "v_dot2c_f32_bf16 v107, %[shi], v121\n\t"
            "s_nop 1\n\t"
            "v_cvt_pk_bf16_f32 v112, v104, v105\n\t"
            "v_cvt_pk_bf16_f32 v113, v106, v107\n\t"
            "buffer_store_dwordx2 v[112:113], v124, %[o], 0 offen\n\t"
            PRIO_STEP
            "s_cmp_lt_u32 %[nxt], %[ntask]\n\t"
            "s_cbranch_scc1 L_task%=\n\t"
            "s_waitcnt vmcnt(0) lgkmcnt(0)"
            : [cnt] "=&s"(s_cnt), [nxt] "=&s"(s_nxt), [t] "=&s"(s_t), [tc] "=&s"(s_tc)
            : "{v81}"(joff), "{v83}"(joff), "{v85}"(joff), "{v87}"(joff), "{v91}"(g8), "{v93}"(ctr), "{v95}"(one), "{v126}"(j8),
              "{v[100:101]}"(w01), "{v[102:103]}"(w23),
              [task] "s"(w), [ntask] "s"(ntask), [bv] "s"(srdBV), [o] "s"(srdO), [slo] "s"(0x00003F80u), [shi] "s"(0x3F800000u), [eps] "s"(eps2),
              [au] "i"(L_AU), [xs] "i"(L_XS), [le0] "i"(LSZ), [le1] "i"(LSZ + 4), [le2] "i"(LSZ + 8), [le3] "i"(LSZ + 12),
              [le4] "i"(LSZ + 16), [le5] "i"(LSZ + 20), [le6] "i"(LSZ + 24), [le7] "i"(LSZ + 28)
            : "v64", "v65", "v66", "v67", "v68", "v69", "v70", "v71", "v72", "v73", "v74", "v75", "v76", "v77", "v78", "v79",
              "v80", "v82", "v84", "v86", "v88", "v89", "v90", "v92", "v94", "v96", "v97", "v98", "v99",
              "v104", "v105", "v106", "v107", "v108", "v109", "v110", "v111", "v112", "v113", "v114", "v115",
              "v116", "v117", "v118", "v119", "v120", "v121", "v122", "v123", "v124", "v125", "v127", "scc", "memory");
#undef STAGE
    }
    MARK(6);
    WAIT_V(0); BAR();
    if (tid == 0) __hip_atomic_store((GLB_AS u64*)(ws + OFF_G + 4096) + b, tagv, __ATOMIC_RELAXED, __HIP_MEMORY_SCOPE_AGENT);
    if (b == 0 && w == 0) {
        wait_flags(ws + OFF_G + 4096, tag, lane);
        const GLB_AS u64* mk = (const GLB_AS u64*)(ws + (200u << 20));
        u64 t0 = ~0ull, a = 0;
        for (int i = lane; i < 4096; i += 64) {
            const u64 s0 = __hip_atomic_load(mk + i, __ATOMIC_RELAXED, __HIP_MEMORY_SCOPE_AGENT), s1 = __hip_atomic_load(mk + MEASURE * 4096 + i, __ATOMIC_RELAXED, __HIP_MEMORY_SCOPE_AGENT);
            t0 = s0 < t0 ? s0 : t0; a = s1 > a ? s1 : a; }
        for (int o = 32; o; o >>= 1) { u64 t = __shfl_xor(t0, o); t0 = t < t0 ? t : t0; t = __shfl_xor(a, o); a = t > a ? t : a; }
        const u64 val = MEASURE == 0 ? 0 : a - t0;
        const u64 q0 = __builtin_amdgcn_s_memrealtime();
        while (__builtin_amdgcn_s_memrealtime() - q0 < val * 10ull) __builtin_amdgcn_s_sleep(2);
    }
}

extern "C" void kernel_launch(void* const* d_in, const int* in_sizes, int n_in,
                              void* d_out, int out_size, void* d_ws, size_t ws_size,
                              hipStream_t stream) {
    const u16* x = (const u16*)d_in[0];
    const u16* ea = (const u16*)d_in[1];
    const int* src = (const int*)d_in[2];
    const int* dst = (const int*)d_in[3];
    const u16* wa = (const u16*)d_in[4];
    const u16* wb = (const u16*)d_in[5];
    const u16* wc = (const u16*)d_in[6];
    const u16* wu = (const u16*)d_in[7];
    const u16* wv = (const u16*)d_in[8];
    (void)hipFuncSetAttribute((const void*)gatedgcn_fused, hipFuncAttributeMaxDynamicSharedMemorySize, L_END);
    gatedgcn_fused<<<256, 1024, L_END, stream>>>(x, ea, src, dst, wa, wb, wc, wu, wv, (char*)d_ws, (u16*)d_out);
}
```

```cpp
#include <hip/hip_runtime.h>
#include <hip/hip_bf16.h>
#include <stdint.h>

typedef unsigned short u16;
typedef unsigned int u32;
typedef unsigned long long u64;
typedef u32 u32x2 __attribute__((ext_vector_type(2)));
typedef u32 u32x4 __attribute__((ext_vector_type(4)));
typedef float f32x2 __attribute__((ext_vector_type(2)));
typedef float f32x4 __attribute__((ext_vector_type(4)));
typedef __bf16 bf16x8 __attribute__((ext_vector_type(8)));
typedef __bf16 bf16x2 __attribute__((ext_vector_type(2)));

constexpr int NN = 10000;
constexpr int NE = 160000;
constexpr int DD = 256;
constexpr int NP = 10048;
constexpr int NBK = 64;
constexpr int NB = 157;
constexpr int NCH = 256;
constexpr int CHE = 625;
constexpr int CAP = 32;
constexpr int CAPN = 44;
constexpr int LST = 45;
constexpr float NLOG2E = -1.4426950408889634f;
constexpr u32 NEUTRAL_REC = (u32)NN;
constexpr u32 NEUTRAL_BV = 0x00007F00u;

constexpr size_t OFF_BV = 0;
constexpr size_t OFF_R4 = OFF_BV + (size_t)4 * NP * 256;
constexpr size_t OFF_R1 = OFF_R4 + (size_t)NBK * NCH * CAP * 4;
constexpr size_t OFF_F1 = OFF_R1 + (size_t)NBK * NCH * CAP;
constexpr size_t OFF_F2 = OFF_F1 + 2048;
constexpr size_t OFF_G = OFF_F2 + 2048;

#define LDS_AS __attribute__((address_space(3)))
#define GLB_AS __attribute__((address_space(1)))

__device__ __forceinline__ void glds16a(const void* sbase, u32 voff, u32 lds) {
    u32 keep;
    asm volatile("s_mov_b32 %0, m0\n\ts_mov_b32 m0, %3\n\ts_nop 0\n\tglobal_load_lds_dwordx4 %1, %2\n\ts_mov_b32 m0, %0" : "=&s"(keep) : "v"(voff), "s"(sbase), "s"(lds) : "memory");
}
__device__ __forceinline__ u32 cvt_pk_bf16(float lo, float hi) {
    const f32x2 v = {lo, hi}; return __builtin_bit_cast(u32, __builtin_convertvector(v, bf16x2));
}
#define WAIT_V(n) asm volatile("s_waitcnt vmcnt(" #n ")" ::: "memory")
#define WAIT_L(n) asm volatile("s_waitcnt lgkmcnt(" #n ")" ::: "memory")
#define BAR() __builtin_amdgcn_s_barrier()
#define AUX_SC1 16

#define MARK(I)

constexpr int L_WR = 0;
constexpr int L_AU = 0;
constexpr int L_XS = 40960;
constexpr int L_XT = 65536;
constexpr int L_FR = L_XT + 81920;
constexpr int L_ST4 = L_FR, L_ST1 = L_FR + 8192, L_BC = L_FR + 10240;
constexpr int L_END = 163840;
constexpr int LSTB = LST * 4;
constexpr int L_LIDX = L_XT;
constexpr int LSZ = (NB * LSTB + 32 + 256 + 15) / 16 * 16;
constexpr int L_LE = L_LIDX + LSZ;
constexpr int L_CNT = L_LE + LSZ;
constexpr int L_HIST = L_CNT + 160 * 4;
constexpr int L_ORD = L_HIST + 128 * 4;
constexpr int L_MISC = L_ORD + 80 * 32;
constexpr int TRASH = NB * LSTB + 32;
static_assert(L_MISC + 16 <= L_FR, "lds");
static_assert(TRASH + 256 <= LSZ && LSZ < 65536 - 64, "lists");

__device__ __forceinline__ float dot2sel(u32 d, u32 sel, float c) {
    return __builtin_amdgcn_fdot2_f32_bf16(__builtin_bit_cast(bf16x2, d), __builtin_bit_cast(bf16x2, sel), c, false);
}
struct Acc { f32x2 n01, n23, d01, d23; };
__device__ __forceinline__ void edge_step(Acc& a, const u32x4 d, const u32 rec, const f32x2 a01, const f32x2 a23, const f32x2 w01, const f32x2 w23, const u32 sel) {
    const float e = __uint_as_float(rec & 0xFFFF0000u);
    const f32x2 ee = {e, e};
    const f32x2 t01 = __builtin_elementwise_fma(ee, w01, a01);
    const f32x2 t23 = __builtin_elementwise_fma(ee, w23, a23);
    f32x2 E01, E23;
    E01.x = __builtin_amdgcn_exp2f(dot2sel(d.x, sel, t01.x));
    E01.y = __builtin_amdgcn_exp2f(dot2sel(d.y, sel, t01.y));
    E23.x = __builtin_amdgcn_exp2f(dot2sel(d.z, sel, t23.x));
    E23.y = __builtin_amdgcn_exp2f(dot2sel(d.w, sel, t23.y));
    const f32x2 one = {1.f, 1.f};
    E01 += one; E23 += one;
    f32x2 s01, s23;
    s01.x = __builtin_amdgcn_rcpf(E01.x); s01.y = __builtin_amdgcn_rcpf(E01.y);
    s23.x = __builtin_amdgcn_rcpf(E23.x); s23.y = __builtin_amdgcn_rcpf(E23.y);
    a.d01 += s01; a.d23 += s23;
    const f32x2 v01 = {__uint_as_float(d.x), __uint_as_float(d.y)};
    const f32x2 v23 = {__uint_as_float(d.z), __uint_as_float(d.w)};
    a.n01 = __builtin_elementwise_fma(s01, v01, a.n01);
    a.n23 = __builtin_elementwise_fma(s23, v23, a.n23);
}
__device__ __forceinline__ void node_finish(const Acc& a, const u32x4 au, const u32x2 xv, u16* outp) {
    const float num[4] = {a.n01.x, a.n01.y, a.n23.x, a.n23.y};
    const float den[4] = {a.d01.x, a.d01.y, a.d23.x, a.d23.y};
    const u32 aus[4] = {au.x, au.y, au.z, au.w};
    const float xs[4] = {__uint_as_float(xv.x << 16), __uint_as_float(xv.x & 0xFFFF0000u), __uint_as_float(xv.y << 16), __uint_as_float(xv.y & 0xFFFF0000u)};
    float o[4];
#pragma unroll
    for (int c = 0; c < 4; ++c) {
        const float z = __uint_as_float(aus[c] & 0xFFFF0000u) + num[c] * __builtin_amdgcn_rcpf(den[c] + 1e-6f);
        const float rl = z > 0.f ? z : 0.f * z;
        o[c] = xs[c] + rl;
    }
    u32x2 ov; ov.x = cvt_pk_bf16(o[0], o[1]); ov.y = cvt_pk_bf16(o[2], o[3]);
    *(u32x2*)outp = ov;
}
template <int CTRL, int RM> __device__ __forceinline__ u32 dpp_add(u32 v) {
    return v + (u32)__builtin_amdgcn_update_dpp(0, (int)v, CTRL, RM, 0xf, false);
}

__device__ __forceinline__ void wait_flags(const char* flags, u64 tag, int lane) {
    const u32 lo = (u32)tag, hi = (u32)(tag >> 32);
    for (int spins = 0; spins < (1 << 21); ++spins) {
        u32x4 a, b;
        asm volatile("global_load_dwordx4 %0, %2, %3 sc1\n\tglobal_load_dwordx4 %1, %2, %3 offset:16 sc1\n\ts_waitcnt vmcnt(0)"
                     : "=&v"(a), "=&v"(b) : "v"(lane * 32), "s"(flags) : "memory");
        const bool ok = a.x == lo && a.y == hi && a.z == lo && a.w == hi && b.x == lo && b.y == hi && b.z == lo && b.w == hi;
        if (__all(ok)) break;
        __builtin_amdgcn_s_sleep(1);
    }
}

#define TRRD(S) { u32x2 a0, a1, b0, b1; \
        asm volatile("ds_read_b64_tr_b16 %0, %4 offset:%c5\n\tds_read_b64_tr_b16 %1, %4 offset:%c6\n\t" \
                     "ds_read_b64_tr_b16 %2, %4 offset:%c7\n\tds_read_b64_tr_b16 %3, %4 offset:%c8" \
                     : "=&v"(a0), "=&v"(a1), "=&v"(b0), "=&v"(b1) \
                     : "v"(trbase), "i"((S) * 4096), "i"((S) * 4096 + 512), "i"(32768 + (S) * 4096), "i"(32768 + (S) * 4096 + 512) : "memory"); \
        wf0[S] = __builtin_bit_cast(bf16x8, __builtin_shufflevector(a0, a1, 0, 1, 2, 3)); \
        wf1[S] = __builtin_bit_cast(bf16x8, __builtin_shufflevector(b0, b1, 0, 1, 2, 3)); }
#define TRRD_ALL TRRD(0) TRRD(1) TRRD(2) TRRD(3) TRRD(4) TRRD(5) TRRD(6) TRRD(7)

__global__ __launch_bounds__(1024, 4) void gatedgcn_fused(
    const u16* __restrict__ x, const u16* __restrict__ ea, const int* __restrict__ src, const int* __restrict__ dst,
    const u16* __restrict__ wa, const u16* __restrict__ wb, const u16* __restrict__ wc, const u16* __restrict__ wu, const u16* __restrict__ wv,
    char* __restrict__ ws, u16* __restrict__ out) {
    extern __shared__ __attribute__((aligned(16))) char smem[];
    const int tid = threadIdx.x, lane = tid & 63;
    const int w = __builtin_amdgcn_readfirstlane(tid >> 6);
    const int b = blockIdx.x;
    const int slice = (b & 7) >> 1;
    const int bucket = (b >> 3) * 2 + (b & 1);
    const int node0 = bucket * NB;
    const int nvalid = (NN - node0) < NB ? (NN - node0) : NB;
    MARK(0);
    asm volatile("" :: "s"(x), "s"(ea), "s"(src), "s"(dst), "s"(wa), "s"(wb), "s"(wc), "s"(wu), "s"(wv), "s"(ws), "s"(out));

    const u64 tag = __hip_atomic_load((const GLB_AS u64*)(ws + OFF_G), __ATOMIC_RELAXED, __HIP_MEMORY_SCOPE_AGENT) + 0x9E3779B97F4A7C15ull;
    int bd = 0, bs = 0; u32 be = 0;
    if (w < 10) { const int e = b * CHE + (tid < CHE ? tid : 0); bd = dst[e]; bs = src[e]; be = ea[e]; }
    const int g = lane >> 4, j = lane & 15;
    const u32x2 wcv = *(const u32x2*)(wc + slice * 64 + j * 4);
    const int wkr = lane >> 3;
    auto stage_w = [&](const u16* W0, const u16* W1) {
#pragma unroll
        for (int i = 0; i < 4; ++i) {
            const int q = (w & 7) * 4 + i;
            const int hh = ((wkr >> 1) & 1) | ((q & 1) << 1);
            glds16a(w < 8 ? W0 : W1, (u32)((q * 8 + wkr) * 512 + slice * 128 + (((lane & 7) ^ (hh << 1)) << 4)), L_WR + (w >> 3) * 32768 + q * 1024);
        }
    };
    stage_w(wb, wv);
#pragma unroll
    for (int i = 0; i < 5; ++i) {
        const int q = w * 5 + i, r = q * 2 + (lane >> 5);
        int grow = node0 + r; grow = grow < NN ? grow : NN - 1;
        glds16a(x, (u32)(grow * 512 + (((lane & 31) ^ (r & 15)) << 4)), L_XT + q * 1024);
    }
    if (tid < 128) *(u32x4*)(smem + L_ST1 + tid * 16) = (u32x4){~0u, ~0u, ~0u, ~0u};
    else if (tid < 128 + NBK) *(u32*)(smem + L_BC + (tid - 128) * 4) = 0;
    f32x2 w01 = {__uint_as_float(wcv.x << 16) * NLOG2E, __uint_as_float(wcv.x & 0xFFFF0000u) * NLOG2E};
    f32x2 w23 = {__uint_as_float(wcv.y << 16) * NLOG2E, __uint_as_float(wcv.y & 0xFFFF0000u) * NLOG2E};
    u32 brec = (u32)bs | (be << 16);
    u64 tagv = tag;
    asm volatile("" : "+v"(w01), "+v"(w23), "+v"(brec), "+v"(bd), "+v"(tagv));
    WAIT_V(0); WAIT_L(0);
    BAR();
    MARK(1);

    const int strip = w & 3, rgp = w >> 2;
    const u32 trbase = L_WR + (8 * g + (j >> 2)) * 128
                     + (((2 * strip + ((lane & 3) >> 1)) ^ (((((j >> 2) >> 1) & 1) | ((g & 1) << 1)) << 1)) << 4) + (lane & 1) * 8;
    bf16x8 wf0[8], wf1[8];
    TRRD_ALL
    if (w < 10 && tid < CHE) {
        const int bk = (int)(((u32)bd * 53432u) >> 23);
        const u32 pos = atomicAdd((u32*)(smem + L_BC + bk * 4), 1u);
        if (pos < CAP) {
            *(u32*)(smem + L_ST4 + (bk * CAP + pos) * 4) = brec;
            *(unsigned char*)(smem + L_ST1 + bk * CAP + pos) = (unsigned char)(bd - bk * NB);
        }
    }
    WAIT_L(0);
    __builtin_amdgcn_sched_barrier(0);
    BAR();
    {
        const __amdgpu_buffer_rsrc_t r4s = __builtin_amdgcn_make_buffer_rsrc(ws + OFF_R4, 0, NBK * NCH * CAP * 4, 0x00020000);
        const __amdgpu_buffer_rsrc_t r1s = __builtin_amdgcn_make_buffer_rsrc(ws + OFF_R1, 0, NBK * NCH * CAP, 0x00020000);
        if (w < 8) {
            const u32x4 v = *(const u32x4*)(smem + L_ST4 + tid * 16);
            __builtin_amdgcn_raw_buffer_store_b128(v, r4s, ((tid >> 3) * NCH + b) * 128 + (tid & 7) * 16, 0, AUX_SC1);
        } else if (w < 10) {
            const int t = tid - 512, bk = t >> 1;
            u32x4 v = *(const u32x4*)(smem + L_ST1 + t * 16);
            if (*(const u32*)(smem + L_BC + bk * 4) > CAP) v = (u32x4){0xFEFEFEFEu, 0xFEFEFEFEu, 0xFEFEFEFEu, 0xFEFEFEFEu};
            __builtin_amdgcn_raw_buffer_store_b128(v, r1s, (bk * NCH + b) * 32 + (t & 1) * 16, 0, AUX_SC1);
        } else if (w == 10 && bucket == 0) {
            __hip_atomic_store((GLB_AS u32*)(ws + OFF_BV + ((size_t)slice * NP + NN) * 256) + lane, NEUTRAL_BV, __ATOMIC_RELAXED, __HIP_MEMORY_SCOPE_AGENT);
        }
        WAIT_L(0);
    }
    stage_w(wa, wu);
    BAR();

    const int xr = lane & 15;
    const u32 xbase = L_XT + xr * 512 + (((xr & 12) | ((g ^ xr) & 3)) << 4);
    const int ntile = rgp < 2 ? 3 : 2;
    {
        const __amdgpu_buffer_rsrc_t bvs = __builtin_amdgcn_make_buffer_rsrc(ws + OFF_BV + ((size_t)slice * NP + node0) * 256, 0, nvalid * 256, 0x00020000);
        char* const scr = smem + L_FR + w * 1024;
        const u32 swr = xr * 64 + ((g ^ ((xr >> 1) & 3)) << 4);
        const u32 srd = (lane >> 2) * 64 + (((lane & 3) ^ ((lane >> 3) & 3)) << 4);
        const u32 svo = (lane >> 2) * 256 + strip * 64 + (lane & 3) * 16;
#pragma unroll 1
        for (int it = 0; it < ntile; ++it) {
            const int tile = rgp + 4 * it;
            const u32 xa = xbase + tile * 8192;
            f32x4 acc0 = {0.f, 0.f, 0.f, 0.f}, acc1 = {0.f, 0.f, 0.f, 0.f};
#pragma unroll
            for (int s = 0; s < 8; ++s) {
                const bf16x8 xb = *(const bf16x8*)(smem + ((xa ^ ((s & 3) << 6)) + (s >> 2) * 256));
                acc0 = __builtin_amdgcn_mfma_f32_16x16x32_bf16(wf0[s], xb, acc0, 0, 0, 0);
                acc1 = __builtin_amdgcn_mfma_f32_16x16x32_bf16(wf1[s], xb, acc1, 0, 0, 0);
            }
            u32x4 d;
#pragma unroll
            for (int i = 0; i < 4; i += 2) {
                const u32 bb = cvt_pk_bf16(acc0[i] * NLOG2E, acc0[i + 1] * NLOG2E);
#pragma unroll
                for (int k = 0; k < 2; ++k) {
                    const u32 vb = __float_as_uint(acc1[i + k]);
                    const u32 bl = k ? (bb >> 16) : (bb & 0xFFFFu);
                    const u32 m = __float_as_uint(__builtin_fmaxf(__builtin_fabsf(acc1[i + k]), 1.17549435e-38f));
                    const u32 u = m + 0x8000u - bl;
                    d[i + k] = (vb & 0x80000000u) | (u & 0x7FFF0000u) | bl;
                }
            }
            *(u32x4*)(scr + swr) = d;
            const u32x4 v = *(const u32x4*)(scr + srd);
            __builtin_amdgcn_raw_buffer_store_b128(v, bvs, svo + (u32)tile * 4096, 0, AUX_SC1);
        }
        if (rgp < 2) WAIT_V(3); else WAIT_V(2);
    }
    BAR();
    if (tid == 0) __hip_atomic_store((GLB_AS u64*)(ws + OFF_F1) + b, tagv, __ATOMIC_RELAXED, __HIP_MEMORY_SCOPE_AGENT);
    MARK(2);
    TRRD_ALL
    WAIT_L(0);
    __builtin_amdgcn_sched_barrier(0);
    BAR();
    {
#pragma unroll 1
        for (int it = 0; it < ntile; ++it) {
            const int tile = rgp + 4 * it;
            const u32 xa = xbase + tile * 8192;
            f32x4 acc0 = {0.f, 0.f, 0.f, 0.f}, acc1 = {0.f, 0.f, 0.f, 0.f};
#pragma unroll
            for (int s = 0; s < 8; ++s) {
                const bf16x8 xb = *(const bf16x8*)(smem + ((xa ^ ((s & 3) << 6)) + (s >> 2) * 256));
                acc0 = __builtin_amdgcn_mfma_f32_16x16x32_bf16(wf0[s], xb, acc0, 0, 0, 0);
                acc1 = __builtin_amdgcn_mfma_f32_16x16x32_bf16(wf1[s], xb, acc1, 0, 0, 0);
            }
            u32x4 d;
#pragma unroll
            for (int i = 0; i < 4; ++i) d[i] = cvt_pk_bf16(acc0[i] * NLOG2E, acc1[i]);
            *(u32x4*)(smem + L_AU + (tile * 16 + xr) * 256 + (((strip * 4 + g) ^ xr) << 4)) = d;
        }
#pragma unroll
        for (int i = 0; i < 2; ++i) {
            const int q = tid + 1024 * i;
            if (q < 1280) {
                const int r = q >> 3, c8 = q & 7;
                const u32x4 v = *(const u32x4*)(smem + L_XT + r * 512 + (((slice * 8 + c8) ^ (r & 15)) << 4));
                *(u32x2*)(smem + L_XS + r * 128 + (((2 * c8) ^ (r & 15)) << 3)) = (u32x2){v.x, v.y};
                *(u32x2*)(smem + L_XS + r * 128 + (((2 * c8 + 1) ^ (r & 15)) << 3)) = (u32x2){v.z, v.w};
            }
        }
    }
    if (w == 15) wait_flags(ws + OFF_F1, tag, lane);
    WAIT_V(0); WAIT_L(0);
    BAR();
    if (tid == 0) __hip_atomic_store((GLB_AS u64*)(ws + OFF_F2) + b, tagv, __ATOMIC_RELAXED, __HIP_MEMORY_SCOPE_AGENT);
    MARK(3);

    u32x4 r4a, r4b; u32x2 r1;
    asm volatile("global_load_dwordx4 %0, %3, %4 sc1\n\tglobal_load_dwordx4 %1, %3, %4 offset:16 sc1\n\tglobal_load_dwordx2 %2, %5, %6 sc1"
                 : "=&v"(r4a), "=&v"(r4b), "=&v"(r1)
                 : "v"(tid * 32), "s"(ws + OFF_R4 + (size_t)bucket * NCH * CAP * 4), "v"(tid * 8), "s"(ws + OFF_R1 + (size_t)bucket * NCH * CAP) : "memory");
    u32* const cnt = (u32*)(smem + L_CNT);
    u32* const hist = (u32*)(smem + L_HIST);
    u32* const misc = (u32*)(smem + L_MISC);
    for (int i = tid; i < LSZ / 16; i += 1024) {
        *(u32x4*)(smem + L_LIDX + i * 16) = (u32x4){NEUTRAL_REC, NEUTRAL_REC, NEUTRAL_REC, NEUTRAL_REC};
        *(u32x4*)(smem + L_LE + i * 16) = (u32x4){0, 0, 0, 0};
    }
    if (tid < 160 + 64) cnt[tid] = 0;
    if (tid == 0) { misc[0] = 16; misc[1] = 0; }
    asm volatile("s_waitcnt vmcnt(0) lgkmcnt(0)" : "+v"(r4a), "+v"(r4b), "+v"(r1) :: "memory");
    __builtin_amdgcn_sched_barrier(0);
    BAR();
    MARK(4);
    {
        const u32 rec[8] = {r4a.x, r4a.y, r4a.z, r4a.w, r4b.x, r4b.y, r4b.z, r4b.w};
        u32 dl[8], slot[8];
        bool ovf = false;
#pragma unroll
        for (int k = 0; k < 8; ++k) { dl[k] = ((k < 4 ? r1.x : r1.y) >> (8 * (k & 3))) & 0xFF; ovf |= dl[k] == 0xFE; }
#pragma unroll
        for (int k = 0; k < 8; ++k) slot[k] = atomicAdd(&cnt[dl[k] < (u32)NB ? dl[k] : 224 + lane], 1u);
#pragma unroll
        for (int k = 0; k < 8; ++k) {
            const bool v = dl[k] < (u32)NB, ok = v && slot[k] < CAPN;
            ovf |= v && !ok;
            const u32 o = ok ? dl[k] * LSTB + slot[k] * 4 : TRASH + lane * 4;
            *(u32*)(smem + L_LIDX + o) = rec[k] & 0xFFFFu;
            *(u32*)(smem + L_LE + o) = rec[k] & 0xFFFF0000u;
        }
        if (ovf) misc[1] = 1;
    }
    WAIT_L(0); BAR();
    const bool slow = misc[1] != 0;
    u32 mydeg = 0, myrank = 0;
    if (tid < nvalid) { mydeg = cnt[tid]; mydeg = mydeg < 63 ? mydeg : 63; myrank = atomicAdd(&hist[63 - mydeg], 1u); }
    WAIT_L(0); BAR();
    if (w < 3) {
        const u32 h0 = hist[lane];
        u32 v = h0;
        v = dpp_add<0x111, 0xf>(v); v = dpp_add<0x112, 0xf>(v); v = dpp_add<0x114, 0xf>(v); v = dpp_add<0x118, 0xf>(v);
        v = dpp_add<0x142, 0xa>(v); v = dpp_add<0x143, 0xc>(v);
        const u32 base = (u32)__builtin_amdgcn_ds_bpermute((int)((63 - mydeg) * 4), (int)(v - h0));
        if (tid < nvalid) {
            const u32 pos = base + myrank;
            const u32x2 ent = {(u32)(L_LIDX + tid * LSTB) | (mydeg << 24), (u32)(tid * 256) | (u32)((tid & 15) << 4)};
            u32x2* o = (u32x2*)(smem + L_ORD) + pos;
            o[0] = ent;
            if (pos == (u32)nvalid - 1) { o[1] = ent; o[2] = ent; o[3] = ent; }
        }
    }
    if (w == 15) wait_flags(ws + OFF_F2, tag, lane);
    WAIT_L(0); BAR();
    if (b == 0 && tid == 0) __hip_atomic_store((GLB_AS u64*)(ws + OFF_G), tagv, __ATOMIC_RELAXED, __HIP_MEMORY_SCOPE_AGENT);
    MARK(5);

    const char* const bvb = ws + OFF_BV + (size_t)slice * NP * 256;
    const u32 joff = j * 16;
    if (slow) {
        __builtin_amdgcn_fence(__ATOMIC_ACQUIRE, "agent");
        u32 sel = 0x00003F80u;
        asm volatile("" : "+s"(sel));
        for (int task = w; task * 4 < nvalid; task += 16) {
            const int n = task * 4 + g;
            const bool nv = n < nvalid;
            const int nl = nv ? n : 0;
            const int node = node0 + nl;
            const u32x4 au = *(const u32x4*)(smem + L_AU + nl * 256 + ((j ^ (nl & 15)) << 4));
            const u32x2 xv = *(const u32x2*)(smem + L_XS + nl * 128 + ((j ^ (nl & 15)) << 3));
            const f32x2 a01 = {__uint_as_float(au.x << 16), __uint_as_float(au.y << 16)};
            const f32x2 a23 = {__uint_as_float(au.z << 16), __uint_as_float(au.w << 16)};
            Acc acc = {{0.f, 0.f}, {0.f, 0.f}, {0.f, 0.f}, {0.f, 0.f}};
            const int lo = node0 + task * 4;
            for (int e0 = 0; e0 < NE; e0 += 64) {
                const int d = dst[e0 + lane];
                unsigned long long m = __ballot((u32)(d - lo) < 4u);
                if (m) {
                    const u32 rc = (u32)src[e0 + lane] | ((u32)ea[e0 + lane] << 16);
                    while (m) {
                        const int l = __builtin_ctzll(m); m &= m - 1;
                        const int dd = __builtin_amdgcn_readlane(d, l);
                        const u32 rr = __builtin_amdgcn_readlane(rc, l);
                        const u32 rec = (nv && dd == node) ? rr : NEUTRAL_REC;
                        const u32x4 dv = *(const u32x4*)(bvb + (size_t)((rec & 0xFFFFu) * 256 + joff));
                        edge_step(acc, dv, rec, a01, a23, w01, w23, sel);
                    }
                }
            }
            if (nv) node_finish(acc, au, xv, out + (size_t)node * DD + slice * 64 + j * 4);
        }
        return;
    }

    const int ntask = (nvalid + 3) >> 2;
    if (w < ntask) {
        u32x4 srdBV, srdO;
        { const size_t p = (size_t)bvb; srdBV = (u32x4){(u32)p, (u32)(p >> 32) | (256u << 16), 0xFFFFFFFFu, 0x00020000u}; }
        { const size_t ob = (size_t)node0 * 512 + slice * 128; const size_t p = (size_t)out + ob; srdO = (u32x4){(u32)p, (u32)(p >> 32), (u32)((size_t)NN * 512 - ob), 0x00020000u}; }
        u32 g8 = L_ORD + g * 8, ctr = L_MISC, one = 1, j8 = j * 8;
        unsigned long long eps2 = 0x358637BD358637BDull;
        u32 s_cnt, s_nxt, s_t, s_tc;
#define STAGE(DS0, DS1, DS2, DS3, DSL, DSH, IX, EP, E1, DN, IXN, O1, O2) \
            "s_waitcnt lgkmcnt(0)\n\t" \
            "buffer_load_dwordx4 " DN ", " IXN ", %[bv], 0 idxen offen sc1\n\t" \
            "v_pk_fma_f32 v[112:113], " EP ", v[100:101], v[96:97] op_sel_hi:[0,1,1]\n\t" \
            "v_pk_fma_f32 v[114:115], " EP ", v[102:103], v[98:99] op_sel_hi:[0,1,1]\n\t" \
            "ds_read_b32 " IX ", v89 offset:" O1 "\n\t" \
            "ds_read_b32 " E1 ", v89 offset:" O2 "\n\t" \
            "s_waitcnt vmcnt(3)\n\t" \
            "v_dot2c_f32_bf16 v112, %[slo], " DS0 "\n\t" \
            "v_dot2c_f32_bf16 v113, %[slo], " DS1 "\n\t" \
            "v_dot2c_f32_bf16 v114, %[slo], " DS2 "\n\t" \
            "v_dot2c_f32_bf16 v115, %[slo], " DS3 "\n\t" \
            "v_exp_f32 v112, v112\n\t" \
            "v_exp_f32 v113, v113\n\t" \
            "v_exp_f32 v114, v114\n\t" \
            "v_exp_f32 v115, v115\n\t" \
            "v_pk_add_f32 v[112:113], v[112:113], 1.0 op_sel_hi:[1,0]\n\t" \
            "v_pk_add_f32 v[114:115], v[114:115], 1.0 op_sel_hi:[1,0]\n\t" \
            "v_rcp_f32 v112, v112\n\t" \
            "v_rcp_f32 v113, v113\n\t" \
            "v_rcp_f32 v114, v114\n\t" \
            "v_rcp_f32 v115, v115\n\t" \
            "s_sub_u32 %[cnt], %[cnt], 1\n\t" \
            "v_pk_add_f32 v[108:109], v[108:109], v[112:113]\n\t" \
            "v_pk_fma_f32 v[104:105], v[112:113], " DSL ", v[104:105]\n\t" \
            "v_pk_add_f32 v[110:111], v[110:111], v[114:115]\n\t" \
            "v_pk_fma_f32 v[106:107], v[114:115], " DSH ", v[106:107]\n\t" \
            "s_cmp_eq_u32 %[cnt], 0\n\t"
#define PRIO_INIT "s_setprio 3\n\t"
#define PRIO_STEP \
            "s_add_u32 %[tc], %[tc], 1\n\t" \
            "s_cmp_lg_u32 %[tc], 1\n\t s_cbranch_scc1 L_p1%=\n\t s_setprio 2\n\t L_p1%=:\n\t" \
            "s_cmp_lg_u32 %[tc], 2\n\t s_cbranch_scc1 L_p2%=\n\t s_setprio 1\n\t L_p2%=:\n\t" \
            "s_cmp_lg_u32 %[tc], 3\n\t s_cbranch_scc1 L_p3%=\n\t s_setprio 0\n\t L_p3%=:\n\t"
        asm volatile(
            PRIO_INIT
            "s_mov_b32 %[tc], 0\n\t"
            "s_lshl_b32 %[t], %[task], 5\n\t"
            "v_add_u32 v127, %[t], v91\n\t"
            "ds_read_b64 v[122:123], v127\n\t"
            "s_mov_b64 exec, 1\n\t"
            "ds_add_rtn_u32 v125, v93, v95\n\t"
            "s_mov_b64 exec, -1\n\t"
            "L_task%=:\n\t"
            "s_waitcnt lgkmcnt(0)\n\t"
            "v_readfirstlane_b32 %[nxt], v125\n\t"
            "v_readfirstlane_b32 %[cnt], v122\n\t"
            "v_and_b32 v89, 0xffffff, v122\n\t"
            "v_and_b32 v127, 0xffffff00, v123\n\t"
            "v_xor_b32 v112, v123, v81\n\t"
            "ds_read_b32 v80, v89\n\t"
            "ds_read_b32 v82, v89 offset:4\n\t"
            "ds_read_b32 v84, v89 offset:8\n\t"
            "ds_read_b32 v86, v89 offset:12\n\t"
            "ds_read_b32 v88, v89 offset:%c[le0]\n\t"
            "ds_read_b32 v90, v89 offset:%c[le1]\n\t"
            "ds_read_b32 v92, v89 offset:%c[le2]\n\t"
            "ds_read_b32 v94, v89 offset:%c[le3]\n\t"
            "v_lshl_add_u32 v124, v127, 1, v126\n\t"
            "v_lshrrev_b32 v113, 1, v112\n\t"
            "ds_read_b128 v[116:119], v112 offset:%c[au]\n\t"
            "ds_read_b64 v[120:121], v113 offset:%c[xs]\n\t"
            "s_lshr_b32 %[cnt], %[cnt], 24\n\t"
            "s_lshl_b32 %[t], %[nxt], 5\n\t"
            "v_add_u32 v127, %[t], v91\n\t"
            "ds_read_b64 v[122:123], v127\n\t"
            "s_mov_b64 exec, 1\n\t"
            "ds_add_rtn_u32 v125, v93, v95\n\t"
            "s_mov_b64 exec, -1\n\t"
            "s_waitcnt lgkmcnt(9)\n\t"
            "buffer_load_dwordx4 v[64:67], v[80:81], %[bv], 0 idxen offen sc1\n\t"
            "buffer_load_dwordx4 v[68:71], v[82:83], %[bv], 0 idxen offen sc1\n\t"
            "buffer_load_dwordx4 v[72:75], v[84:85], %[bv], 0 idxen offen sc1\n\t"
            "v_mov_b64 v[104:105], 0\n\t"
            "v_mov_b64 v[106:107], 0\n\t"
            "v_mov_b64 v[108:109], 0\n\t"
            "v_mov_b64 v[110:111], 0\n\t"
            "s_waitcnt lgkmcnt(2)\n\t"
            "v_lshlrev_b32 v96, 16, v116\n\t"
            "v_lshlrev_b32 v97, 16, v117\n\t"
            "v_lshlrev_b32 v98, 16, v118\n\t"
            "v_lshlrev_b32 v99, 16, v119\n\t"
            "s_cmp_eq_u32 %[cnt], 0\n\t"
            "s_cbranch_scc1 L_fin%=\n\t"
            "L_loop%=:\n\t"
            STAGE("v64", "v65", "v66", "v67", "v[64:65]", "v[66:67]", "v80", "v[88:89]", "v88", "v[76:79]", "v[86:87]", "16", "%c[le4]")
            "s_cbranch_scc1 L_fin%=\n\t"
            STAGE("v68", "v69", "v70", "v71", "v[68:69]", "v[70:71]", "v82", "v[90:91]", "v90", "v[64:67]", "v[80:81]", "20", "%c[le5]")
            "s_cbranch_scc1 L_fin%=\n\t"
            STAGE("v72", "v73", "v74", "v75", "v[72:73]", "v[74:75]", "v84", "v[92:93]", "v92", "v[68:71]", "v[82:83]", "24", "%c[le6]")
            "s_cbranch_scc1 L_fin%=\n\t"
            STAGE("v76", "v77", "v78", "v79", "v[76:77]", "v[78:79]", "v86", "v[94:95]", "v94", "v[72:75]", "v[84:85]", "28", "%c[le7]")
            "v_add_u32 v89, 16, v89\n\t"
            "s_cbranch_scc0 L_loop%=\n\t"
            "L_fin%=:\n\t"
            "v_pk_add_f32 v[108:109], v[108:109], %[eps]\n\t"
            "v_pk_add_f32 v[110:111], v[110:111], %[eps]\n\t"
            "v_rcp_f32 v108, v108\n\t"
            "v_rcp_f32 v109, v109\n\t"
            "v_rcp_f32 v110, v110\n\t"
            "v_rcp_f32 v111, v111\n\t"
            "v_pk_mul_f32 v[104:105], v[104:105], v[108:109]\n\t"
            "v_pk_mul_f32 v[106:107], v[106:107], v[110:111]\n\t"
            "v_dot2c_f32_bf16 v104, %[shi], v116\n\t"
            "v_dot2c_f32_bf16 v105, %[shi], v117\n\t"
            "v_dot2c_f32_bf16 v106, %[shi], v118\n\t"
            "v_dot2c_f32_bf16 v107, %[shi], v119\n\t"
            "v_maximum3_f32 v104, v104, 0, 0\n\t"
            "v_maximum3_f32 v105, v105, 0, 0\n\t"
            "v_maximum3_f32 v106, v106, 0, 0\n\t"
            "v_maximum3_f32 v107, v107, 0, 0\n\t"
            "v_dot2c_f32_bf16 v104, %[slo], v120\n\t"
            "v_dot2c_f32_bf16 v105, %[shi], v120\n\t"
            "v_dot2c_f32_bf16 v106, %[slo], v121\n\t"
            "v_dot2c_f32_bf16 v107, %[shi], v121\n\t"
            "s_nop 1\n\t"
            "v_cvt_pk_bf16_f32 v112, v104, v105\n\t"
            "v_cvt_pk_bf16_f32 v113, v106, v107\n\t"
            "buffer_store_dwordx2 v[112:113], v124, %[o], 0 offen nt\n\t"
            PRIO_STEP
            "s_cmp_lt_u32 %[nxt], %[ntask]\n\t"
            "s_cbranch_scc1 L_task%=\n\t"
            "s_waitcnt vmcnt(0) lgkmcnt(0)"
            : [cnt] "=&s"(s_cnt), [nxt] "=&s"(s_nxt), [t] "=&s"(s_t), [tc] "=&s"(s_tc)
            : "{v81}"(joff), "{v83}"(joff), "{v85}"(joff), "{v87}"(joff), "{v91}"(g8), "{v93}"(ctr), "{v95}"(one), "{v126}"(j8),
              "{v[100:101]}"(w01), "{v[102:103]}"(w23),
              [task] "s"(w), [ntask] "s"(ntask), [bv] "s"(srdBV), [o] "s"(srdO), [slo] "s"(0x00003F80u), [shi] "s"(0x3F800000u), [eps] "s"(eps2),
              [au] "i"(L_AU), [xs] "i"(L_XS), [le0] "i"(LSZ), [le1] "i"(LSZ + 4), [le2] "i"(LSZ + 8), [le3] "i"(LSZ + 12),
              [le4] "i"(LSZ + 16), [le5] "i"(LSZ + 20), [le6] "i"(LSZ + 24), [le7] "i"(LSZ + 28)
            : "v64", "v65", "v66", "v67", "v68", "v69", "v70", "v71", "v72", "v73", "v74", "v75", "v76", "v77", "v78", "v79",
              "v80", "v82", "v84", "v86", "v88", "v89", "v90", "v92", "v94", "v96", "v97", "v98", "v99",
              "v104", "v105", "v106", "v107", "v108", "v109", "v110", "v111", "v112", "v113", "v114", "v115",
              "v116", "v117", "v118", "v119", "v120", "v121", "v122", "v123", "v124", "v125", "v127", "scc", "memory");
#undef STAGE
    }
    MARK(6);
}

extern "C" void kernel_launch(void* const* d_in, const int* in_sizes, int n_in,
                              void* d_out, int out_size, void* d_ws, size_t ws_size,
                              hipStream_t stream) {
    const u16* x = (const u16*)d_in[0];
    const u16* ea = (const u16*)d_in[1];
    const int* src = (const int*)d_in[2];
    const int* dst = (const int*)d_in[3];
    const u16* wa = (const u16*)d_in[4];
    const u16* wb = (const u16*)d_in[5];
    const u16* wc = (const u16*)d_in[6];
    const u16* wu = (const u16*)d_in[7];
    const u16* wv = (const u16*)d_in[8];
    (void)hipFuncSetAttribute((const void*)gatedgcn_fused, hipFuncAttributeMaxDynamicSharedMemorySize, L_END);
    gatedgcn_fused<<<256, 1024, L_END, stream>>>(x, ea, src, dst, wa, wb, wc, wu, wv, (char*)d_ws, (u16*)d_out);
}
```

```cpp
#include <hip/hip_runtime.h>
#include <hip/hip_bf16.h>
#include <stdint.h>

typedef unsigned short u16;
typedef unsigned int u32;
typedef unsigned long long u64;
typedef u32 u32x2 __attribute__((ext_vector_type(2)));
typedef u32 u32x4 __attribute__((ext_vector_type(4)));
typedef float f32x2 __attribute__((ext_vector_type(2)));
typedef float f32x4 __attribute__((ext_vector_type(4)));
typedef __bf16 bf16x8 __attribute__((ext_vector_type(8)));
typedef __bf16 bf16x2 __attribute__((ext_vector_type(2)));

constexpr int NN = 10000;
constexpr int NE = 160000;
constexpr int DD = 256;
constexpr int NP = 10048;
constexpr int NBK = 64;
constexpr int NB = 157;
constexpr int NCH = 256;
constexpr int CHE = 625;
constexpr int CAP = 32;
constexpr int CAPN = 44;
constexpr float NLOG2E = -1.4426950408889634f;
constexpr u32 NEUTRAL_REC = (u32)NN;
constexpr int NNR = 32;
constexpr u32 NEUTRAL_BV = 0x00007F00u;

constexpr size_t OFF_BV = 0;
constexpr int RGB = 192;
constexpr size_t OFF_R4 = OFF_BV + (size_t)4 * NP * 256;
constexpr size_t OFF_F1 = OFF_R4 + (size_t)NBK * NCH * RGB;
constexpr size_t OFF_F2 = OFF_F1 + 2048;
constexpr size_t OFF_G = OFF_F2 + 2048;

#define GLB_AS __attribute__((address_space(1)))

__device__ __forceinline__ void glds16a(const void* sbase, u32 voff, u32 lds) {
    u32 keep;
    asm volatile("s_mov_b32 %0, m0\n\ts_mov_b32 m0, %3\n\ts_nop 0\n\tglobal_load_lds_dwordx4 %1, %2\n\ts_mov_b32 m0, %0" : "=&s"(keep) : "v"(voff), "s"(sbase), "s"(lds) : "memory");
}
__device__ __forceinline__ u32 cvt_pk_bf16(float lo, float hi) {
    const f32x2 v = {lo, hi}; return __builtin_bit_cast(u32, __builtin_convertvector(v, bf16x2));
}
#define WAIT_V(n) asm volatile("s_waitcnt vmcnt(" #n ")" ::: "memory")
#define WAIT_L(n) asm volatile("s_waitcnt lgkmcnt(" #n ")" ::: "memory")
#define BAR() __builtin_amdgcn_s_barrier()
#define AUX_SC1 16

constexpr int L_FR = 0;
constexpr int L_ST = L_FR, L_BC = L_FR + NBK * RGB, L_PF = L_BC + 256;
constexpr int L_WR = 16384;
constexpr int L_XT = L_WR + 65536;
constexpr int L_END = 163840;
constexpr int LSTB = 92;
constexpr int LSZ = (NB * LSTB + 16 + 15) / 16 * 16;
constexpr int L_LE = L_FR;
constexpr int L_AU = L_WR;
constexpr int L_LIDX = L_AU + 40960;
constexpr int L_CNT = L_LIDX + LSZ;
constexpr int L_HIST = L_CNT + 160 * 4;
constexpr int L_ORD = L_HIST + 64 * 4;
constexpr int L_ORDEND = L_ORD + 80 * 32;
constexpr int L_MISC = 16128;
constexpr int IO = L_LIDX - L_LE;

__device__ __forceinline__ float dot2sel(u32 d, u32 sel, float c) {
    return __builtin_amdgcn_fdot2_f32_bf16(__builtin_bit_cast(bf16x2, d), __builtin_bit_cast(bf16x2, sel), c, false);
}
struct Acc { f32x2 n01, n23, d01, d23; };
__device__ __forceinline__ void edge_step(Acc& a, const u32x4 d, const u32 rec, const f32x2 a01, const f32x2 a23, const f32x2 w01, const f32x2 w23, const u32 sel) {
    const float e = __uint_as_float(rec & 0xFFFF0000u);
    const f32x2 ee = {e, e};
    const f32x2 t01 = __builtin_elementwise_fma(ee, w01, a01);
    const f32x2 t23 = __builtin_elementwise_fma(ee, w23, a23);
    f32x2 E01, E23;
    E01.x = __builtin_amdgcn_exp2f(dot2sel(d.x, sel, t01.x));
    E01.y = __builtin_amdgcn_exp2f(dot2sel(d.y, sel, t01.y));
    E23.x = __builtin_amdgcn_exp2f(dot2sel(d.z, sel, t23.x));
    E23.y = __builtin_amdgcn_exp2f(dot2sel(d.w, sel, t23.y));
    const f32x2 one = {1.f, 1.f};
    E01 += one; E23 += one;
    f32x2 s01, s23;
    s01.x = __builtin_amdgcn_rcpf(E01.x); s01.y = __builtin_amdgcn_rcpf(E01.y);
    s23.x = __builtin_amdgcn_rcpf(E23.x); s23.y = __builtin_amdgcn_rcpf(E23.y);
    a.d01 += s01; a.d23 += s23;
    const f32x2 v01 = {__uint_as_float(d.x), __uint_as_float(d.y)};
    const f32x2 v23 = {__uint_as_float(d.z), __uint_as_float(d.w)};
    a.n01 = __builtin_elementwise_fma(s01, v01, a.n01);
    a.n23 = __builtin_elementwise_fma(s23, v23, a.n23);
}
__device__ __forceinline__ void node_finish(const Acc& a, const u32x4 au, const u32x2 xv, u16* outp) {
    const float num[4] = {a.n01.x, a.n01.y, a.n23.x, a.n23.y};
    const float den[4] = {a.d01.x, a.d01.y, a.d23.x, a.d23.y};
    const u32 aus[4] = {au.x, au.y, au.z, au.w};
    const float xs[4] = {__uint_as_float(xv.x << 16), __uint_as_float(xv.x & 0xFFFF0000u), __uint_as_float(xv.y << 16), __uint_as_float(xv.y & 0xFFFF0000u)};
    float o[4];
#pragma unroll
    for (int c = 0; c < 4; ++c) {
        const float z = __uint_as_float(aus[c] & 0xFFFF0000u) + num[c] * __builtin_amdgcn_rcpf(den[c] + 1e-6f);
        const float rl = z > 0.f ? z : 0.f * z;
        o[c] = xs[c] + rl;
    }
    u32x2 ov; ov.x = cvt_pk_bf16(o[0], o[1]); ov.y = cvt_pk_bf16(o[2], o[3]);
    *(u32x2*)outp = ov;
}
template <int CTRL, int RM> __device__ __forceinline__ u32 dpp_add(u32 v) {
    return v + (u32)__builtin_amdgcn_update_dpp(0, (int)v, CTRL, RM, 0xf, false);
}

__device__ __forceinline__ void wait_flags(const char* flags, u64 tag, int lane) {
    const u32 lo = (u32)tag, hi = (u32)(tag >> 32);
    for (int spins = 0; spins < (1 << 21); ++spins) {
        u32x4 a, b;
        asm volatile("global_load_dwordx4 %0, %2, %3 sc1\n\tglobal_load_dwordx4 %1, %2, %3 offset:16 sc1\n\ts_waitcnt vmcnt(0)"
                     : "=&v"(a), "=&v"(b) : "v"(lane * 32), "s"(flags) : "memory");
        const bool ok = a.x == lo && a.y == hi && a.z == lo && a.w == hi && b.x == lo && b.y == hi && b.z == lo && b.w == hi;
        if (__all(ok)) break;
        __builtin_amdgcn_s_sleep(1);
    }
}

__device__ __forceinline__ void barrier_wait_flags(const char* flags, u64 tag, int lane) {
    const u32 lo = (u32)tag, hi = (u32)(tag >> 32);
    u32x4 a, b;
    asm volatile("global_load_dwordx4 %0, %2, %3 sc1\n\tglobal_load_dwordx4 %1, %2, %3 offset:16 sc1\n\ts_barrier\n\ts_waitcnt vmcnt(0)"
                 : "=&v"(a), "=&v"(b) : "v"(lane * 32), "s"(flags) : "memory");
    const bool ok = a.x == lo && a.y == hi && a.z == lo && a.w == hi && b.x == lo && b.y == hi && b.z == lo && b.w == hi;
    if (!__all(ok)) wait_flags(flags, tag, lane);
}

#define TRRD(S) { u32x2 a0, a1, b0, b1; \
        asm volatile("ds_read_b64_tr_b16 %0, %4 offset:%c5\n\tds_read_b64_tr_b16 %1, %4 offset:%c6\n\t" \
                     "ds_read_b64_tr_b16 %2, %4 offset:%c7\n\tds_read_b64_tr_b16 %3, %4 offset:%c8" \
                     : "=&v"(a0), "=&v"(a1), "=&v"(b0), "=&v"(b1) \
                     : "v"(trbase), "i"((S) * 4096), "i"((S) * 4096 + 512), "i"(32768 + (S) * 4096), "i"(32768 + (S) * 4096 + 512) : "memory"); \
        wf0[S] = __builtin_bit_cast(bf16x8, __builtin_shufflevector(a0, a1, 0, 1, 2, 3)); \
        wf1[S] = __builtin_bit_cast(bf16x8, __builtin_shufflevector(b0, b1, 0, 1, 2, 3)); }
#define TRRD_ALL TRRD(0) TRRD(1) TRRD(2) TRRD(3) TRRD(4) TRRD(5) TRRD(6) TRRD(7)

__global__ __launch_bounds__(1024, 4) void gatedgcn_fused(
    const u16* __restrict__ x, const u16* __restrict__ ea, const int* __restrict__ src, const int* __restrict__ dst,
    const u16* __restrict__ wa, const u16* __restrict__ wb, const u16* __restrict__ wc, const u16* __restrict__ wu, const u16* __restrict__ wv,
    char* __restrict__ ws, u16* __restrict__ out) {
    extern __shared__ __attribute__((aligned(16))) char smem[];
    const int tid = threadIdx.x, lane = tid & 63;
    const int w = __builtin_amdgcn_readfirstlane(tid >> 6);
    const int b = blockIdx.x;
    const int slice = (b & 7) >> 1;
    const int bucket = (b >> 3) * 2 + (b & 1);
    const int node0 = bucket * NB;
    const int nvalid = (NN - node0) < NB ? (NN - node0) : NB;
    if (w == 15) {
        u64 pc; u32 keep, len;
        asm volatile("s_getpc_b64 %0\n\tgcn_pf_from:\n\ts_mov_b32 %1, gcn_pf_to-gcn_pf_from-4" : "=s"(pc), "=s"(len));
#pragma unroll
        for (int i = 0; i < 3; ++i) {
            const u32 o = lane * 64 + i * 4096;
            asm volatile("s_mov_b32 %0, m0\n\ts_mov_b32 m0, %3\n\ts_nop 0\n\tglobal_load_lds_dword %1, %2\n\ts_mov_b32 m0, %0"
                         : "=&s"(keep) : "v"(o < len ? o : len), "s"(pc), "s"((u32)L_PF) : "memory");
        }
    }
    asm volatile("" :: "s"(x), "s"(ea), "s"(src), "s"(dst), "s"(wa), "s"(wb), "s"(wc), "s"(wu), "s"(wv), "s"(ws), "s"(out));

    const int g = lane >> 4, j = lane & 15;
    const int wkr = lane >> 3;
    auto stage_w = [&](const u16* W0, const u16* W1) {
#pragma unroll
        for (int i = 0; i < 4; ++i) {
            const int q = (w & 7) * 4 + i;
            const int hh = ((wkr >> 1) & 1) | ((q & 1) << 1);
            glds16a(w < 8 ? W0 : W1, (u32)((q * 8 + wkr) * 512 + slice * 128 + (((lane & 7) ^ (hh << 1)) << 4)), L_WR + (w >> 3) * 32768 + q * 1024);
        }
    };
    auto stage_x = [&](int q) {
        const int r = q * 2 + (lane >> 5);
        int grow = node0 + r; grow = grow < NN ? grow : NN - 1;
        glds16a(x, (u32)(grow * 512 + (((lane & 31) ^ (r & 15)) << 4)), L_XT + q * 1024);
    };
    u64 gw; u32x2 wcv; u32 bd, bs, be;
    {
        const u32 eo = (u32)(b * CHE + (tid < CHE ? tid : 0));
        asm volatile("global_load_dword %2, %9, %10\n\tglobal_load_dword %3, %9, %11\n\tglobal_load_ushort %4, %12, %13\n\t"
                     "global_load_dwordx2 %0, %5, %6 sc1\n\tglobal_load_dwordx2 %1, %7, %8"
                     : "=&v"(gw), "=&v"(wcv), "=&v"(bd), "=&v"(bs), "=&v"(be)
                     : "v"(0u), "s"(ws + OFF_G), "v"((u32)(slice * 128 + j * 8)), "s"(wc), "v"(eo * 4), "s"(dst), "s"(src), "v"(eo * 2), "s"(ea) : "memory");
    }
    if (w == 14) {
        const int q = ((b >> 3) & 7) * 64 + lane;
        const char* p = (const char*)(q < 256 ? wa : wu) + (size_t)(q & 255) * 512 + slice * 128;
        u32 keep;
        asm volatile("s_mov_b32 %0, m0\n\ts_mov_b32 m0, %2\n\ts_nop 0\n\tglobal_load_lds_dword %1, off\n\ts_mov_b32 m0, %0" : "=&s"(keep) : "v"(p), "s"((u32)(L_PF + 256)) : "memory");
    }
    stage_w(wb, wv);
    stage_x(w * 2); stage_x(w * 2 + 1);
    if (tid < 128) *(u32x4*)(smem + L_ST + (tid >> 1) * RGB + 128 + (tid & 1) * 16) = (u32x4){~0u, ~0u, ~0u, ~0u};
    else if (tid < 128 + NBK) *(u32*)(smem + L_BC + (tid - 128) * 4) = 0;
    else if (tid >= 256 && tid < 258) *(u32*)(smem + L_MISC + (tid - 256) * 4) = tid == 256 ? 16 : 0;
    WAIT_L(0);
    BAR();
    stage_x(32 + w * 2); stage_x(32 + w * 2 + 1);
    stage_x(64 + w);
    asm volatile("s_waitcnt vmcnt(9)" : "+v"(gw), "+v"(wcv), "+v"(bd), "+v"(bs), "+v"(be) :: "memory");
    __builtin_amdgcn_sched_barrier(0);
    const u64 tag = gw + 0x9E3779B97F4A7C15ull;
    f32x2 w01 = {__uint_as_float(wcv.x << 16) * NLOG2E, __uint_as_float(wcv.x & 0xFFFF0000u) * NLOG2E};
    f32x2 w23 = {__uint_as_float(wcv.y << 16) * NLOG2E, __uint_as_float(wcv.y & 0xFFFF0000u) * NLOG2E};
    if (w < 10 && tid < CHE) {
        const int bk = (int)((bd * 53432u) >> 23);
        const u32 pos = atomicAdd((u32*)(smem + L_BC + bk * 4), 1u);
        if (pos < CAP) {
            *(u32*)(smem + L_ST + bk * RGB + pos * 4) = bs | (be << 16);
            *(unsigned char*)(smem + L_ST + bk * RGB + 128 + pos) = (unsigned char)(bd - bk * NB);
        }
    }
    WAIT_V(3); WAIT_L(0);
    BAR();
    {
        const __amdgpu_buffer_rsrc_t r4s = __builtin_amdgcn_make_buffer_rsrc(ws + OFF_R4, 0, NBK * NCH * RGB, 0x00020000);
        const __amdgpu_buffer_rsrc_t nrs = __builtin_amdgcn_make_buffer_rsrc(ws + OFF_BV + (size_t)slice * NP * 256, 0, NP * 256, 0x00020000);
        if (w < 12) {
            const int bk = tid / 12, p = tid - bk * 12;
            u32x4 v = *(const u32x4*)(smem + L_ST + tid * 16);
            if (p >= 8 && *(const u32*)(smem + L_BC + bk * 4) > CAP) v = (u32x4){0xFEFEFEFEu, 0xFEFEFEFEu, 0xFEFEFEFEu, 0xFEFEFEFEu};
            __builtin_amdgcn_raw_buffer_store_b128(v, r4s, (bk * NCH + b) * RGB + p * 16, 0, AUX_SC1);
        } else {
            const u32x4 nv = {NEUTRAL_BV, NEUTRAL_BV, NEUTRAL_BV, NEUTRAL_BV};
            __builtin_amdgcn_raw_buffer_store_b128(nv, nrs, bucket < 2 ? (NN + (w - 12) * 8 + (lane >> 3)) * 256 + bucket * 128 + (lane & 7) * 16 : NP * 256 + 64, 0, AUX_SC1);
        }
    }

    const int strip = w & 3, rgp = w >> 2;
    const u32 trbase = L_WR + (8 * g + (j >> 2)) * 128
                     + (((2 * strip + ((lane & 3) >> 1)) ^ (((((j >> 2) >> 1) & 1) | ((g & 1) << 1)) << 1)) << 4) + (lane & 1) * 8;
    bf16x8 wf0[8], wf1[8];
    TRRD_ALL
    WAIT_L(0);
    __builtin_amdgcn_sched_barrier(0);
    const int xr = lane & 15;
    const u32 xbase = L_XT + xr * 512 + (((xr & 12) | ((g ^ xr) & 3)) << 4);
    const __amdgpu_buffer_rsrc_t bvs = __builtin_amdgcn_make_buffer_rsrc(ws + OFF_BV + ((size_t)slice * NP + node0) * 256, 0, nvalid * 256, 0x00020000);
    const u32 svo = (lane >> 2) * 256 + strip * 64 + (lane & 3) * 16;
    const u32 bperm = ((lane >> 2) + 16 * (lane & 3)) * 4;
    auto bv_tile = [&](int tile) {
        const u32 xa = xbase + tile * 8192;
        f32x4 acc0 = {0.f, 0.f, 0.f, 0.f}, acc1 = {0.f, 0.f, 0.f, 0.f};
#pragma unroll
        for (int s = 0; s < 8; ++s) {
            const bf16x8 xb = *(const bf16x8*)(smem + ((xa ^ ((s & 3) << 6)) + (s >> 2) * 256));
            acc0 = __builtin_amdgcn_mfma_f32_16x16x32_bf16(wf0[s], xb, acc0, 0, 0, 0);
            acc1 = __builtin_amdgcn_mfma_f32_16x16x32_bf16(wf1[s], xb, acc1, 0, 0, 0);
        }
        const f32x4 bsc = acc0 * NLOG2E;
        const u32 bb01 = cvt_pk_bf16(bsc[0], bsc[1]), bb23 = cvt_pk_bf16(bsc[2], bsc[3]);
        u32 d0, d1, d2, d3, t0, t1;
        asm("s_nop 7\n\t"
            "v_xor_b32 %4, 0x80008000, %10\n\t"
            "v_xor_b32 %5, 0x80008000, %11\n\t"
            "v_max_f32_e64 %0, |%6|, %12\n\t"
            "v_max_f32_e64 %1, |%7|, %12\n\t"
            "v_max_f32_e64 %2, |%8|, %12\n\t"
            "v_max_f32_e64 %3, |%9|, %12\n\t"
            "v_sub_u32_sdwa %0, %0, sext(%4) dst_sel:DWORD dst_unused:UNUSED_PAD src0_sel:DWORD src1_sel:WORD_0\n\t"
            "v_sub_u32_sdwa %1, %1, sext(%4) dst_sel:DWORD dst_unused:UNUSED_PAD src0_sel:DWORD src1_sel:WORD_1\n\t"
            "v_sub_u32_sdwa %2, %2, sext(%5) dst_sel:DWORD dst_unused:UNUSED_PAD src0_sel:DWORD src1_sel:WORD_0\n\t"
            "v_sub_u32_sdwa %3, %3, sext(%5) dst_sel:DWORD dst_unused:UNUSED_PAD src0_sel:DWORD src1_sel:WORD_1\n\t"
            "v_bfi_b32 %0, %13, %0, %6\n\t"
            "v_bfi_b32 %1, %13, %1, %7\n\t"
            "v_bfi_b32 %2, %13, %2, %8\n\t"
            "v_bfi_b32 %3, %13, %3, %9\n\t"
            "v_perm_b32 %0, %0, %10, %14\n\t"
            "v_perm_b32 %1, %1, %10, %15\n\t"
            "v_perm_b32 %2, %2, %11, %14\n\t"
            "v_perm_b32 %3, %3, %11, %15"
            : "=&v"(d0), "=&v"(d1), "=&v"(d2), "=&v"(d3), "=&v"(t0), "=&v"(t1)
            : "v"(acc1[0]), "v"(acc1[1]), "v"(acc1[2]), "v"(acc1[3]), "v"(bb01), "v"(bb23),
              "s"(0x00800000u), "s"(0x7FFFFFFFu), "s"(0x07060100u), "s"(0x07060302u));
        u32x4 v;
        v.x = (u32)__builtin_amdgcn_ds_bpermute((int)bperm, (int)d0); v.y = (u32)__builtin_amdgcn_ds_bpermute((int)bperm, (int)d1);
        v.z = (u32)__builtin_amdgcn_ds_bpermute((int)bperm, (int)d2); v.w = (u32)__builtin_amdgcn_ds_bpermute((int)bperm, (int)d3);
        __builtin_amdgcn_raw_buffer_store_b128(v, bvs, svo + (u32)tile * 4096, 0, AUX_SC1);
    };
    bv_tile(rgp);
    WAIT_V(1);
    BAR();
    if (tid == 15 * 64) __hip_atomic_store((GLB_AS u64*)(ws + OFF_F1) + b, tag, __ATOMIC_RELAXED, __HIP_MEMORY_SCOPE_AGENT);
    stage_w(wa, wu);
    bv_tile(rgp + 4);
    if (rgp < 2) bv_tile(rgp + 8);
    if (rgp < 2) WAIT_V(2); else WAIT_V(1);
    BAR();
    TRRD_ALL
    WAIT_L(0);
    __builtin_amdgcn_sched_barrier(0);
    BAR();
    u32* const cnt = (u32*)(smem + L_CNT);
    u32* const hist = (u32*)(smem + L_HIST);
    u32* const misc = (u32*)(smem + L_MISC);
    if (tid < LSZ / 16) {
        const u32 NR2 = (NEUTRAL_REC + (u32)((tid + b) & (NNR - 1))) * 0x10001u;
        *(u32x4*)(smem + L_LIDX + tid * 16) = (u32x4){NR2, NR2, NR2, NR2};
        *(u32x4*)(smem + L_LE + tid * 16) = (u32x4){0, 0, 0, 0};
    }
    if (tid < 160) cnt[tid] = L_LE + tid * LSTB;
    else if (tid < 160 + 64) hist[tid - 160] = 0;
    auto au_tile = [&](int tile) {
        const u32 xa = xbase + tile * 8192;
        f32x4 acc0 = {0.f, 0.f, 0.f, 0.f}, acc1 = {0.f, 0.f, 0.f, 0.f};
#pragma unroll
        for (int s = 0; s < 8; ++s) {
            const bf16x8 xb = *(const bf16x8*)(smem + ((xa ^ ((s & 3) << 6)) + (s >> 2) * 256));
            acc0 = __builtin_amdgcn_mfma_f32_16x16x32_bf16(wf0[s], xb, acc0, 0, 0, 0);
            acc1 = __builtin_amdgcn_mfma_f32_16x16x32_bf16(wf1[s], xb, acc1, 0, 0, 0);
        }
        u32x4 d;
#pragma unroll
        for (int i = 0; i < 4; ++i) d[i] = cvt_pk_bf16(acc0[i] * NLOG2E, acc1[i]);
        *(u32x4*)(smem + L_AU + (tile * 16 + xr) * 256 + (((strip * 4 + g) ^ xr) << 4)) = d;
    };
    if (w == 15) wait_flags(ws + OFF_F1, tag, lane);
    else au_tile(rgp);
    WAIT_V(0); WAIT_L(0);
    BAR();
    if (tid == 0) __hip_atomic_store((GLB_AS u64*)(ws + OFF_F2) + b, tag, __ATOMIC_RELAXED, __HIP_MEMORY_SCOPE_AGENT);
    u32x4 r4a, r4b; u32x2 r1;
    asm volatile("global_load_dwordx4 %0, %3, %4 sc1\n\tglobal_load_dwordx4 %1, %3, %4 offset:16 sc1\n\tglobal_load_dwordx2 %2, %5, %4 sc1"
                 : "=&v"(r4a), "=&v"(r4b), "=&v"(r1)
                 : "v"((tid >> 2) * RGB + (tid & 3) * 32), "s"(ws + OFF_R4 + (size_t)bucket * NCH * RGB), "v"((tid >> 2) * RGB + 128 + (tid & 3) * 8) : "memory");
    au_tile(rgp + 4);
    if (rgp < 2) au_tile(rgp + 8);
    else if (w == 15) au_tile(rgp);
    asm volatile("s_waitcnt vmcnt(0) lgkmcnt(0)" : "+v"(r4a), "+v"(r4b), "+v"(r1) :: "memory");
    __builtin_amdgcn_sched_barrier(0);
    {
        u32 a0, a1, a2, a3, a4, a5, a6, a7, e0, e1, e2, e3, e4, e5, e6, e7; u64 m0, m1, m2, m3, m4, m5, m6, m7, ov;
        asm volatile(
            "v_bfe_u32 %0, %[tx], 0, 8\n\t"  "v_bfe_u32 %1, %[tx], 8, 8\n\t"  "v_bfe_u32 %2, %[tx], 16, 8\n\t" "v_lshrrev_b32 %3, 24, %[tx]\n\t"
            "v_bfe_u32 %4, %[ty], 0, 8\n\t"  "v_bfe_u32 %5, %[ty], 8, 8\n\t"  "v_bfe_u32 %6, %[ty], 16, 8\n\t" "v_lshrrev_b32 %7, 24, %[ty]\n\t"
            "v_cmp_eq_u32 %24, %[fe], %[tx]\n\t"
            "v_cmp_gt_u32 %16, %[nb], %0\n\t" "v_mad_u32_u24 %8, %0, %[ls], %[le]\n\t" "v_lshl_add_u32 %0, %0, 2, %[cb]\n\t"
            "v_cmp_gt_u32 %17, %[nb], %1\n\t" "v_mad_u32_u24 %9, %1, %[ls], %[le]\n\t" "v_lshl_add_u32 %1, %1, 2, %[cb]\n\t"
            "v_cmp_gt_u32 %18, %[nb], %2\n\t" "v_mad_u32_u24 %10, %2, %[ls], %[le]\n\t" "v_lshl_add_u32 %2, %2, 2, %[cb]\n\t"
            "v_cmp_gt_u32 %19, %[nb], %3\n\t" "v_mad_u32_u24 %11, %3, %[ls], %[le]\n\t" "v_lshl_add_u32 %3, %3, 2, %[cb]\n\t"
            "v_cmp_gt_u32 %20, %[nb], %4\n\t" "v_mad_u32_u24 %12, %4, %[ls], %[le]\n\t" "v_lshl_add_u32 %4, %4, 2, %[cb]\n\t"
            "v_cmp_gt_u32 %21, %[nb], %5\n\t" "v_mad_u32_u24 %13, %5, %[ls], %[le]\n\t" "v_lshl_add_u32 %5, %5, 2, %[cb]\n\t"
            "v_cmp_gt_u32 %22, %[nb], %6\n\t" "v_mad_u32_u24 %14, %6, %[ls], %[le]\n\t" "v_lshl_add_u32 %6, %6, 2, %[cb]\n\t"
            "v_cmp_gt_u32 %23, %[nb], %7\n\t" "v_mad_u32_u24 %15, %7, %[ls], %[le]\n\t" "v_lshl_add_u32 %7, %7, 2, %[cb]\n\t"
            "s_mov_b64 exec, %16\n\t" "ds_add_rtn_u32 %0, %0, %[two]\n\t"
            "s_mov_b64 exec, %17\n\t" "ds_add_rtn_u32 %1, %1, %[two]\n\t"
            "s_mov_b64 exec, %18\n\t" "ds_add_rtn_u32 %2, %2, %[two]\n\t"
            "s_mov_b64 exec, %19\n\t" "ds_add_rtn_u32 %3, %3, %[two]\n\t"
            "s_mov_b64 exec, %20\n\t" "ds_add_rtn_u32 %4, %4, %[two]\n\t"
            "s_mov_b64 exec, %21\n\t" "ds_add_rtn_u32 %5, %5, %[two]\n\t"
            "s_mov_b64 exec, %22\n\t" "ds_add_rtn_u32 %6, %6, %[two]\n\t"
            "s_mov_b64 exec, %23\n\t" "ds_add_rtn_u32 %7, %7, %[two]\n\t"
            "s_waitcnt lgkmcnt(0)\n\t"
            "s_mov_b64 exec, %16\n\t" "v_cmp_lt_u32 %16, %0, %8\n\t"
            "s_mov_b64 exec, %17\n\t" "v_cmp_lt_u32 %17, %1, %9\n\t"
            "s_mov_b64 exec, %18\n\t" "v_cmp_lt_u32 %18, %2, %10\n\t"
            "s_mov_b64 exec, %19\n\t" "v_cmp_lt_u32 %19, %3, %11\n\t"
            "s_mov_b64 exec, %20\n\t" "v_cmp_lt_u32 %20, %4, %12\n\t"
            "s_mov_b64 exec, %21\n\t" "v_cmp_lt_u32 %21, %5, %13\n\t"
            "s_mov_b64 exec, %22\n\t" "v_cmp_lt_u32 %22, %6, %14\n\t"
            "s_mov_b64 exec, %23\n\t" "v_cmp_lt_u32 %23, %7, %15\n\t"
            "s_mov_b64 exec, %16\n\t" "ds_write_b16 %0, %[r0] offset:%c[io]\n\t" "ds_write_b16_d16_hi %0, %[r0]\n\t"
            "s_mov_b64 exec, %17\n\t" "ds_write_b16 %1, %[r1] offset:%c[io]\n\t" "ds_write_b16_d16_hi %1, %[r1]\n\t"
            "s_mov_b64 exec, %18\n\t" "ds_write_b16 %2, %[r2] offset:%c[io]\n\t" "ds_write_b16_d16_hi %2, %[r2]\n\t"
            "s_mov_b64 exec, %19\n\t" "ds_write_b16 %3, %[r3] offset:%c[io]\n\t" "ds_write_b16_d16_hi %3, %[r3]\n\t"
            "s_mov_b64 exec, %20\n\t" "ds_write_b16 %4, %[r4] offset:%c[io]\n\t" "ds_write_b16_d16_hi %4, %[r4]\n\t"
            "s_mov_b64 exec, %21\n\t" "ds_write_b16 %5, %[r5] offset:%c[io]\n\t" "ds_write_b16_d16_hi %5, %[r5]\n\t"
            "s_mov_b64 exec, %22\n\t" "ds_write_b16 %6, %[r6] offset:%c[io]\n\t" "ds_write_b16_d16_hi %6, %[r6]\n\t"
            "s_mov_b64 exec, %23\n\t" "ds_write_b16 %7, %[r7] offset:%c[io]\n\t" "ds_write_b16_d16_hi %7, %[r7]\n\t"
            "s_mov_b64 exec, -1\n\t"
            "s_waitcnt lgkmcnt(0)"
            : "=&v"(a0), "=&v"(a1), "=&v"(a2), "=&v"(a3), "=&v"(a4), "=&v"(a5), "=&v"(a6), "=&v"(a7),
              "=&v"(e0), "=&v"(e1), "=&v"(e2), "=&v"(e3), "=&v"(e4), "=&v"(e5), "=&v"(e6), "=&v"(e7),
              "=&s"(m0), "=&s"(m1), "=&s"(m2), "=&s"(m3), "=&s"(m4), "=&s"(m5), "=&s"(m6), "=&s"(m7), "=&s"(ov)
            : [tx] "v"(r1.x), [ty] "v"(r1.y), [fe] "s"(0xFEFEFEFEu), [nb] "s"((u32)NB), [cb] "s"((u32)L_CNT), [two] "v"(2u),
              [ls] "s"((u32)LSTB), [le] "v"((u32)(L_LE + LSTB)),
              [r0] "v"(r4a.x), [r1] "v"(r4a.y), [r2] "v"(r4a.z), [r3] "v"(r4a.w), [r4] "v"(r4b.x), [r5] "v"(r4b.y), [r6] "v"(r4b.z), [r7] "v"(r4b.w),
              [io] "i"(IO)
            : "memory");
        if (ov != 0 && lane == 0) misc[1] = 1;
    }
    WAIT_L(0); BAR();
    if (w == 15) {
        barrier_wait_flags(ws + OFF_F2, tag, lane);
    } else {
        u32 mydeg = 0, myrank = 0;
        if (tid < nvalid) {
            mydeg = (cnt[tid] - (u32)(L_LE + tid * LSTB)) >> 1;
            if (mydeg > (u32)CAPN) { misc[1] = 1; mydeg = CAPN; }
            myrank = atomicAdd(&hist[63 - mydeg], 1u);
        }
        WAIT_L(0); BAR();
        if (w < 3) {
            const u32 h0 = hist[lane];
            u32 v = h0;
            v = dpp_add<0x111, 0xf>(v); v = dpp_add<0x112, 0xf>(v); v = dpp_add<0x114, 0xf>(v); v = dpp_add<0x118, 0xf>(v);
            v = dpp_add<0x142, 0xa>(v); v = dpp_add<0x143, 0xc>(v);
            const u32 base = (u32)__builtin_amdgcn_ds_bpermute((int)((63 - mydeg) * 4), (int)(v - h0));
            if (tid < nvalid) {
                const u32 pos = base + myrank;
                const u32x2 ent = {(u32)(L_LE + tid * LSTB) | (mydeg << 24), (u32)(tid * 256) | (u32)((tid & 15) << 4)};
                u32x2* o = (u32x2*)(smem + L_ORD) + pos;
                o[0] = ent;
                if (pos == (u32)nvalid - 1) { o[1] = ent; o[2] = ent; o[3] = ent; }
            }
        }
    }
    WAIT_L(0); BAR();
    const bool slow = misc[1] != 0;
    if (b == 0 && tid == 0) __hip_atomic_store((GLB_AS u64*)(ws + OFF_G), tag, __ATOMIC_RELAXED, __HIP_MEMORY_SCOPE_AGENT);

    const char* const bvb = ws + OFF_BV + (size_t)slice * NP * 256;
    const u32 joff = j * 16;
    if (slow) {
        __builtin_amdgcn_fence(__ATOMIC_ACQUIRE, "agent");
        u32 sel = 0x00003F80u;
        asm volatile("" : "+s"(sel));
        for (int task = w; task * 4 < nvalid; task += 16) {
            const int n = task * 4 + g;
            const bool nv = n < nvalid;
            const int nl = nv ? n : 0;
            const int node = node0 + nl;
            const u32x4 au = *(const u32x4*)(smem + L_AU + nl * 256 + ((j ^ (nl & 15)) << 4));
            const u32x2 xv = *(const u32x2*)(x + (size_t)node * DD + slice * 64 + j * 4);
            const f32x2 a01 = {__uint_as_float(au.x << 16), __uint_as_float(au.y << 16)};
            const f32x2 a23 = {__uint_as_float(au.z << 16), __uint_as_float(au.w << 16)};
            Acc acc = {{0.f, 0.f}, {0.f, 0.f}, {0.f, 0.f}, {0.f, 0.f}};
            const int lo = node0 + task * 4;
            for (int e0 = 0; e0 < NE; e0 += 64) {
                const int d = dst[e0 + lane];
                unsigned long long m = __ballot((u32)(d - lo) < 4u);
                if (m) {
                    const u32 rc = (u32)src[e0 + lane] | ((u32)ea[e0 + lane] << 16);
                    while (m) {
                        const int l = __builtin_ctzll(m); m &= m - 1;
                        const int dd = __builtin_amdgcn_readlane(d, l);
                        const u32 rr = __builtin_amdgcn_readlane(rc, l);
                        const u32 rec = (nv && dd == node) ? rr : NEUTRAL_REC;
                        const u32x4 dv = *(const u32x4*)(bvb + (size_t)((rec & 0xFFFFu) * 256 + joff));
                        edge_step(acc, dv, rec, a01, a23, w01, w23, sel);
                    }
                }
            }
            if (nv) node_finish(acc, au, xv, out + (size_t)node * DD + slice * 64 + j * 4);
        }
        return;
    }

    const int ntask = (nvalid + 3) >> 2;
    if (w < ntask) {
        u32x4 srdBV, srdO;
        { const size_t p = (size_t)bvb; srdBV = (u32x4){(u32)p, (u32)(p >> 32) | (256u << 16), 0xFFFFFFFFu, 0x00020000u}; }
        { const size_t ob = (size_t)node0 * 512 + slice * 128; const size_t p = (size_t)out + ob; srdO = (u32x4){(u32)p, (u32)(p >> 32), (u32)((size_t)NN * 512 - ob), 0x00020000u}; }
        u32 g8 = L_ORD + g * 8, ctr = L_MISC, one = 1, j8 = j * 8;
        unsigned long long eps2 = 0x358637BD358637BDull;
        u32 s_cnt, s_nxt, s_t, s_tc;
        u32x4 sel4;
#pragma unroll
        for (int r = 0; r < 4; ++r) sel4[r] = ((lane & 3) == r && (j >> 2) == g) ? 0x00003F80u : 0u;
#define STAGE(TC0, TC1, TC2, TC3, TCL, TCH, TN, TNL, TNH, DL, DH, DNX, EN, IX, E1, DN, IXN, O1, O2) \
            "s_waitcnt lgkmcnt(0)\n\t" \
            "buffer_load_dwordx4 " DN ", " IXN ", %[bv], 0 idxen offen sc1\n\t" \
            "v_pk_fma_f32 " TNL ", " EN ", v[100:101], v[96:97] op_sel_hi:[0,1,1]\n\t" \
            "v_pk_fma_f32 " TNH ", " EN ", v[102:103], v[98:99] op_sel_hi:[0,1,1]\n\t" \
            "ds_read_u16 " IX ", v89 offset:" O2 "\n\t" \
            "ds_read_u16_d16_hi " E1 ", v89 offset:" O1 "\n\t" \
            "s_waitcnt vmcnt(2)\n\t" \
            "v_mfma_f32_16x16x32_bf16 " TN ", v[60:63], " DNX ", " TN "\n\t" \
            "v_exp_f32 " TC0 ", " TC0 "\n\t" \
            "v_exp_f32 " TC1 ", " TC1 "\n\t" \
            "v_exp_f32 " TC2 ", " TC2 "\n\t" \
            "v_exp_f32 " TC3 ", " TC3 "\n\t" \
            "v_pk_add_f32 " TCL ", " TCL ", 1.0 op_sel_hi:[1,0]\n\t" \
            "v_pk_add_f32 " TCH ", " TCH ", 1.0 op_sel_hi:[1,0]\n\t" \
            "v_rcp_f32 " TC0 ", " TC0 "\n\t" \
            "v_rcp_f32 " TC1 ", " TC1 "\n\t" \
            "v_rcp_f32 " TC2 ", " TC2 "\n\t" \
            "v_rcp_f32 " TC3 ", " TC3 "\n\t" \
            "s_sub_u32 %[cnt], %[cnt], 1\n\t" \
            "v_pk_add_f32 v[108:109], v[108:109], " TCL "\n\t" \
            "v_pk_fma_f32 v[104:105], " TCL ", " DL ", v[104:105]\n\t" \
            "v_pk_add_f32 v[110:111], v[110:111], " TCH "\n\t" \
            "v_pk_fma_f32 v[106:107], " TCH ", " DH ", v[106:107]\n\t" \
            "s_cmp_eq_u32 %[cnt], 0\n\t"
#define TA "v112", "v113", "v114", "v115", "v[112:113]", "v[114:115]", "v[56:59]", "v[56:57]", "v[58:59]"
#define TB "v56", "v57", "v58", "v59", "v[56:57]", "v[58:59]", "v[112:115]", "v[112:113]", "v[114:115]"
#define STAGE_(T, ...) STAGE(T, __VA_ARGS__)
#define PRIO_INIT "s_setprio 3\n\t"
#define PRIO_STEP \
            "s_add_u32 %[tc], %[tc], 1\n\t" \
            "s_cmp_lg_u32 %[tc], 1\n\t s_cbranch_scc1 L_p1%=\n\t s_setprio 2\n\t L_p1%=:\n\t" \
            "s_cmp_lg_u32 %[tc], 2\n\t s_cbranch_scc1 L_p2%=\n\t s_setprio 1\n\t L_p2%=:\n\t" \
            "s_cmp_lg_u32 %[tc], 3\n\t s_cbranch_scc1 L_p3%=\n\t s_setprio 0\n\t L_p3%=:\n\t"
        asm volatile(
            PRIO_INIT
            "v_mov_b32 v88, 0\n\t"
            "v_mov_b32 v90, 0\n\t"
            "v_mov_b32 v92, 0\n\t"
            "v_mov_b32 v94, 0\n\t"
            "s_mov_b32 %[tc], 0\n\t"
            "s_lshl_b32 %[t], %[task], 5\n\t"
            "v_add_u32 v127, %[t], v91\n\t"
            "ds_read_b64 v[122:123], v127\n\t"
            "s_mov_b64 exec, 1\n\t"
            "ds_add_rtn_u32 v125, v93, v95\n\t"
            "s_mov_b64 exec, -1\n\t"
            "L_task%=:\n\t"
            "s_waitcnt lgkmcnt(0)\n\t"
            "v_readfirstlane_b32 %[nxt], v125\n\t"
            "v_readfirstlane_b32 %[cnt], v122\n\t"
            "v_and_b32 v89, 0xffffff, v122\n\t"
            "v_and_b32 v127, 0xffffff00, v123\n\t"
            "v_xor_b32 v112, v123, v81\n\t"
            "ds_read_u16 v80, v89 offset:%c[le0]\n\t"
            "ds_read_u16 v82, v89 offset:%c[le1]\n\t"
            "ds_read_u16 v84, v89 offset:%c[le2]\n\t"
            "ds_read_u16 v86, v89 offset:%c[le3]\n\t"
            "ds_read_u16_d16_hi v88, v89\n\t"
            "ds_read_u16_d16_hi v90, v89 offset:2\n\t"
            "ds_read_u16_d16_hi v92, v89 offset:4\n\t"
            "ds_read_u16_d16_hi v94, v89 offset:6\n\t"
            "v_lshl_add_u32 v124, v127, 1, v126\n\t"
            "ds_read_b128 v[116:119], v112 offset:%c[au]\n\t"
            "global_load_dwordx2 v[120:121], v124, %[xb]\n\t"
            "s_lshr_b32 %[cnt], %[cnt], 24\n\t"
            "s_lshl_b32 %[t], %[nxt], 5\n\t"
            "v_add_u32 v127, %[t], v91\n\t"
            "ds_read_b64 v[122:123], v127\n\t"
            "s_mov_b64 exec, 1\n\t"
            "ds_add_rtn_u32 v125, v93, v95\n\t"
            "s_mov_b64 exec, -1\n\t"
            "s_waitcnt lgkmcnt(8)\n\t"
            "buffer_load_dwordx4 v[64:67], v[80:81], %[bv], 0 idxen offen sc1\n\t"
            "buffer_load_dwordx4 v[68:71], v[82:83], %[bv], 0 idxen offen sc1\n\t"
            "buffer_load_dwordx4 v[72:75], v[84:85], %[bv], 0 idxen offen sc1\n\t"
            "v_mov_b64 v[104:105], 0\n\t"
            "v_mov_b64 v[106:107], 0\n\t"
            "v_mov_b64 v[108:109], 0\n\t"
            "v_mov_b64 v[110:111], 0\n\t"
            "s_waitcnt lgkmcnt(2)\n\t"
            "v_lshlrev_b32 v96, 16, v116\n\t"
            "v_lshlrev_b32 v97, 16, v117\n\t"
            "v_lshlrev_b32 v98, 16, v118\n\t"
            "v_lshlrev_b32 v99, 16, v119\n\t"
            "s_cmp_eq_u32 %[cnt], 0\n\t"
            "s_cbranch_scc1 L_fin%=\n\t"
            "v_pk_fma_f32 v[112:113], v[88:89], v[100:101], v[96:97] op_sel_hi:[0,1,1]\n\t"
            "v_pk_fma_f32 v[114:115], v[88:89], v[102:103], v[98:99] op_sel_hi:[0,1,1]\n\t"
            "s_waitcnt vmcnt(2)\n\t"
            "s_nop 0\n\t"
            "v_mfma_f32_16x16x32_bf16 v[112:115], v[60:63], v[64:67], v[112:115]\n\t"
            "s_nop 0\n\t"
            "L_loop%=:\n\t"
            STAGE_(TA, "v[64:65]", "v[66:67]", "v[68:71]", "v[90:91]", "v80", "v88", "v[76:79]", "v[86:87]", "8", "%c[le4]")
            "s_cbranch_scc1 L_fin%=\n\t"
            STAGE_(TB, "v[68:69]", "v[70:71]", "v[72:75]", "v[92:93]", "v82", "v90", "v[64:67]", "v[80:81]", "10", "%c[le5]")
            "s_cbranch_scc1 L_fin%=\n\t"
            STAGE_(TA, "v[72:73]", "v[74:75]", "v[76:79]", "v[94:95]", "v84", "v92", "v[68:71]", "v[82:83]", "12", "%c[le6]")
            "s_cbranch_scc1 L_fin%=\n\t"
            STAGE_(TB, "v[76:77]", "v[78:79]", "v[64:67]", "v[88:89]", "v86", "v94", "v[72:75]", "v[84:85]", "14", "%c[le7]")
            "v_add_u32 v89, 8, v89\n\t"
            "s_cbranch_scc0 L_loop%=\n\t"
            "L_fin%=:\n\t"
            "s_waitcnt vmcnt(3)\n\t"
            "v_pk_add_f32 v[108:109], v[108:109], %[eps]\n\t"
            "v_pk_add_f32 v[110:111], v[110:111], %[eps]\n\t"
            "v_rcp_f32 v108, v108\n\t"
            "v_rcp_f32 v109, v109\n\t"
            "v_rcp_f32 v110, v110\n\t"
            "v_rcp_f32 v111, v111\n\t"
            "v_pk_mul_f32 v[104:105], v[104:105], v[108:109]\n\t"
            "v_pk_mul_f32 v[106:107], v[106:107], v[110:111]\n\t"
            "v_dot2c_f32_bf16 v104, %[shi], v116\n\t"
            "v_dot2c_f32_bf16 v105, %[shi], v117\n\t"
            "v_dot2c_f32_bf16 v106, %[shi], v118\n\t"
            "v_dot2c_f32_bf16 v107, %[shi], v119\n\t"
            "v_maximum3_f32 v104, v104, 0, 0\n\t"
            "v_maximum3_f32 v105, v105, 0, 0\n\t"
            "v_maximum3_f32 v106, v106, 0, 0\n\t"
            "v_maximum3_f32 v107, v107, 0, 0\n\t"
            "v_dot2c_f32_bf16 v104, %[slo], v120\n\t"
            "v_dot2c_f32_bf16 v105, %[shi], v120\n\t"
            "v_dot2c_f32_bf16 v106, %[slo], v121\n\t"
            "v_dot2c_f32_bf16 v107, %[shi], v121\n\t"
            "s_nop 1\n\t"
            "v_cvt_pk_bf16_f32 v112, v104, v105\n\t"
            "v_cvt_pk_bf16_f32 v113, v106, v107\n\t"
            "buffer_store_dwordx2 v[112:113], v124, %[o], 0 offen nt\n\t"
            PRIO_STEP
            "s_cmp_lt_u32 %[nxt], %[ntask]\n\t"
            "s_cbranch_scc1 L_task%=\n\t"
            "s_waitcnt vmcnt(0) lgkmcnt(0)\n\t"
            "gcn_pf_to:"
            : [cnt] "=&s"(s_cnt), [nxt] "=&s"(s_nxt), [t] "=&s"(s_t), [tc] "=&s"(s_tc)
            :
              "{v81}"(joff), "{v83}"(joff), "{v85}"(joff), "{v87}"(joff), "{v91}"(g8), "{v93}"(ctr), "{v95}"(one), "{v126}"(j8),
              "{v[100:101]}"(w01), "{v[102:103]}"(w23), "{v[60:63]}"(sel4),
              [task] "s"(w), [ntask] "s"(ntask), [bv] "s"(srdBV), [o] "s"(srdO), [slo] "s"(0x00003F80u), [shi] "s"(0x3F800000u), [eps] "s"(eps2),
              [xb] "s"((const char*)x + (size_t)node0 * 512 + slice * 128),
              [au] "i"(L_AU), [le0] "i"(IO), [le1] "i"(IO + 2), [le2] "i"(IO + 4), [le3] "i"(IO + 6),
              [le4] "i"(IO + 8), [le5] "i"(IO + 10), [le6] "i"(IO + 12), [le7] "i"(IO + 14)
            : "v56", "v57", "v58", "v59", "v64", "v65", "v66", "v67", "v68", "v69", "v70", "v71", "v72", "v73", "v74", "v75", "v76", "v77", "v78", "v79",
              "v80", "v82", "v84", "v86", "v88", "v89", "v90", "v92", "v94", "v96", "v97", "v98", "v99",
              "v104", "v105", "v106", "v107", "v108", "v109", "v110", "v111", "v112", "v113", "v114", "v115",
              "v116", "v117", "v118", "v119", "v120", "v121", "v122", "v123", "v124", "v125", "v127", "scc", "memory");
#undef STAGE
#undef STAGE_
#undef TA
#undef TB
    }
}

extern "C" void kernel_launch(void* const* d_in, const int* in_sizes, int n_in,
                              void* d_out, int out_size, void* d_ws, size_t ws_size,
                              hipStream_t stream) {
    const u16* x = (const u16*)d_in[0];
    const u16* ea = (const u16*)d_in[1];
    const int* src = (const int*)d_in[2];
    const int* dst = (const int*)d_in[3];
    const u16* wa = (const u16*)d_in[4];
    const u16* wb = (const u16*)d_in[5];
    const u16* wc = (const u16*)d_in[6];
    const u16* wu = (const u16*)d_in[7];
    const u16* wv = (const u16*)d_in[8];
    (void)hipFuncSetAttribute((const void*)gatedgcn_fused, hipFuncAttributeMaxDynamicSharedMemorySize, L_END);
    gatedgcn_fused<<<256, 1024, L_END, stream>>>(x, ea, src, dst, wa, wb, wc, wu, wv, (char*)d_ws, (u16*)d_out);
}
```
